```python
import torch

def _softmax(x, axis=-1):
    return torch.softmax(x, dim=axis)
def _gelu(x):
    return torch.nn.functional.gelu(x, approximate="tanh")
def _silu(x):
    return torch.nn.functional.silu(x)
def _sigmoid(x):
    return torch.sigmoid(x)
def _layernorm(x, axis=-1, eps=1e-5):
    m = x.mean(dim=axis, keepdim=True)
    v = x.var(dim=axis, keepdim=True, unbiased=False)
    return (x - m) / torch.sqrt(v + eps)
def _scatter_add(tgt, idx, src, axis=0):
    return tgt.clone().index_add_(axis, idx, src)
def _segment_sum(src, seg, n):
    out = torch.zeros((n,) + src.shape[1:], dtype=src.dtype)
    return out.index_add_(0, seg, src)
def _pad(x, pw):
    flat = tuple(v for pair in reversed(pw) for v in pair)
    return torch.nn.functional.pad(x, flat)

kernel_name = 'interleave_layers__sliding_block__glu_gated_swiglu__prompt'

def setup_inputs():

    _w = (lambda a: (lambda t: t.to(torch.bfloat16) if t.is_floating_point() else t)(torch.as_tensor(a)))
    def _A_setup():
        B, S, D = 1, 2048, 640
        return {
            "x": _w((lambda *s: torch.randn(*s, dtype=torch.bfloat16) * 0.05)(B, S, D)),
            "wqkv": _w((lambda *s: torch.randn(*s, dtype=torch.bfloat16) * 0.05)(D, 3 * D)),
            "wo": _w((lambda *s: torch.randn(*s, dtype=torch.bfloat16) * 0.05)(D, D)),
        }
    _A_in = _A_setup()

    def _B_setup():
        B, S, D, F = 1, 2048, 640, 1728
        return {
            "x": _w((lambda *s: torch.randn(*s, dtype=torch.bfloat16) * 0.05)(B, S, D)),
            "w1": _w((lambda *s: torch.randn(*s, dtype=torch.bfloat16) * 0.8)(D, F) * (2.0 / D) ** 0.5),
            "w3": _w((lambda *s: torch.randn(*s, dtype=torch.bfloat16) * 0.8)(D, F) * (2.0 / D) ** 0.5),
            "w2": _w((lambda *s: torch.randn(*s, dtype=torch.bfloat16) * 0.8)(F, D) * (2.0 / F) ** 0.5),
        }
    _B_in = _B_setup()

    return {
        "A_x": _A_in["x"],
        "A_wqkv": _A_in["wqkv"],
        "A_wo": _A_in["wo"],
        "B_x": _B_in["x"],
        "B_w1": _B_in["w1"],
        "B_w3": _B_in["w3"],
        "B_w2": _B_in["w2"],
    }

def reference(A_x, A_wqkv, A_wo, B_x, B_w1, B_w3, B_w2):
    def _A_ref(x, wqkv, wo):
        B, S, D = x.shape
        H = 32; Dh = D // H; W = 256
        qkv = (lambda x, *d: x.permute(*d))((x @ wqkv).reshape(B, S, 3, H, Dh), 2, 0, 3, 1, 4)
        q, k, v = qkv[0], qkv[1], qkv[2]
        scores = torch.einsum("bhqd,bhkd->bhqk", q, k) * (Dh ** -0.5)
        idx = torch.arange(S)
        mask = (idx[:, None] - idx[None, :] >= 0) & (idx[:, None] - idx[None, :] < W)
        scores = torch.where(mask[None, None], scores, -1e9)
        p = _softmax(scores, axis=-1)
        o = torch.einsum("bhqk,bhkd->bhqd", p, v)
        return (lambda x, *d: x.permute(*d))(o, 0, 2, 1, 3).reshape(B, S, D) @ wo

    def _B_ref(x, w1, w3, w2):
        gate = torch.einsum("bsd,df->bsf", x, w1)
        up = torch.einsum("bsd,df->bsf", x, w3)
        h = _silu(gate) * up
        out = torch.einsum("bsf,fd->bsd", h, w2)
        return out

    x = A_x
    x = x + _A_ref(_layernorm(x, axis=-1), A_wqkv, A_wo)
    x = x + _B_ref(_layernorm(x, axis=-1), B_w1, B_w3, B_w2)
    x = x + _A_ref(_layernorm(x, axis=-1), A_wqkv, A_wo)
    x = x + _B_ref(_layernorm(x, axis=-1), B_w1, B_w3, B_w2)
    return x
```

```cpp
#include <hip/hip_runtime.h>
#include <hip/hip_bf16.h>
#include <stdint.h>
typedef unsigned short u16;
typedef short bf16x8 __attribute__((ext_vector_type(8)));
typedef float f32x4 __attribute__((ext_vector_type(4)));
constexpr int S=2048, D=640, H=32, DH=20, W=256, F=1728;
__device__ __forceinline__ float b2f(u16 v){ return __uint_as_float(((unsigned)v)<<16); }
__device__ __forceinline__ u16 f2b(float f){ unsigned u=__float_as_uint(f); u += 0x7fffu + ((u>>16)&1u); return (u16)(u>>16); }

__global__ void ln_k(const u16* x, u16* y){
  int row=blockIdx.x; int t=threadIdx.x;
  float v[10]; float s=0;
  for(int i=0;i<10;i++){ v[i]=b2f(x[row*D+t+64*i]); s+=v[i]; }
  for(int m=32;m>=1;m>>=1) s+=__shfl_xor(s,m);
  float mean=s/D; float q=0;
  for(int i=0;i<10;i++){ float d=v[i]-mean; q+=d*d; }
  for(int m=32;m>=1;m>>=1) q+=__shfl_xor(q,m);
  float r=rsqrtf(q/D+1e-5f);
  for(int i=0;i<10;i++) y[row*D+t+64*i]=f2b((v[i]-mean)*r);
}
template<int MODE>
__global__ __launch_bounds__(256) void gemm_k(const u16* A,const u16* B,const u16* res,u16* C,int M,int N,int K){
  __shared__ u16 As[64][40]; __shared__ u16 Bs[64][40];
  int bm=blockIdx.y*64,bn=blockIdx.x*64; int t=threadIdx.x,w=t>>6,l=t&63; int wr=w>>1,wc=w&1;
  f32x4 acc[2][2]={};
  for(int k0=0;k0<K;k0+=32){
    { int r=t>>2,c=(t&3)*8; *(uint4*)&As[r][c]=*(const uint4*)&A[(size_t)(bm+r)*K+k0+c]; }
    { int kk=t>>3,c=(t&7)*8; for(int j=0;j<8;j++){ int n=bn+c+j; Bs[c+j][kk]= n<N? B[(size_t)(k0+kk)*N+n]:(u16)0; } }
    __syncthreads();
    bf16x8 a[2],b[2];
    for(int i=0;i<2;i++){ a[i]=*(bf16x8*)&As[wr*32+i*16+(l&15)][(l>>4)*8]; b[i]=*(bf16x8*)&Bs[wc*32+i*16+(l&15)][(l>>4)*8]; }
    for(int i=0;i<2;i++)for(int j=0;j<2;j++) acc[i][j]=__builtin_amdgcn_mfma_f32_16x16x32_bf16(a[i],b[j],acc[i][j],0,0,0);
    __syncthreads();
  }
  for(int i=0;i<2;i++)for(int j=0;j<2;j++)for(int r=0;r<4;r++){
    int row=bm+wr*32+i*16+(l>>4)*4+r, col=bn+wc*32+j*16+(l&15);
    if(col<N){ float v=acc[i][j][r]; if(MODE==1) v=b2f(res[(size_t)row*N+col])+b2f(f2b(v)); C[(size_t)row*N+col]=f2b(v);} }
}
__global__ void attn_k(const u16* qkv,u16* o){
  int h=blockIdx.y; int q=blockIdx.x*64+threadIdx.x;
  float qv[DH]; for(int d=0;d<DH;d++) qv[d]=b2f(qkv[(size_t)q*3*D+h*DH+d])*0.22360679774997896f;
  float m=-1e30f,l=0,acc[DH]; for(int d=0;d<DH;d++)acc[d]=0;
  int k0=q-W+1; if(k0<0)k0=0;
  for(int k=k0;k<=q;k++){ float s=0; for(int d=0;d<DH;d++) s+=qv[d]*b2f(qkv[(size_t)k*3*D+D+h*DH+d]);
    float mn=fmaxf(m,s); float a=__expf(m-mn),p=__expf(s-mn); l=l*a+p; for(int d=0;d<DH;d++) acc[d]=acc[d]*a+p*b2f(qkv[(size_t)k*3*D+2*D+h*DH+d]); m=mn; }
  for(int d=0;d<DH;d++) o[(size_t)q*D+h*DH+d]=f2b(acc[d]/l);
}
__global__ void swiglu_k(const u16* g,const u16* u,u16* h,int n){ int i=blockIdx.x*256+threadIdx.x; if(i<n){ float a=b2f(g[i]); h[i]=f2b(a/(1.f+__expf(-a))*b2f(u[i])); } }

extern "C" void kernel_launch(void* const* d_in, const int* in_sizes, int n_in, void* d_out, int out_size, void* d_ws, size_t ws_size, hipStream_t stream){
  const u16* x=(const u16*)d_in[0]; const u16* wqkv=(const u16*)d_in[1]; const u16* wo=(const u16*)d_in[2];
  const u16* w1=(const u16*)d_in[4]; const u16* w3=(const u16*)d_in[5]; const u16* w2=(const u16*)d_in[6];
  u16* ws=(u16*)d_ws; u16* xn=ws; u16* qkv=xn+S*D; u16* o=qkv+S*3*D; u16* xa=o+S*D; u16* xb=xa+S*D; u16* g=xb+S*D; u16* u=g+S*F; u16* h=u+S*F;
  const u16* cur=x; u16* out=(u16*)d_out;
  for(int L=0;L<2;L++){
    ln_k<<<S,64,0,stream>>>(cur,xn);
    gemm_k<0><<<dim3(30,32),256,0,stream>>>(xn,wqkv,nullptr,qkv,S,3*D,D);
    attn_k<<<dim3(S/64,H),64,0,stream>>>(qkv,o);
    gemm_k<1><<<dim3(10,32),256,0,stream>>>(o,wo,cur,xa,S,D,D);
    ln_k<<<S,64,0,stream>>>(xa,xn);
    gemm_k<0><<<dim3(27,32),256,0,stream>>>(xn,w1,nullptr,g,S,F,D);
    gemm_k<0><<<dim3(27,32),256,0,stream>>>(xn,w3,nullptr,u,S,F,D);
    swiglu_k<<<(S*F+255)/256,256,0,stream>>>(g,u,h,S*F);
    u16* dst = L==1? out: xb;
    gemm_k<1><<<dim3(10,32),256,0,stream>>>(h,w2,xa,dst,S,D,F);
    cur=dst;
  }
}
```

```cpp
#include <hip/hip_runtime.h>
#include <hip/hip_bf16.h>
#include <stdint.h>

__global__ __launch_bounds__(256,1) void mk_main(char* ws, char* out, const char* x, const char* wqkv, const char* wo, const char* w1, const char* w3, const char* w2){
  unsigned tid = threadIdx.x;
  asm volatile(
    "s_mov_b32 s11, %2\n"
    "s_mov_b64 s[98:99], %1\n"
    "s_load_dwordx16 s[68:83], s[98:99], 0\n"
    "s_waitcnt lgkmcnt(0)\n"
    "s_mov_b64 s[12:13], s[68:69]\n"
    "s_mov_b64 s[14:15], s[70:71]\n"
    "s_mov_b32 s16, s12\n"
    "s_and_b32 s17, s13, 0xffff\n"
    "s_mov_b32 s18, 0x10000000\n"
    "s_mov_b32 s19, 0x00020000\n"
    "s_mov_b32 s20, s14\n"
    "s_and_b32 s21, s15, 0xffff\n"
    "s_mov_b32 s22, 2621440\n"
    "s_mov_b32 s23, 0x00020000\n"
    "s_and_b32 s30, s11, 7\n"
    "s_lshr_b32 s31, s11, 3\n"
    "s_lshr_b32 s35, s30, 1\n"
    "s_lshl_b32 s35, s35, 2\n"
    "s_lshr_b32 s36, s31, 3\n"
    "s_add_u32 s24, s35, s36\n"
    "s_and_b32 s35, s30, 1\n"
    "s_lshl_b32 s35, s35, 3\n"
    "s_and_b32 s36, s31, 7\n"
    "s_add_u32 s25, s35, s36\n"
    "v_lshrrev_b32 v6, 6, v0\n"
    "s_nop 0\n"
    "v_readfirstlane_b32 s26, v6\n"
    "v_and_b32 v6, 63, v0\n"
    "v_lshlrev_b32 v1, 4, v6\n"
    "v_add_u32 v2, 0x10000, v1\n"
    "v_add_u32 v3, 0x20000, v1\n"
    "v_and_b32 v4, 31, v6\n"
    "v_lshrrev_b32 v5, 5, v6\n"
    "s_lshl_b32 s27, s24, 2\n"
    "s_add_u32 s27, s27, s26\n"
    "s_lshl_b32 s29, s26, 12\n"
    "s_lshl_b32 s93, s26, 10\n"
    "s_mul_i32 s53, s25, 2560\n"
    "v_lshlrev_b32 v7, 11, v5\n"
    "v_lshl_add_u32 v7, v4, 3, v7\n"
    "v_lshlrev_b32 v8, 6, v5\n"
    "v_add_u32 v10, 159744, v8\n"
    "v_add_u32 v11, 160768, v8\n"
    "v_cmp_eq_u32 s[48:49], 1, v5\n"
    "v_mov_b32 v208, 0x7ffffff0\n"
    "s_nop 0\n"
    "v_cndmask_b32 v9, v1, v208, s[48:49]\n"
    "v_mov_b32 v219, 0xff800000\n"
    "v_mov_b32 v222, 0x3f803f80\n"
    "s_mul_i32 s51, s26, 6144\n"
    "s_add_u32 s51, s51, 86016\n"
    "v_mul_u32_u24 v208, 48, v4\n"
    "v_add_u32 v208, s51, v208\n"
    "v_lshl_add_u32 v14, v5, 4, v208\n"
    "v_add_u32 v15, 32, v208\n"
    "v_add_u32 v12, 49152, v14\n"
    "v_mul_u32_u24 v209, 3072, v5\n"
    "v_add3_u32 v13, v209, v15, 0\n"
    "v_add_u32 v13, 49152, v13\n"
    "v_bfe_u32 v209, v6, 2, 2\n"
    "v_lshl_add_u32 v209, v5, 2, v209\n"
    "v_mul_u32_u24 v209, 48, v209\n"
    "v_add_u32 v209, s51, v209\n"
    "v_bfe_u32 v210, v6, 4, 1\n"
    "v_and_b32 v211, 3, v6\n"
    "v_lshl_add_u32 v210, v210, 2, v211\n"
    "v_lshlrev_b32 v211, 2, v210\n"
    "v_mov_b32 v212, 0x55543120\n"
    "v_lshrrev_b32 v212, v211, v212\n"
    "v_and_b32 v212, 15, v212\n"
    "v_lshl_add_u32 v216, v212, 3, v209\n"
    "v_lshlrev_b32 v211, 2, v210\n"
    "v_mov_b32 v212, 0x55542013\n"
    "v_lshrrev_b32 v212, v211, v212\n"
    "v_and_b32 v212, 15, v212\n"
    "v_lshl_add_u32 v217, v212, 3, v209\n"
    "v_lshrrev_b32 v210, 4, v6\n"
    "v_and_b32 v209, 7, v6\n"
    "v_lshlrev_b32 v209, 4, v209\n"
    "v_lshl_add_u32 v209, v210, 7, v209\n"
    "v_add_u32 v218, 2048, v209\n"
    "v_and_b32 v209, 15, v6\n"
    "v_lshlrev_b32 v209, 4, v209\n"
    "v_lshl_add_u32 v0, v210, 9, v209\n"
    "v_and_b32 v210, 1, v210\n"
    "v_cmp_eq_u32 s[8:9], 1, v210\n"
    "s_mov_b32 s95, 0\n"
    "s_mov_b32 s28, 0\n"
    "v_mov_b32 v208, 54218752\n"
    "buffer_load_dwordx2 v[236:237], v208, s[16:19], 0 offen sc1\n"
    "s_mov_b64 s[40:41], s[72:73]\n"
    "s_and_b32 s41, s41, 0xffff\n"
    "s_mov_b32 s42, 2621440\n"
    "s_mov_b32 s43, 0x00020000\n"
    "v_mul_u32_u24 v78, 1280, v4\n"
    "v_lshl_add_u32 v78, v5, 4, v78\n"
    "s_mul_i32 s31, s27, 40960\n"
    "s_mul_i32 s35, s25, 80\n"
    "s_add_u32 s31, s31, s35\n"
    "buffer_load_dwordx4 v[240:243], v78, s[40:43], s31 offen\n"
    "buffer_load_dwordx4 v[244:247], v78, s[40:43], s31 offen offset:32\n"
    "v_mov_b32 v248, 0\n"
    "v_mov_b32 v249, 0\n"
    "v_mov_b32 v250, 0\n"
    "v_mov_b32 v251, 0\n"
    "s_mov_b64 exec, 0xffffffff\n"
    "buffer_load_dwordx4 v[248:251], v78, s[40:43], s31 offen offset:64\n"
    "s_mov_b64 exec, -1\n"
    "s_mov_b64 s[40:41], s[74:75]\n"
    "s_and_b32 s41, s41, 0xffff\n"
    "s_mov_b32 s42, 2457600\n"
    "s_mov_b32 s43, 0x00020000\n"
    "s_and_b32 s63, s24, 3\n"
    "s_lshr_b32 s62, s24, 2\n"
    "v_and_b32 v76, 7, v6\n"
    "v_lshrrev_b32 v77, 3, v6\n"
    "v_lshrrev_b32 v80, 2, v76\n"
    "v_mul_u32_u24 v80, 20, v80\n"
    "v_and_b32 v81, 1, v76\n"
    "v_lshl_add_u32 v80, v81, 3, v80\n"
    "v_bfe_u32 v82, v76, 1, 1\n"
    "v_lshl_add_u32 v80, v82, 2, v80\n"
    "s_add_u32 s31, s63, 1\n"
    "s_mul_i32 s31, s31, 640\n"
    "s_cmp_eq_u32 s63, 3\n"
    "s_cselect_b32 s31, 0, s31\n"
    "v_add_u32 v80, s31, v80\n"
    "v_lshrrev_b32 v83, 1, v76\n"
    "v_add_u32 v84, 1, v83\n"
    "v_mul_u32_u24 v84, 640, v84\n"
    "v_mad_u32_u24 v84, v81, 20, v84\n"
    "v_subrev_u32 v85, 2, v83\n"
    "v_mul_u32_u24 v85, 20, v85\n"
    "v_cmp_gt_u32 vcc, 2, v83\n"
    "s_nop 1\n"
    "v_cndmask_b32 v84, v85, v84, vcc\n"
    "v_add_u32 v84, 16, v84\n"
    "s_cmp_eq_u32 s63, 2\n"
    "s_cselect_b64 s[90:91], -1, 0\n"
    "v_cndmask_b32 v80, v80, v84, s[90:91]\n"
    "s_mul_i32 s31, s25, 40\n"
    "v_add_u32 v80, s31, v80\n"
    "v_lshlrev_b32 v80, 1, v80\n"
    "v_mul_u32_u24 v86, 3840, v77\n"
    "v_add_u32 v70, v86, v80\n"
    "v_cmp_le_u32 vcc, 2, v83\n"
    "v_cmp_eq_u32 s[66:67], 1, v81\n"
    "s_nop 1\n"
    "s_and_b64 vcc, vcc, s[66:67]\n"
    "s_and_b64 vcc, vcc, s[90:91]\n"
    "v_mov_b32 v86, 0x7ffffff0\n"
    "s_nop 0\n"
    "v_cndmask_b32 v70, v70, v86, vcc\n"
    "s_mul_i32 s61, s62, 160\n"
    "s_mul_i32 s31, s26, 40\n"
    "s_add_u32 s61, s61, s31\n"
    "s_mul_i32 s61, s61, 3840\n"
    "buffer_load_dwordx2 v[16:17], v70, s[40:43], s61 offen\n"
    "s_add_u32 s61, s61, 30720\n"
    "buffer_load_dwordx2 v[18:19], v70, s[40:43], s61 offen\n"
    "s_add_u32 s61, s61, 30720\n"
    "buffer_load_dwordx2 v[20:21], v70, s[40:43], s61 offen\n"
    "s_add_u32 s61, s61, 30720\n"
    "buffer_load_dwordx2 v[22:23], v70, s[40:43], s61 offen\n"
    "s_add_u32 s61, s61, 30720\n"
    "buffer_load_dwordx2 v[24:25], v70, s[40:43], s61 offen\n"
    "s_add_u32 s61, s61, 30720\n"
    "s_mov_b32 s64, s11\n"
    "s_cmp_lt_u32 s64, 108\n"
    "s_cbranch_scc1 LldF_1_%=\n"
    "s_cmp_lt_u32 s64, 182\n"
    "s_cbranch_scc1 LldO_2_%=\n"
    "s_branch LldN_3_%=\n"
    "LldF_1_%=:\n"
    "s_mov_b64 s[40:41], s[78:79]\n"
    "s_and_b32 s41, s41, 0xffff\n"
    "s_mov_b32 s42, 2211840\n"
    "s_mov_b32 s43, 0x00020000\n"
    "s_mov_b64 s[44:45], s[80:81]\n"
    "s_and_b32 s45, s45, 0xffff\n"
    "s_mov_b32 s46, 2211840\n"
    "s_mov_b32 s47, 0x00020000\n"
    "s_lshr_b32 s65, s64, 2\n"
    "s_and_b32 s59, s64, 3\n"
    "s_mul_i32 s61, s59, 160\n"
    "s_mul_i32 s31, s26, 40\n"
    "s_add_u32 s61, s61, s31\n"
    "s_mul_i32 s61, s61, 3456\n"
    "s_lshl_b32 s31, s65, 7\n"
    "s_add_u32 s61, s61, s31\n"
    "v_mul_u32_u24 v80, 3456, v77\n"
    "v_lshl_add_u32 v71, v76, 4, v80\n"
    "buffer_load_dwordx4 v[100:103], v71, s[40:43], s61 offen\n"
    "buffer_load_dwordx4 v[104:107], v71, s[44:47], s61 offen\n"
    "s_add_u32 s61, s61, 27648\n"
    "buffer_load_dwordx4 v[108:111], v71, s[40:43], s61 offen\n"
    "buffer_load_dwordx4 v[112:115], v71, s[44:47], s61 offen\n"
    "s_add_u32 s61, s61, 27648\n"
    "buffer_load_dwordx4 v[116:119], v71, s[40:43], s61 offen\n"
    "buffer_load_dwordx4 v[120:123], v71, s[44:47], s61 offen\n"
    "s_add_u32 s61, s61, 27648\n"
    "buffer_load_dwordx4 v[124:127], v71, s[40:43], s61 offen\n"
    "buffer_load_dwordx4 v[128:131], v71, s[44:47], s61 offen\n"
    "s_add_u32 s61, s61, 27648\n"
    "buffer_load_dwordx4 v[132:135], v71, s[40:43], s61 offen\n"
    "buffer_load_dwordx4 v[136:139], v71, s[44:47], s61 offen\n"
    "s_add_u32 s61, s61, 27648\n"
    "s_branch LldN_3_%=\n"
    "LldO_2_%=:\n"
    "s_cmp_lt_u32 s64, 128\n"
    "s_cbranch_scc0 Low2_4_%=\n"
    "s_mov_b64 s[40:41], s[76:77]\n"
    "s_mov_b32 s42, 819200\n"
    "s_sub_u32 s65, s64, 108\n"
    "s_lshl_b32 s68, s65, 5\n"
    "s_add_u32 s69, s68, 16\n"
    "s_branch Logo_5_%=\n"
    "Low2_4_%=:\n"
    "s_mov_b64 s[40:41], s[82:83]\n"
    "s_mov_b32 s42, 2211840\n"
    "s_sub_u32 s65, s64, 128\n"
    "s_lshl_b32 s61, s65, 1\n"
    "s_add_u32 s61, s61, 0\n"
    "s_and_b32 s66, s61, 31\n"
    "s_lshr_b32 s66, s66, 1\n"
    "s_and_b32 s67, s61, 1\n"
    "s_lshr_b32 s35, s61, 5\n"
    "s_lshl_b32 s35, s35, 1\n"
    "s_add_u32 s67, s67, s35\n"
    "s_sub_u32 s31, s61, 64\n"
    "s_mul_i32 s35, s31, 43691\n"
    "s_lshr_b32 s35, s35, 17\n"
    "s_mul_i32 s59, s35, 3\n"
    "s_sub_u32 s59, s31, s59\n"
    "s_add_u32 s59, s59, 4\n"
    "s_cmp_ge_u32 s61, 64\n"
    "s_cselect_b32 s66, s35, s66\n"
    "s_cselect_b32 s67, s59, s67\n"
    "s_sub_u32 s31, s61, 100\n"
    "s_lshr_b32 s35, s31, 1\n"
    "s_add_u32 s35, s35, 12\n"
    "s_and_b32 s59, s31, 1\n"
    "s_add_u32 s59, s59, 4\n"
    "s_cmp_ge_u32 s61, 100\n"
    "s_cselect_b32 s66, s35, s66\n"
    "s_cselect_b32 s67, s59, s67\n"
    "s_mul_i32 s35, s66, 7\n"
    "s_mul_i32 s59, s66, 6\n"
    "s_add_u32 s59, s59, 12\n"
    "s_cmp_lt_u32 s66, 12\n"
    "s_cselect_b32 s35, s35, s59\n"
    "s_add_u32 s35, s35, s67\n"
    "s_lshl_b32 s68, s35, 4\n"
    "s_lshl_b32 s61, s65, 1\n"
    "s_add_u32 s61, s61, 1\n"
    "s_and_b32 s66, s61, 31\n"
    "s_lshr_b32 s66, s66, 1\n"
    "s_and_b32 s67, s61, 1\n"
    "s_lshr_b32 s35, s61, 5\n"
    "s_lshl_b32 s35, s35, 1\n"
    "s_add_u32 s67, s67, s35\n"
    "s_sub_u32 s31, s61, 64\n"
    "s_mul_i32 s35, s31, 43691\n"
    "s_lshr_b32 s35, s35, 17\n"
    "s_mul_i32 s59, s35, 3\n"
    "s_sub_u32 s59, s31, s59\n"
    "s_add_u32 s59, s59, 4\n"
    "s_cmp_ge_u32 s61, 64\n"
    "s_cselect_b32 s66, s35, s66\n"
    "s_cselect_b32 s67, s59, s67\n"
    "s_sub_u32 s31, s61, 100\n"
    "s_lshr_b32 s35, s31, 1\n"
    "s_add_u32 s35, s35, 12\n"
    "s_and_b32 s59, s31, 1\n"
    "s_add_u32 s59, s59, 4\n"
    "s_cmp_ge_u32 s61, 100\n"
    "s_cselect_b32 s66, s35, s66\n"
    "s_cselect_b32 s67, s59, s67\n"
    "s_mul_i32 s35, s66, 7\n"
    "s_mul_i32 s59, s66, 6\n"
    "s_add_u32 s59, s59, 12\n"
    "s_cmp_lt_u32 s66, 12\n"
    "s_cselect_b32 s35, s35, s59\n"
    "s_add_u32 s35, s35, s67\n"
    "s_lshl_b32 s69, s35, 4\n"
    "Logo_5_%=:\n"
    "s_and_b32 s41, s41, 0xffff\n"
    "s_mov_b32 s43, 0x00020000\n"
    "s_cmp_lt_u32 s26, 2\n"
    "s_cselect_b32 s61, s68, s69\n"
    "s_and_b32 s31, s26, 1\n"
    "s_lshl_b32 s31, s31, 3\n"
    "s_add_u32 s61, s61, s31\n"
    "s_mul_i32 s61, s61, 1280\n"
    "buffer_load_dwordx4 v[100:103], v1, s[40:43], s61 offen\n"
    "s_add_u32 s61, s61, 1024\n"
    "buffer_load_dwordx4 v[104:107], v1, s[40:43], s61 offen\n"
    "s_add_u32 s61, s61, 1024\n"
    "buffer_load_dwordx4 v[108:111], v1, s[40:43], s61 offen\n"
    "s_add_u32 s61, s61, 1024\n"
    "buffer_load_dwordx4 v[112:115], v1, s[40:43], s61 offen\n"
    "s_add_u32 s61, s61, 1024\n"
    "buffer_load_dwordx4 v[116:119], v1, s[40:43], s61 offen\n"
    "s_add_u32 s61, s61, 1024\n"
    "buffer_load_dwordx4 v[120:123], v1, s[40:43], s61 offen\n"
    "s_add_u32 s61, s61, 1024\n"
    "buffer_load_dwordx4 v[124:127], v1, s[40:43], s61 offen\n"
    "s_add_u32 s61, s61, 1024\n"
    "buffer_load_dwordx4 v[128:131], v1, s[40:43], s61 offen\n"
    "s_add_u32 s61, s61, 1024\n"
    "buffer_load_dwordx4 v[132:135], v1, s[40:43], s61 offen\n"
    "s_add_u32 s61, s61, 1024\n"
    "buffer_load_dwordx4 v[136:139], v1, s[40:43], s61 offen\n"
    "s_add_u32 s61, s61, 1024\n"
    "LldN_3_%=:\n"
    "v_lshlrev_b32 v80, 6, v77\n"
    "v_lshl_add_u32 v80, v76, 3, v80\n"
    "s_mul_i32 s31, s26, 2560\n"
    "s_add_u32 s31, s31, 49152\n"
    "v_add_u32 v72, s31, v80\n"
    "s_cmp_lt_u32 s64, 182\n"
    "s_cbranch_scc1 Lq1_6_%=\n"
    "s_waitcnt vmcnt(0)\n"
    "s_branch Lq2_7_%=\n"
    "Lq1_6_%=:\n"
    "s_waitcnt vmcnt(10)\n"
    "Lq2_7_%=:\n"
    "ds_write_b64 v72, v[16:17] offset:0\n"
    "ds_write_b64 v72, v[18:19] offset:512\n"
    "ds_write_b64 v72, v[20:21] offset:1024\n"
    "ds_write_b64 v72, v[22:23] offset:1536\n"
    "ds_write_b64 v72, v[24:25] offset:2048\n"
    "v_readfirstlane_b32 s47, v236\n"
    "v_readfirstlane_b32 s97, v237\n"
    "s_add_u32 s47, s47, 1\n"
    "s_mov_b64 s[84:85], s[40:41]\n"
    "s_mov_b64 s[86:87], s[42:43]\n"
    "s_mov_b32 s40, 0\n"
    "s_mov_b32 s41, 0\n"
    "s_mul_i32 s39, s27, 40960\n"
    "s_add_u32 s39, s39, s53\n"
    "s_add_u32 s39, s39, s40\n"
    "s_add_u32 s39, s39, 10637312\n"
    "buffer_store_dwordx4 v[240:243], v1, s[16:19], s39 offen sc1\n"
    "buffer_store_dwordx4 v[244:247], v1, s[16:19], s39 offen offset:1024 sc1\n"
    "buffer_store_dwordx4 v[248:251], v9, s[16:19], s39 offen offset:2048 sc1\n"
    "v_mov_b32 v64, v240\n"
    "v_mov_b32 v65, v241\n"
    "v_mov_b32 v66, v242\n"
    "v_mov_b32 v67, v243\n"
    "v_mov_b32 v68, v244\n"
    "v_mov_b32 v69, v245\n"
    "v_mov_b32 v70, v246\n"
    "v_mov_b32 v71, v247\n"
    "v_mov_b32 v72, v248\n"
    "v_mov_b32 v73, v249\n"
    "v_mov_b32 v74, v250\n"
    "v_mov_b32 v75, v251\n"
    "v_permlane16_swap_b32 v64, v66\n"
    "v_permlane16_swap_b32 v65, v67\n"
    "v_permlane16_swap_b32 v68, v70\n"
    "v_permlane16_swap_b32 v69, v71\n"
    "v_permlane16_swap_b32 v72, v74\n"
    "v_permlane16_swap_b32 v73, v75\n"
    "v_dot2_f32_bf16 v48, v64, v222, 0\n"
    "v_dot2_f32_bf16 v49, v64, v64, 0\n"
    "v_dot2_f32_bf16 v48, v65, v222, v48\n"
    "v_dot2_f32_bf16 v49, v65, v65, v49\n"
    "v_dot2_f32_bf16 v48, v68, v222, v48\n"
    "v_dot2_f32_bf16 v49, v68, v68, v49\n"
    "v_dot2_f32_bf16 v48, v69, v222, v48\n"
    "v_dot2_f32_bf16 v49, v69, v69, v49\n"
    "v_dot2_f32_bf16 v48, v72, v222, v48\n"
    "v_dot2_f32_bf16 v49, v72, v72, v49\n"
    "v_dot2_f32_bf16 v48, v73, v222, v48\n"
    "v_dot2_f32_bf16 v49, v73, v73, v49\n"
    "v_dot2_f32_bf16 v50, v66, v222, 0\n"
    "v_dot2_f32_bf16 v51, v66, v66, 0\n"
    "v_dot2_f32_bf16 v50, v67, v222, v50\n"
    "v_dot2_f32_bf16 v51, v67, v67, v51\n"
    "v_dot2_f32_bf16 v50, v70, v222, v50\n"
    "v_dot2_f32_bf16 v51, v70, v70, v51\n"
    "v_dot2_f32_bf16 v50, v71, v222, v50\n"
    "v_dot2_f32_bf16 v51, v71, v71, v51\n"
    "v_dot2_f32_bf16 v50, v74, v222, v50\n"
    "v_dot2_f32_bf16 v51, v74, v74, v51\n"
    "v_dot2_f32_bf16 v50, v75, v222, v50\n"
    "v_dot2_f32_bf16 v51, v75, v75, v51\n"
    "v_mov_b32 v52, v48\n"
    "v_mov_b32 v53, v49\n"
    "v_mov_b32 v54, v50\n"
    "v_mov_b32 v55, v51\n"
    "v_permlane32_swap_b32 v48, v52\n"
    "v_permlane32_swap_b32 v49, v53\n"
    "v_permlane32_swap_b32 v50, v54\n"
    "v_permlane32_swap_b32 v51, v55\n"
    "v_add_f32 v48, v48, v52\n"
    "v_add_f32 v49, v49, v53\n"
    "v_add_f32 v50, v50, v54\n"
    "v_add_f32 v51, v51, v55\n"
    "v_mov_b32 v52, v48\n"
    "v_mov_b32 v53, v49\n"
    "v_mov_b32 v54, v50\n"
    "v_mov_b32 v55, v51\n"
    "v_permlane16_swap_b32 v48, v52\n"
    "v_permlane16_swap_b32 v49, v53\n"
    "v_permlane16_swap_b32 v50, v54\n"
    "v_permlane16_swap_b32 v51, v55\n"
    "v_add_f32 v48, v48, v52\n"
    "v_add_f32 v49, v49, v53\n"
    "v_add_f32 v50, v50, v54\n"
    "v_add_f32 v51, v51, v55\n"
    "v_cndmask_b32 v48, v48, v50, s[8:9]\n"
    "v_cndmask_b32 v49, v49, v51, s[8:9]\n"
    "s_lshl_b32 s31, s27, 4\n"
    "s_add_u32 s31, s31, s25\n"
    "s_lshl_b32 s31, s31, 8\n"
    "s_add_u32 s31, s31, s41\n"
    "s_add_u32 s31, s31, 21123072\n"
    "v_lshrrev_b32 v52, 1, v9\n"
    "buffer_store_dwordx2 v[48:49], v52, s[16:19], s31 offen sc1\n"
    "v_mov_b32 v81, 0\n"
    "s_lshl_b32 s31, s24, 4\n"
    "s_add_u32 s31, s31, s25\n"
    "s_lshl_b32 s35, s31, 7\n"
    "s_add_u32 s35, s35, 131072\n"
    "v_lshlrev_b32 v80, 15, v6\n"
    "s_mov_b64 exec, 3\n"
    "buffer_store_dword v81, v80, s[16:19], s35 offen sc1\n"
    "s_mov_b64 exec, -1\n"
    "s_lshl_b32 s35, s24, 10\n"
    "s_add_u32 s35, s35, 0\n"
    "v_and_b32 v80, 7, v6\n"
    "v_mul_u32_u24 v80, 16384, v80\n"
    "v_lshrrev_b32 v82, 3, v6\n"
    "v_lshl_add_u32 v80, v82, 8, v80\n"
    "v_mov_b32 v82, 0x7ffffff0\n"
    "s_cmp_eq_u32 s25, 0\n"
    "s_cselect_b64 s[90:91], 0, -1\n"
    "v_cndmask_b32 v80, v80, v82, s[90:91]\n"
    "s_mov_b64 exec, 0xffffff\n"
    "buffer_store_dword v81, v80, s[16:19], s35 offen sc1\n"
    "s_mov_b64 exec, -1\n"
    "s_waitcnt lgkmcnt(0)\n"
    "s_barrier\n"
    "v_bfe_u32 v79, v6, 2, 2\n"
    "v_lshl_add_u32 v79, v5, 3, v79\n"
    "v_lshlrev_b32 v79, 6, v79\n"
    "v_bfe_u32 v80, v6, 4, 1\n"
    "v_and_b32 v81, 3, v6\n"
    "v_lshl_add_u32 v80, v80, 2, v81\n"
    "v_lshl_add_u32 v79, v80, 3, v79\n"
    "v_add_u32 v79, 49152, v79\n"
    "s_mul_i32 s58, s25, 163840\n"
    "s_add_u32 s58, s58, 262144\n"
    "s_mov_b32 s59, 3072\n"
    "s_cmp_lt_u32 s63, 3\n"
    "s_cselect_b32 s59, s59, 1024\n"
    "s_mul_i32 s31, s63, 40\n"
    "s_cselect_b32 s31, s63, s31\n"
    "s_lshl_b32 s31, s31, 10\n"
    "s_add_u32 s58, s58, s31\n"
    "s_mul_i32 s31, s26, 3\n"
    "s_add_u32 s31, s31, 0\n"
    "s_min_u32 s31, s31, 9\n"
    "s_lshl_b32 s35, s31, 10\n"
    "v_add_u32 v73, s35, v79\n"
    "ds_read_b64_tr_b16 v[16:17], v73\n"
    "ds_read_b64_tr_b16 v[18:19], v73 offset:256\n"
    "s_mul_i32 s35, s62, 10\n"
    "s_add_u32 s31, s31, s35\n"
    "s_mul_i32 s61, s31, s59\n"
    "s_add_u32 s68, s61, s58\n"
    "s_mul_i32 s31, s26, 3\n"
    "s_add_u32 s31, s31, 1\n"
    "s_min_u32 s31, s31, 9\n"
    "s_lshl_b32 s35, s31, 10\n"
    "v_add_u32 v73, s35, v79\n"
    "ds_read_b64_tr_b16 v[20:21], v73\n"
    "ds_read_b64_tr_b16 v[22:23], v73 offset:256\n"
    "s_mul_i32 s35, s62, 10\n"
    "s_add_u32 s31, s31, s35\n"
    "s_mul_i32 s61, s31, s59\n"
    "s_add_u32 s69, s61, s58\n"
    "s_mul_i32 s31, s26, 3\n"
    "s_add_u32 s31, s31, 2\n"
    "s_min_u32 s31, s31, 9\n"
    "s_lshl_b32 s35, s31, 10\n"
    "v_add_u32 v73, s35, v79\n"
    "ds_read_b64_tr_b16 v[24:25], v73\n"
    "ds_read_b64_tr_b16 v[26:27], v73 offset:256\n"
    "s_mul_i32 s35, s62, 10\n"
    "s_add_u32 s31, s31, s35\n"
    "s_mul_i32 s61, s31, s59\n"
    "s_add_u32 s65, s61, s58\n"
    "s_waitcnt lgkmcnt(4)\n"
    "buffer_store_dwordx4 v[16:19], v1, s[16:19], s68 offen sc1\n"
    "s_waitcnt lgkmcnt(2)\n"
    "buffer_store_dwordx4 v[20:23], v1, s[16:19], s69 offen sc1\n"
    "s_waitcnt lgkmcnt(0)\n"
    "buffer_store_dwordx4 v[24:27], v1, s[16:19], s65 offen sc1\n"
    "v_dot2_f32_bf16 v90, v16, v222, 0\n"
    "v_dot2_f32_bf16 v90, v17, v222, v90\n"
    "v_dot2_f32_bf16 v90, v18, v222, v90\n"
    "v_dot2_f32_bf16 v90, v19, v222, v90\n"
    "v_dot2_f32_bf16 v91, v20, v222, 0\n"
    "v_dot2_f32_bf16 v91, v21, v222, v91\n"
    "v_dot2_f32_bf16 v91, v22, v222, v91\n"
    "v_dot2_f32_bf16 v91, v23, v222, v91\n"
    "v_dot2_f32_bf16 v92, v24, v222, 0\n"
    "v_dot2_f32_bf16 v92, v25, v222, v92\n"
    "v_dot2_f32_bf16 v92, v26, v222, v92\n"
    "v_dot2_f32_bf16 v92, v27, v222, v92\n"
    "s_nop 2\n"
    "s_cmp_eq_u32 s26, 3\n"
    "s_cselect_b64 s[90:91], -1, 0\n"
    "v_cndmask_b32 v91, v91, 0, s[90:91]\n"
    "v_cndmask_b32 v92, v92, 0, s[90:91]\n"
    "v_add_f32 v74, v90, v91\n"
    "v_add_f32 v74, v74, v92\n"
    "v_mov_b32 v80, v74\n"
    "s_nop 1\n"
    "v_permlane32_swap_b32 v74, v80\n"
    "s_nop 1\n"
    "v_add_f32 v74, v74, v80\n"
    "s_lshl_b32 s31, s26, 7\n"
    "s_add_u32 s31, s31, 161792\n"
    "v_lshl_add_u32 v80, v4, 2, s31\n"
    "ds_write_b32 v80, v74\n"
    "s_waitcnt lgkmcnt(0)\n"
    "s_barrier\n"
    "v_lshlrev_b32 v80, 2, v4\n"
    "v_add_u32 v80, 161792, v80\n"
    "ds_read_b32 v82, v80 offset:0\n"
    "ds_read_b32 v83, v80 offset:128\n"
    "ds_read_b32 v84, v80 offset:256\n"
    "ds_read_b32 v85, v80 offset:384\n"
    "s_waitcnt lgkmcnt(0)\n"
    "v_add_f32 v82, v82, v83\n"
    "v_add_f32 v84, v84, v85\n"
    "v_add_f32 v82, v82, v84\n"
    "v_bfe_u32 v87, v4, 2, 1\n"
    "v_lshlrev_b32 v87, 4, v87\n"
    "v_and_b32 v81, 3, v4\n"
    "v_add_u32 v87, v87, v81\n"
    "v_lshrrev_b32 v81, 3, v4\n"
    "v_lshl_add_u32 v87, v81, 2, v87\n"
    "v_lshlrev_b32 v87, 2, v87\n"
    "v_mov_b32 v81, 0x7ffffff0\n"
    "s_cmp_eq_u32 s26, 0\n"
    "s_cselect_b64 s[90:91], 0, -1\n"
    "s_or_b64 s[90:91], s[90:91], s[48:49]\n"
    "v_cndmask_b32 v80, v87, v81, s[90:91]\n"
    "s_mul_i32 s60, s62, 8192\n"
    "s_mul_i32 s31, s25, 512\n"
    "s_add_u32 s60, s60, s31\n"
    "s_lshl_b32 s31, s63, 7\n"
    "s_add_u32 s60, s60, s31\n"
    "s_add_u32 s60, s60, 10539008\n"
    "buffer_store_dword v82, v80, s[16:19], s60 offen sc1\n"
    "s_waitcnt vmcnt(10)\n"
    "s_cmp_lt_u32 s64, 108\n"
    "s_cbranch_scc1 LbkF_8_%=\n"
    "s_cmp_lt_u32 s64, 182\n"
    "s_cbranch_scc1 LbkO_9_%=\n"
    "s_branch LbkX_10_%=\n"
    "LbkF_8_%=:\n"
    "v_lshlrev_b32 v80, 8, v77\n"
    "v_lshl_add_u32 v80, v76, 4, v80\n"
    "s_mul_i32 s31, s26, 10240\n"
    "v_add_u32 v72, s31, v80\n"
    "ds_write_b128 v72, v[100:103] offset:0\n"
    "ds_write_b128 v72, v[104:107] offset:128\n"
    "ds_write_b128 v72, v[108:111] offset:2048\n"
    "ds_write_b128 v72, v[112:115] offset:2176\n"
    "ds_write_b128 v72, v[116:119] offset:4096\n"
    "ds_write_b128 v72, v[120:123] offset:4224\n"
    "ds_write_b128 v72, v[124:127] offset:6144\n"
    "ds_write_b128 v72, v[128:131] offset:6272\n"
    "ds_write_b128 v72, v[132:135] offset:8192\n"
    "ds_write_b128 v72, v[136:139] offset:8320\n"
    "s_branch LbkX_10_%=\n"
    "LbkO_9_%=:\n"
    "s_mul_i32 s31, s26, 10240\n"
    "v_add_u32 v72, s31, v1\n"
    "ds_write_b128 v72, v[100:103] offset:0\n"
    "ds_write_b128 v72, v[104:107] offset:1024\n"
    "ds_write_b128 v72, v[108:111] offset:2048\n"
    "ds_write_b128 v72, v[112:115] offset:3072\n"
    "ds_write_b128 v72, v[116:119] offset:4096\n"
    "ds_write_b128 v72, v[120:123] offset:5120\n"
    "ds_write_b128 v72, v[124:127] offset:6144\n"
    "ds_write_b128 v72, v[128:131] offset:7168\n"
    "ds_write_b128 v72, v[132:135] offset:8192\n"
    "ds_write_b128 v72, v[136:139] offset:9216\n"
    "LbkX_10_%=:\n"
    "s_waitcnt lgkmcnt(0)\n"
    "s_waitcnt vmcnt(0)\n"
    "s_barrier\n"
    "s_lshl_b32 s31, s24, 4\n"
    "s_add_u32 s31, s31, s25\n"
    "s_cmp_lg_u32 s26, 0\n"
    "s_cbranch_scc1 Lpf_11_%=\n"
    "s_lshl_b32 s35, s31, 7\n"
    "s_add_u32 s35, s35, 54153216\n"
    "v_mov_b32 v80, s47\n"
    "v_mov_b32 v81, s97\n"
    "v_mov_b32 v82, 0\n"
    "s_mov_b64 exec, 1\n"
    "buffer_store_dwordx2 v[80:81], v82, s[16:19], s35 offen sc1\n"
    "s_mov_b64 exec, -1\n"
    "Lpf_11_%=:\n"
    "s_cmp_lt_u32 s64, 108\n"
    "s_cbranch_scc1 LouF_12_%=\n"
    "s_cmp_lt_u32 s64, 182\n"
    "s_cbranch_scc1 LouO_13_%=\n"
    "s_cmp_lt_u32 s64, 226\n"
    "s_cbranch_scc0 LouY_14_%=\n"
    "v_mov_b32 v16, 0\n"
    "v_mov_b32 v17, 0\n"
    "v_mov_b32 v18, 0\n"
    "v_mov_b32 v19, 0\n"
    "s_add_u32 s66, s26, 12\n"
    "s_cmp_lt_u32 s64, 222\n"
    "s_cbranch_scc0 Lzcs_15_%=\n"
    "s_mul_i32 s58, s66, 286720\n"
    "s_add_u32 s31, s64, 58\n"
    "s_lshl_b32 s31, s31, 10\n"
    "s_add_u32 s58, s58, s31\n"
    "s_add_u32 s58, s58, 3706880\n"
    "buffer_store_dwordx4 v[16:19], v1, s[16:19], s58 offen sc1\n"
    "s_branch LouY_14_%=\n"
    "Lzcs_15_%=:\n"
    "s_sub_u32 s31, s64, 222\n"
    "s_mul_i32 s60, s31, 16384\n"
    "s_mul_i32 s31, s66, 1024\n"
    "s_add_u32 s60, s60, s31\n"
    "s_add_u32 s60, s60, 10572544\n"
    "v_lshrrev_b32 v20, 2, v9\n"
    "buffer_store_dword v16, v20, s[16:19], s60 offen sc1\n"
    "s_branch LouY_14_%=\n"
    "LouF_12_%=:\n"
    "s_lshr_b32 s65, s64, 2\n"
    "s_lshl_b32 s65, s65, 2\n"
    "s_add_u32 s65, s65, s26\n"
    "s_and_b32 s62, s64, 3\n"
    "s_mul_i32 s66, s65, 37450\n"
    "s_lshr_b32 s66, s66, 18\n"
    "s_mul_i32 s67, s66, 7\n"
    "s_sub_u32 s67, s65, s67\n"
    "s_sub_u32 s31, s65, 84\n"
    "s_mul_i32 s35, s31, 43691\n"
    "s_lshr_b32 s35, s35, 18\n"
    "s_mul_i32 s59, s35, 6\n"
    "s_sub_u32 s59, s31, s59\n"
    "s_add_u32 s35, s35, 12\n"
    "s_cmp_ge_u32 s65, 84\n"
    "s_cselect_b32 s66, s35, s66\n"
    "s_cselect_b32 s67, s59, s67\n"
    "s_mul_i32 s58, s66, 286720\n"
    "s_add_u32 s58, s58, 3706880\n"
    "s_mul_i32 s31, s62, 10\n"
    "s_mul_i32 s35, s31, 3\n"
    "s_add_u32 s35, s35, s67\n"
    "s_mul_i32 s61, s67, 40\n"
    "s_add_u32 s61, s61, s31\n"
    "s_mov_b32 s59, 3072\n"
    "s_cmp_lt_u32 s67, 3\n"
    "s_cselect_b32 s35, s35, s61\n"
    "s_cselect_b32 s59, s59, 1024\n"
    "s_lshl_b32 s35, s35, 10\n"
    "s_add_u32 s58, s58, s35\n"
    "v_bfe_u32 v79, v6, 2, 2\n"
    "v_lshl_add_u32 v79, v5, 3, v79\n"
    "v_lshlrev_b32 v79, 8, v79\n"
    "v_bfe_u32 v80, v6, 4, 1\n"
    "v_lshl_add_u32 v79, v80, 7, v79\n"
    "v_and_b32 v80, 1, v6\n"
    "v_lshl_add_u32 v79, v80, 4, v79\n"
    "v_bfe_u32 v80, v6, 1, 1\n"
    "v_lshl_add_u32 v79, v80, 3, v79\n"
    "s_lshl_b32 s31, s26, 5\n"
    "v_add_u32 v73, s31, v79\n"
    "ds_read_b64_tr_b16 v[16:17], v73 offset:0\n"
    "ds_read_b64_tr_b16 v[18:19], v73 offset:1024\n"
    "ds_read_b64_tr_b16 v[20:21], v73 offset:4096\n"
    "ds_read_b64_tr_b16 v[22:23], v73 offset:5120\n"
    "ds_read_b64_tr_b16 v[24:25], v73 offset:8192\n"
    "ds_read_b64_tr_b16 v[26:27], v73 offset:9216\n"
    "ds_read_b64_tr_b16 v[28:29], v73 offset:12288\n"
    "ds_read_b64_tr_b16 v[30:31], v73 offset:13312\n"
    "ds_read_b64_tr_b16 v[32:33], v73 offset:16384\n"
    "ds_read_b64_tr_b16 v[34:35], v73 offset:17408\n"
    "ds_read_b64_tr_b16 v[36:37], v73 offset:20480\n"
    "ds_read_b64_tr_b16 v[38:39], v73 offset:21504\n"
    "s_waitcnt lgkmcnt(10)\n"
    "buffer_store_dwordx4 v[16:19], v1, s[16:19], s58 offen sc1\n"
    "s_add_u32 s58, s58, s59\n"
    "ds_read_b64_tr_b16 v[40:41], v73 offset:24576\n"
    "ds_read_b64_tr_b16 v[42:43], v73 offset:25600\n"
    "s_waitcnt lgkmcnt(10)\n"
    "buffer_store_dwordx4 v[20:23], v1, s[16:19], s58 offen sc1\n"
    "s_add_u32 s58, s58, s59\n"
    "ds_read_b64_tr_b16 v[44:45], v73 offset:28672\n"
    "ds_read_b64_tr_b16 v[46:47], v73 offset:29696\n"
    "s_waitcnt lgkmcnt(10)\n"
    "buffer_store_dwordx4 v[24:27], v1, s[16:19], s58 offen sc1\n"
    "s_add_u32 s58, s58, s59\n"
    "ds_read_b64_tr_b16 v[48:49], v73 offset:32768\n"
    "ds_read_b64_tr_b16 v[50:51], v73 offset:33792\n"
    "s_waitcnt lgkmcnt(10)\n"
    "buffer_store_dwordx4 v[28:31], v1, s[16:19], s58 offen sc1\n"
    "s_add_u32 s58, s58, s59\n"
    "ds_read_b64_tr_b16 v[52:53], v73 offset:36864\n"
    "ds_read_b64_tr_b16 v[54:55], v73 offset:37888\n"
    "s_waitcnt lgkmcnt(10)\n"
    "buffer_store_dwordx4 v[32:35], v1, s[16:19], s58 offen sc1\n"
    "s_add_u32 s58, s58, s59\n"
    "s_waitcnt lgkmcnt(8)\n"
    "buffer_store_dwordx4 v[36:39], v1, s[16:19], s58 offen sc1\n"
    "s_add_u32 s58, s58, s59\n"
    "s_waitcnt lgkmcnt(6)\n"
    "buffer_store_dwordx4 v[40:43], v1, s[16:19], s58 offen sc1\n"
    "s_add_u32 s58, s58, s59\n"
    "s_waitcnt lgkmcnt(4)\n"
    "buffer_store_dwordx4 v[44:47], v1, s[16:19], s58 offen sc1\n"
    "s_add_u32 s58, s58, s59\n"
    "s_waitcnt lgkmcnt(2)\n"
    "buffer_store_dwordx4 v[48:51], v1, s[16:19], s58 offen sc1\n"
    "s_add_u32 s58, s58, s59\n"
    "s_waitcnt lgkmcnt(0)\n"
    "buffer_store_dwordx4 v[52:55], v1, s[16:19], s58 offen sc1\n"
    "s_add_u32 s58, s58, s59\n"
    "v_dot2_f32_bf16 v74, v16, v222, 0\n"
    "v_dot2_f32_bf16 v74, v17, v222, v74\n"
    "v_dot2_f32_bf16 v74, v18, v222, v74\n"
    "v_dot2_f32_bf16 v74, v19, v222, v74\n"
    "v_dot2_f32_bf16 v74, v20, v222, v74\n"
    "v_dot2_f32_bf16 v74, v21, v222, v74\n"
    "v_dot2_f32_bf16 v74, v22, v222, v74\n"
    "v_dot2_f32_bf16 v74, v23, v222, v74\n"
    "v_dot2_f32_bf16 v74, v24, v222, v74\n"
    "v_dot2_f32_bf16 v74, v25, v222, v74\n"
    "v_dot2_f32_bf16 v74, v26, v222, v74\n"
    "v_dot2_f32_bf16 v74, v27, v222, v74\n"
    "v_dot2_f32_bf16 v74, v28, v222, v74\n"
    "v_dot2_f32_bf16 v74, v29, v222, v74\n"
    "v_dot2_f32_bf16 v74, v30, v222, v74\n"
    "v_dot2_f32_bf16 v74, v31, v222, v74\n"
    "v_dot2_f32_bf16 v74, v32, v222, v74\n"
    "v_dot2_f32_bf16 v74, v33, v222, v74\n"
    "v_dot2_f32_bf16 v74, v34, v222, v74\n"
    "v_dot2_f32_bf16 v74, v35, v222, v74\n"
    "v_dot2_f32_bf16 v74, v36, v222, v74\n"
    "v_dot2_f32_bf16 v74, v37, v222, v74\n"
    "v_dot2_f32_bf16 v74, v38, v222, v74\n"
    "v_dot2_f32_bf16 v74, v39, v222, v74\n"
    "v_dot2_f32_bf16 v74, v40, v222, v74\n"
    "v_dot2_f32_bf16 v74, v41, v222, v74\n"
    "v_dot2_f32_bf16 v74, v42, v222, v74\n"
    "v_dot2_f32_bf16 v74, v43, v222, v74\n"
    "v_dot2_f32_bf16 v74, v44, v222, v74\n"
    "v_dot2_f32_bf16 v74, v45, v222, v74\n"
    "v_dot2_f32_bf16 v74, v46, v222, v74\n"
    "v_dot2_f32_bf16 v74, v47, v222, v74\n"
    "v_dot2_f32_bf16 v74, v48, v222, v74\n"
    "v_dot2_f32_bf16 v74, v49, v222, v74\n"
    "v_dot2_f32_bf16 v74, v50, v222, v74\n"
    "v_dot2_f32_bf16 v74, v51, v222, v74\n"
    "v_dot2_f32_bf16 v74, v52, v222, v74\n"
    "v_dot2_f32_bf16 v74, v53, v222, v74\n"
    "v_dot2_f32_bf16 v74, v54, v222, v74\n"
    "v_dot2_f32_bf16 v74, v55, v222, v74\n"
    "s_nop 2\n"
    "v_mov_b32 v80, v74\n"
    "s_nop 1\n"
    "v_permlane32_swap_b32 v74, v80\n"
    "s_nop 1\n"
    "v_add_f32 v74, v74, v80\n"
    "v_mov_b32 v81, 0x7ffffff0\n"
    "v_cndmask_b32 v80, v87, v81, s[48:49]\n"
    "s_mul_i32 s60, s62, 16384\n"
    "s_mul_i32 s31, s66, 1024\n"
    "s_add_u32 s60, s60, s31\n"
    "s_lshl_b32 s31, s67, 7\n"
    "s_add_u32 s60, s60, s31\n"
    "s_add_u32 s60, s60, 10571776\n"
    "buffer_store_dword v74, v80, s[16:19], s60 offen sc1\n"
    "s_branch LouY_14_%=\n"
    "LouO_13_%=:\n"
    "s_cmp_lt_u32 s64, 128\n"
    "s_mov_b32 s58, 8310784\n"
    "s_cselect_b32 s58, 2883584, s58\n"
    "s_mov_b32 s59, 138240\n"
    "s_cselect_b32 s59, 51200, s59\n"
    "s_mov_b32 s31, 128\n"
    "s_cselect_b32 s31, 108, s31\n"
    "s_sub_u32 s65, s64, s31\n"
    "s_mul_i32 s31, s65, 2560\n"
    "s_add_u32 s58, s58, s31\n"
    "s_lshl_b32 s31, s26, 2\n"
    "s_mul_i32 s35, s31, s59\n"
    "s_add_u32 s58, s58, s35\n"
    "v_bfe_u32 v79, v6, 2, 2\n"
    "v_lshrrev_b32 v80, 4, v6\n"
    "v_lshl_add_u32 v79, v80, 3, v79\n"
    "v_mul_u32_u24 v79, 1280, v79\n"
    "s_mul_i32 s31, s26, 320\n"
    "v_add_u32 v79, s31, v79\n"
    "v_and_b32 v81, 3, v6\n"
    "v_lshl_add_u32 v73, v81, 3, v79\n"
    "v_and_b32 v81, 1, v6\n"
    "v_lshl_add_u32 v71, v81, 3, v79\n"
    "v_add_u32 v71, 64, v71\n"
    "v_and_b32 v81, 15, v6\n"
    "v_lshlrev_b32 v82, 4, v81\n"
    "v_lshl_add_u32 v82, v80, 7, v82\n"
    "v_add_u32 v82, 2048, v82\n"
    "v_mov_b32 v86, 0x7ffffff0\n"
    "v_cmp_gt_u32 vcc, 8, v81\n"
    "s_nop 1\n"
    "v_cndmask_b32 v70, v86, v82, vcc\n"
    "ds_read_b64_tr_b16 v[16:17], v73 offset:0\n"
    "ds_read_b64_tr_b16 v[18:19], v73 offset:5120\n"
    "ds_read_b64_tr_b16 v[20:21], v73 offset:32\n"
    "ds_read_b64_tr_b16 v[22:23], v73 offset:5152\n"
    "ds_read_b64_tr_b16 v[24:25], v71 offset:0\n"
    "ds_read_b64_tr_b16 v[26:27], v71 offset:5120\n"
    "ds_read_b64_tr_b16 v[28:29], v73 offset:80\n"
    "ds_read_b64_tr_b16 v[30:31], v73 offset:5200\n"
    "ds_read_b64_tr_b16 v[32:33], v73 offset:112\n"
    "ds_read_b64_tr_b16 v[34:35], v73 offset:5232\n"
    "ds_read_b64_tr_b16 v[36:37], v71 offset:80\n"
    "ds_read_b64_tr_b16 v[38:39], v71 offset:5200\n"
    "s_waitcnt lgkmcnt(0)\n"
    "ds_read_b64_tr_b16 v[40:41], v73 offset:160\n"
    "ds_read_b64_tr_b16 v[42:43], v73 offset:5280\n"
    "ds_read_b64_tr_b16 v[44:45], v73 offset:192\n"
    "ds_read_b64_tr_b16 v[46:47], v73 offset:5312\n"
    "ds_read_b64_tr_b16 v[48:49], v71 offset:160\n"
    "ds_read_b64_tr_b16 v[50:51], v71 offset:5280\n"
    "ds_read_b64_tr_b16 v[52:53], v73 offset:240\n"
    "ds_read_b64_tr_b16 v[54:55], v73 offset:5360\n"
    "ds_read_b64_tr_b16 v[56:57], v73 offset:272\n"
    "ds_read_b64_tr_b16 v[58:59], v73 offset:5392\n"
    "ds_read_b64_tr_b16 v[60:61], v71 offset:240\n"
    "ds_read_b64_tr_b16 v[62:63], v71 offset:5360\n"
    "s_waitcnt lgkmcnt(0)\n"
    "buffer_store_dwordx4 v[16:19], v1, s[16:19], s58 offen sc1\n"
    "buffer_store_dwordx4 v[20:23], v1, s[16:19], s58 offen offset:1024 sc1\n"
    "buffer_store_dwordx4 v[24:27], v70, s[16:19], s58 offen sc1\n"
    "s_add_u32 s58, s58, s59\n"
    "buffer_store_dwordx4 v[28:31], v1, s[16:19], s58 offen sc1\n"
    "buffer_store_dwordx4 v[32:35], v1, s[16:19], s58 offen offset:1024 sc1\n"
    "buffer_store_dwordx4 v[36:39], v70, s[16:19], s58 offen sc1\n"
    "s_add_u32 s58, s58, s59\n"
    "buffer_store_dwordx4 v[40:43], v1, s[16:19], s58 offen sc1\n"
    "buffer_store_dwordx4 v[44:47], v1, s[16:19], s58 offen offset:1024 sc1\n"
    "buffer_store_dwordx4 v[48:51], v70, s[16:19], s58 offen sc1\n"
    "s_add_u32 s58, s58, s59\n"
    "buffer_store_dwordx4 v[52:55], v1, s[16:19], s58 offen sc1\n"
    "buffer_store_dwordx4 v[56:59], v1, s[16:19], s58 offen offset:1024 sc1\n"
    "buffer_store_dwordx4 v[60:63], v70, s[16:19], s58 offen sc1\n"
    "s_add_u32 s58, s58, s59\n"
    "LouY_14_%=:\n"
    "s_cmp_lg_u32 s26, 0\n"
    "s_cbranch_scc1 Lpw_16_%=\n"
    "v_lshlrev_b32 v208, 4, v6\n"
    "v_add_u32 v208, s25, v208\n"
    "s_lshl_b32 s35, s24, 4\n"
    "v_subrev_u32 v209, 16, v6\n"
    "v_add_u32 v209, s35, v209\n"
    "v_cmp_gt_u32 vcc, 16, v6\n"
    "s_nop 1\n"
    "v_cndmask_b32 v208, v209, v208, vcc\n"
    "v_lshlrev_b32 v208, 7, v208\n"
    "v_add_u32 v208, 54153216, v208\n"
    "s_mov_b32 s50, 0\n"
    "s_mov_b64 exec, 0xffffffff\n"
    "Lfp_17_%=:\n"
    "v_mov_b32 v210, 0\n"
    "v_mov_b32 v211, 0\n"
    "buffer_atomic_add_x2 v[210:211], v208, s[16:19], 0 offen sc0\n"
    "s_waitcnt vmcnt(0)\n"
    "v_cmp_ne_u32 vcc, s47, v210\n"
    "v_cmp_ne_u32 s[90:91], s97, v211\n"
    "s_add_u32 s50, s50, 1\n"
    "s_nop 1\n"
    "s_or_b64 vcc, vcc, s[90:91]\n"
    "s_cbranch_vccz Lfpd_18_%=\n"
    "s_cmp_lt_u32 s50, 20000\n"
    "s_cbranch_scc1 Lfp_17_%=\n"
    "Lfpd_18_%=:\n"
    "s_mov_b64 exec, -1\n"
    "Lpw_16_%=:\n"
    "s_waitcnt lgkmcnt(0)\n"
    "s_barrier\n"
    "s_mov_b32 s28, 0\n"
    "s_mov_b32 s40, 0\n"
    "s_mov_b32 s41, 0\n"
    "s_mov_b32 s42, 0\n"
    "s_mov_b32 s43, 0\n"
    "s_mov_b32 s44, 0\n"
    "s_mov_b32 s46, 0\n"
    "s_mov_b32 s45, 0\n"
    "s_mul_i32 s37, s25, 163840\n"
    "s_add_u32 s37, s37, 262144\n"
    "s_add_u32 s37, s37, s29\n"
    "s_add_u32 m0, s29, 32768\n"
    "s_add_u32 s14, s37, 0\n"
    "buffer_load_dwordx4 v1, s[16:19], s14 offen offset:0 sc1 lds\n"
    "buffer_load_dwordx4 v1, s[16:19], s14 offen offset:1024 sc1 lds\n"
    "buffer_load_dwordx4 v1, s[16:19], s14 offen offset:2048 sc1 lds\n"
    "buffer_load_dwordx4 v1, s[16:19], s14 offen offset:3072 sc1 lds\n"
    "s_add_u32 m0, s29, 49152\n"
    "s_add_u32 s14, s37, 16384\n"
    "buffer_load_dwordx4 v1, s[16:19], s14 offen offset:0 sc1 lds\n"
    "buffer_load_dwordx4 v1, s[16:19], s14 offen offset:1024 sc1 lds\n"
    "buffer_load_dwordx4 v1, s[16:19], s14 offen offset:2048 sc1 lds\n"
    "buffer_load_dwordx4 v1, s[16:19], s14 offen offset:3072 sc1 lds\n"
    "s_add_u32 m0, s29, 65536\n"
    "s_add_u32 s14, s37, 32768\n"
    "buffer_load_dwordx4 v1, s[16:19], s14 offen offset:0 sc1 lds\n"
    "buffer_load_dwordx4 v1, s[16:19], s14 offen offset:1024 sc1 lds\n"
    "buffer_load_dwordx4 v1, s[16:19], s14 offen offset:2048 sc1 lds\n"
    "buffer_load_dwordx4 v1, s[16:19], s14 offen offset:3072 sc1 lds\n"
    "s_add_u32 m0, s29, 81920\n"
    "s_add_u32 s14, s37, 49152\n"
    "buffer_load_dwordx4 v1, s[16:19], s14 offen offset:0 sc1 lds\n"
    "buffer_load_dwordx4 v1, s[16:19], s14 offen offset:1024 sc1 lds\n"
    "buffer_load_dwordx4 v1, s[16:19], s14 offen offset:2048 sc1 lds\n"
    "buffer_load_dwordx4 v1, s[16:19], s14 offen offset:3072 sc1 lds\n"
    "s_add_u32 m0, s29, 98304\n"
    "s_add_u32 s14, s37, 65536\n"
    "buffer_load_dwordx4 v1, s[16:19], s14 offen offset:0 sc1 lds\n"
    "buffer_load_dwordx4 v1, s[16:19], s14 offen offset:1024 sc1 lds\n"
    "buffer_load_dwordx4 v1, s[16:19], s14 offen offset:2048 sc1 lds\n"
    "buffer_load_dwordx4 v1, s[16:19], s14 offen offset:3072 sc1 lds\n"
    "s_add_u32 m0, s29, 114688\n"
    "s_add_u32 s14, s37, 81920\n"
    "buffer_load_dwordx4 v1, s[16:19], s14 offen offset:0 sc1 lds\n"
    "buffer_load_dwordx4 v1, s[16:19], s14 offen offset:1024 sc1 lds\n"
    "buffer_load_dwordx4 v1, s[16:19], s14 offen offset:2048 sc1 lds\n"
    "buffer_load_dwordx4 v1, s[16:19], s14 offen offset:3072 sc1 lds\n"
    "s_add_u32 m0, s29, 131072\n"
    "s_add_u32 s14, s37, 98304\n"
    "buffer_load_dwordx4 v1, s[16:19], s14 offen offset:0 sc1 lds\n"
    "buffer_load_dwordx4 v1, s[16:19], s14 offen offset:1024 sc1 lds\n"
    "buffer_load_dwordx4 v1, s[16:19], s14 offen offset:2048 sc1 lds\n"
    "buffer_load_dwordx4 v1, s[16:19], s14 offen offset:3072 sc1 lds\n"
    "s_add_u32 m0, s29, 0\n"
    "s_add_u32 s14, s37, 114688\n"
    "buffer_load_dwordx4 v1, s[16:19], s14 offen offset:0 sc1 lds\n"
    "buffer_load_dwordx4 v1, s[16:19], s14 offen offset:1024 sc1 lds\n"
    "buffer_load_dwordx4 v1, s[16:19], s14 offen offset:2048 sc1 lds\n"
    "buffer_load_dwordx4 v1, s[16:19], s14 offen offset:3072 sc1 lds\n"
    "s_waitcnt vmcnt(32)\n"
    "s_barrier\n"
    "s_cmp_lg_u32 s26, 0\n"
    "s_cbranch_scc1 Lpf_19_%=\n"
    "s_lshl_b32 s35, s11, 7\n"
    "s_add_u32 s35, s35, 54185984\n"
    "v_mov_b32 v80, s47\n"
    "v_mov_b32 v81, s97\n"
    "v_mov_b32 v82, 0\n"
    "s_mov_b64 exec, 1\n"
    "buffer_store_dwordx2 v[80:81], v82, s[16:19], s35 offen sc1\n"
    "s_mov_b64 exec, -1\n"
    "Lpf_19_%=:\n"
    "v_lshl_add_u32 v209, v5, 2, 0\n"
    "v_cmp_le_u32 s[58:59], v209, v4\n"
    "v_lshl_add_u32 v209, v5, 2, 1\n"
    "v_cmp_le_u32 s[60:61], v209, v4\n"
    "v_lshl_add_u32 v209, v5, 2, 2\n"
    "v_cmp_le_u32 s[62:63], v209, v4\n"
    "v_lshl_add_u32 v209, v5, 2, 3\n"
    "v_cmp_le_u32 s[64:65], v209, v4\n"
    "v_lshl_add_u32 v209, v5, 2, 8\n"
    "v_cmp_le_u32 s[66:67], v209, v4\n"
    "v_lshl_add_u32 v209, v5, 2, 9\n"
    "v_cmp_le_u32 s[68:69], v209, v4\n"
    "v_lshl_add_u32 v209, v5, 2, 10\n"
    "v_cmp_le_u32 s[70:71], v209, v4\n"
    "v_lshl_add_u32 v209, v5, 2, 11\n"
    "v_cmp_le_u32 s[72:73], v209, v4\n"
    "v_lshl_add_u32 v209, v5, 2, 16\n"
    "v_cmp_le_u32 s[74:75], v209, v4\n"
    "v_lshl_add_u32 v209, v5, 2, 17\n"
    "v_cmp_le_u32 s[76:77], v209, v4\n"
    "v_lshl_add_u32 v209, v5, 2, 18\n"
    "v_cmp_le_u32 s[78:79], v209, v4\n"
    "v_lshl_add_u32 v209, v5, 2, 19\n"
    "v_cmp_le_u32 s[80:81], v209, v4\n"
    "v_lshl_add_u32 v209, v5, 2, 24\n"
    "v_cmp_le_u32 s[82:83], v209, v4\n"
    "v_lshl_add_u32 v209, v5, 2, 25\n"
    "v_cmp_le_u32 s[84:85], v209, v4\n"
    "v_lshl_add_u32 v209, v5, 2, 26\n"
    "v_cmp_le_u32 s[86:87], v209, v4\n"
    "v_lshl_add_u32 v209, v5, 2, 27\n"
    "v_cmp_le_u32 s[88:89], v209, v4\n"
    "s_cmp_lg_u32 s26, 0\n"
    "s_cbranch_scc1 Lcs_20_%=\n"
    "s_mul_i32 s31, s25, 512\n"
    "s_add_u32 s31, s31, 10539008\n"
    "v_lshlrev_b32 v208, 3, v6\n"
    "buffer_load_dwordx2 v[16:17], v208, s[16:19], s31 offen sc1\n"
    "s_add_u32 s31, s31, 8192\n"
    "buffer_load_dwordx2 v[20:21], v208, s[16:19], s31 offen sc1\n"
    "s_add_u32 s31, s31, 8192\n"
    "buffer_load_dwordx2 v[24:25], v208, s[16:19], s31 offen sc1\n"
    "s_add_u32 s31, s31, 8192\n"
    "buffer_load_dwordx2 v[28:29], v208, s[16:19], s31 offen sc1\n"
    "s_waitcnt vmcnt(0)\n"
    "v_add_f32 v16, v16, v20\n"
    "v_add_f32 v24, v24, v28\n"
    "v_add_f32 v16, v16, v24\n"
    "v_add_f32 v17, v17, v21\n"
    "v_add_f32 v25, v25, v29\n"
    "v_add_f32 v17, v17, v25\n"
    "v_add_u32 v208, 159744, v208\n"
    "ds_write_b64 v208, v[16:17]\n"
    "s_waitcnt lgkmcnt(0)\n"
    "Lcs_20_%=:\n"
    "s_waitcnt vmcnt(0)\n"
    "s_barrier\n"
    "Ltop_21_%=:\n"
    "s_mul_i32 s38, s27, 40960\n"
    "s_add_u32 s38, s38, s40\n"
    "s_add_u32 s38, s38, 10637312\n"
    "s_add_u32 s15, s38, 0\n"
    "buffer_load_dwordx4 a[0:3], v1, s[16:19], s15 offen offset:0 sc1\n"
    "buffer_load_dwordx4 a[4:7], v1, s[16:19], s15 offen offset:1024 sc1\n"
    "buffer_load_dwordx4 a[8:11], v1, s[16:19], s15 offen offset:2048 sc1\n"
    "buffer_load_dwordx4 a[12:15], v1, s[16:19], s15 offen offset:3072 sc1\n"
    "s_add_u32 s15, s38, 4096\n"
    "buffer_load_dwordx4 a[16:19], v1, s[16:19], s15 offen offset:0 sc1\n"
    "buffer_load_dwordx4 a[20:23], v1, s[16:19], s15 offen offset:1024 sc1\n"
    "buffer_load_dwordx4 a[24:27], v1, s[16:19], s15 offen offset:2048 sc1\n"
    "buffer_load_dwordx4 a[28:31], v1, s[16:19], s15 offen offset:3072 sc1\n"
    "s_add_u32 s15, s38, 8192\n"
    "buffer_load_dwordx4 a[32:35], v1, s[16:19], s15 offen offset:0 sc1\n"
    "buffer_load_dwordx4 a[36:39], v1, s[16:19], s15 offen offset:1024 sc1\n"
    "buffer_load_dwordx4 a[40:43], v1, s[16:19], s15 offen offset:2048 sc1\n"
    "buffer_load_dwordx4 a[44:47], v1, s[16:19], s15 offen offset:3072 sc1\n"
    "ds_read_b128 v[168:171], v1 offset:32768\n"
    "ds_read_b128 v[172:175], v1 offset:33792\n"
    "ds_read_b128 v[176:179], v1 offset:34816\n"
    "ds_read_b128 v[180:183], v1 offset:35840\n"
    "ds_read_b128 v[184:187], v1 offset:36864\n"
    "ds_read_b128 v[188:191], v1 offset:37888\n"
    "ds_read_b128 v[192:195], v1 offset:38912\n"
    "ds_read_b128 v[196:199], v1 offset:39936\n"
    "s_waitcnt vmcnt(11) lgkmcnt(4)\n"
    "v_mfma_f32_32x32x16_bf16 v[16:31], v[168:171], a[0:3], 0\n"
    "ds_read_b128 v[200:203], v1 offset:40960\n"
    "v_mfma_f32_32x32x16_bf16 v[32:47], v[172:175], a[0:3], 0\n"
    "ds_read_b128 v[204:207], v1 offset:41984\n"
    "s_add_u32 s15, s38, 12288\n"
    "buffer_load_dwordx4 a[48:51], v1, s[16:19], s15 offen offset:0 sc1\n"
    "v_mfma_f32_32x32x16_bf16 v[48:63], v[176:179], a[0:3], 0\n"
    "ds_read_b128 v[168:171], v1 offset:43008\n"
    "s_waitcnt vmcnt(11)\n"
    "v_mfma_f32_32x32x16_bf16 v[16:31], v[180:183], a[4:7], v[16:31]\n"
    "ds_read_b128 v[172:175], v1 offset:44032\n"
    "s_waitcnt lgkmcnt(4)\n"
    "v_mfma_f32_32x32x16_bf16 v[32:47], v[184:187], a[4:7], v[32:47]\n"
    "ds_read_b128 v[176:179], v1 offset:45056\n"
    "buffer_load_dwordx4 a[52:55], v1, s[16:19], s15 offen offset:1024 sc1\n"
    "v_mfma_f32_32x32x16_bf16 v[48:63], v[188:191], a[4:7], v[48:63]\n"
    "ds_read_b128 v[180:183], v1 offset:46080\n"
    "s_waitcnt vmcnt(11)\n"
    "v_mfma_f32_32x32x16_bf16 v[16:31], v[192:195], a[8:11], v[16:31]\n"
    "ds_read_b128 v[184:187], v1 offset:47104\n"
    "v_mfma_f32_32x32x16_bf16 v[32:47], v[196:199], a[8:11], v[32:47]\n"
    "ds_read_b128 v[188:191], v1 offset:48128\n"
    "buffer_load_dwordx4 a[56:59], v1, s[16:19], s15 offen offset:2048 sc1\n"
    "s_waitcnt lgkmcnt(4)\n"
    "v_mfma_f32_32x32x16_bf16 v[48:63], v[200:203], a[8:11], v[48:63]\n"
    "ds_read_b128 v[192:195], v1 offset:49152\n"
    "s_waitcnt vmcnt(11)\n"
    "v_mfma_f32_32x32x16_bf16 v[16:31], v[204:207], a[12:15], v[16:31]\n"
    "ds_read_b128 v[196:199], v1 offset:50176\n"
    "v_mfma_f32_32x32x16_bf16 v[32:47], v[168:171], a[12:15], v[32:47]\n"
    "ds_read_b128 v[200:203], v1 offset:51200\n"
    "buffer_load_dwordx4 a[60:63], v1, s[16:19], s15 offen offset:3072 sc1\n"
    "v_mfma_f32_32x32x16_bf16 v[48:63], v[172:175], a[12:15], v[48:63]\n"
    "ds_read_b128 v[204:207], v1 offset:52224\n"
    "s_waitcnt vmcnt(11) lgkmcnt(4)\n"
    "v_mfma_f32_32x32x16_bf16 v[16:31], v[176:179], a[16:19], v[16:31]\n"
    "ds_read_b128 v[168:171], v1 offset:53248\n"
    "v_mfma_f32_32x32x16_bf16 v[32:47], v[180:183], a[16:19], v[32:47]\n"
    "ds_read_b128 v[172:175], v1 offset:54272\n"
    "s_add_u32 s15, s38, 16384\n"
    "buffer_load_dwordx4 a[64:67], v1, s[16:19], s15 offen offset:0 sc1\n"
    "v_mfma_f32_32x32x16_bf16 v[48:63], v[184:187], a[16:19], v[48:63]\n"
    "ds_read_b128 v[176:179], v1 offset:55296\n"
    "s_waitcnt vmcnt(11)\n"
    "v_mfma_f32_32x32x16_bf16 v[16:31], v[188:191], a[20:23], v[16:31]\n"
    "ds_read_b128 v[180:183], v1 offset:56320\n"
    "s_waitcnt lgkmcnt(4)\n"
    "v_mfma_f32_32x32x16_bf16 v[32:47], v[192:195], a[20:23], v[32:47]\n"
    "ds_read_b128 v[184:187], v1 offset:57344\n"
    "buffer_load_dwordx4 a[68:71], v1, s[16:19], s15 offen offset:1024 sc1\n"
    "v_mfma_f32_32x32x16_bf16 v[48:63], v[196:199], a[20:23], v[48:63]\n"
    "ds_read_b128 v[188:191], v1 offset:58368\n"
    "s_waitcnt vmcnt(11)\n"
    "v_mfma_f32_32x32x16_bf16 v[16:31], v[200:203], a[24:27], v[16:31]\n"
    "ds_read_b128 v[192:195], v1 offset:59392\n"
    "v_mfma_f32_32x32x16_bf16 v[32:47], v[204:207], a[24:27], v[32:47]\n"
    "ds_read_b128 v[196:199], v1 offset:60416\n"
    "buffer_load_dwordx4 a[72:75], v1, s[16:19], s15 offen offset:2048 sc1\n"
    "s_waitcnt lgkmcnt(4)\n"
    "v_mfma_f32_32x32x16_bf16 v[48:63], v[168:171], a[24:27], v[48:63]\n"
    "ds_read_b128 v[200:203], v1 offset:61440\n"
    "s_waitcnt vmcnt(11)\n"
    "v_mfma_f32_32x32x16_bf16 v[16:31], v[172:175], a[28:31], v[16:31]\n"
    "ds_read_b128 v[204:207], v1 offset:62464\n"
    "v_mfma_f32_32x32x16_bf16 v[32:47], v[176:179], a[28:31], v[32:47]\n"
    "ds_read_b128 v[168:171], v1 offset:63488\n"
    "buffer_load_dwordx4 a[76:79], v1, s[16:19], s15 offen offset:3072 sc1\n"
    "v_mfma_f32_32x32x16_bf16 v[48:63], v[180:183], a[28:31], v[48:63]\n"
    "ds_read_b128 v[172:175], v1 offset:64512\n"
    "s_waitcnt vmcnt(11) lgkmcnt(4)\n"
    "v_mfma_f32_32x32x16_bf16 v[16:31], v[184:187], a[32:35], v[16:31]\n"
    "ds_read_b128 v[176:179], v2 offset:0\n"
    "v_mfma_f32_32x32x16_bf16 v[32:47], v[188:191], a[32:35], v[32:47]\n"
    "ds_read_b128 v[180:183], v2 offset:1024\n"
    "s_add_u32 s15, s38, 20480\n"
    "buffer_load_dwordx4 a[80:83], v1, s[16:19], s15 offen offset:0 sc1\n"
    "v_mfma_f32_32x32x16_bf16 v[48:63], v[192:195], a[32:35], v[48:63]\n"
    "ds_read_b128 v[184:187], v2 offset:2048\n"
    "s_waitcnt vmcnt(11)\n"
    "v_mfma_f32_32x32x16_bf16 v[16:31], v[196:199], a[36:39], v[16:31]\n"
    "ds_read_b128 v[188:191], v2 offset:3072\n"
    "s_waitcnt lgkmcnt(4)\n"
    "v_mfma_f32_32x32x16_bf16 v[32:47], v[200:203], a[36:39], v[32:47]\n"
    "ds_read_b128 v[192:195], v2 offset:4096\n"
    "buffer_load_dwordx4 a[84:87], v1, s[16:19], s15 offen offset:1024 sc1\n"
    "v_mfma_f32_32x32x16_bf16 v[48:63], v[204:207], a[36:39], v[48:63]\n"
    "ds_read_b128 v[196:199], v2 offset:5120\n"
    "s_waitcnt vmcnt(11)\n"
    "v_mfma_f32_32x32x16_bf16 v[16:31], v[168:171], a[40:43], v[16:31]\n"
    "ds_read_b128 v[200:203], v2 offset:6144\n"
    "v_mfma_f32_32x32x16_bf16 v[32:47], v[172:175], a[40:43], v[32:47]\n"
    "ds_read_b128 v[204:207], v2 offset:7168\n"
    "buffer_load_dwordx4 a[88:91], v1, s[16:19], s15 offen offset:2048 sc1\n"
    "s_waitcnt lgkmcnt(4)\n"
    "v_mfma_f32_32x32x16_bf16 v[48:63], v[176:179], a[40:43], v[48:63]\n"
    "ds_read_b128 v[168:171], v2 offset:8192\n"
    "s_waitcnt vmcnt(11)\n"
    "v_mfma_f32_32x32x16_bf16 v[16:31], v[180:183], a[44:47], v[16:31]\n"
    "ds_read_b128 v[172:175], v2 offset:9216\n"
    "v_mfma_f32_32x32x16_bf16 v[32:47], v[184:187], a[44:47], v[32:47]\n"
    "ds_read_b128 v[176:179], v2 offset:10240\n"
    "buffer_load_dwordx4 a[92:95], v1, s[16:19], s15 offen offset:3072 sc1\n"
    "v_mfma_f32_32x32x16_bf16 v[48:63], v[188:191], a[44:47], v[48:63]\n"
    "ds_read_b128 v[180:183], v2 offset:11264\n"
    "s_waitcnt vmcnt(11) lgkmcnt(4)\n"
    "v_mfma_f32_32x32x16_bf16 v[16:31], v[192:195], a[48:51], v[16:31]\n"
    "ds_read_b128 v[184:187], v2 offset:12288\n"
    "v_mfma_f32_32x32x16_bf16 v[32:47], v[196:199], a[48:51], v[32:47]\n"
    "ds_read_b128 v[188:191], v2 offset:13312\n"
    "s_add_u32 s15, s38, 24576\n"
    "buffer_load_dwordx4 a[96:99], v1, s[16:19], s15 offen offset:0 sc1\n"
    "v_mfma_f32_32x32x16_bf16 v[48:63], v[200:203], a[48:51], v[48:63]\n"
    "ds_read_b128 v[192:195], v2 offset:14336\n"
    "s_waitcnt vmcnt(11)\n"
    "v_mfma_f32_32x32x16_bf16 v[16:31], v[204:207], a[52:55], v[16:31]\n"
    "ds_read_b128 v[196:199], v2 offset:15360\n"
    "s_waitcnt lgkmcnt(4)\n"
    "v_mfma_f32_32x32x16_bf16 v[32:47], v[168:171], a[52:55], v[32:47]\n"
    "s_barrier\n"
    "ds_read_b128 v[200:203], v2 offset:16384\n"
    "buffer_load_dwordx4 a[100:103], v1, s[16:19], s15 offen offset:1024 sc1\n"
    "v_mfma_f32_32x32x16_bf16 v[48:63], v[172:175], a[52:55], v[48:63]\n"
    "ds_read_b128 v[204:207], v2 offset:17408\n"
    "s_waitcnt vmcnt(11)\n"
    "v_mfma_f32_32x32x16_bf16 v[16:31], v[176:179], a[56:59], v[16:31]\n"
    "ds_read_b128 v[168:171], v2 offset:18432\n"
    "v_mfma_f32_32x32x16_bf16 v[32:47], v[180:183], a[56:59], v[32:47]\n"
    "ds_read_b128 v[172:175], v2 offset:19456\n"
    "buffer_load_dwordx4 a[104:107], v1, s[16:19], s15 offen offset:2048 sc1\n"
    "s_waitcnt lgkmcnt(4)\n"
    "v_mfma_f32_32x32x16_bf16 v[48:63], v[184:187], a[56:59], v[48:63]\n"
    "ds_read_b128 v[176:179], v2 offset:20480\n"
    "s_waitcnt vmcnt(11)\n"
    "v_mfma_f32_32x32x16_bf16 v[16:31], v[188:191], a[60:63], v[16:31]\n"
    "ds_read_b128 v[180:183], v2 offset:21504\n"
    "v_mfma_f32_32x32x16_bf16 v[32:47], v[192:195], a[60:63], v[32:47]\n"
    "ds_read_b128 v[184:187], v2 offset:22528\n"
    "buffer_load_dwordx4 a[108:111], v1, s[16:19], s15 offen offset:3072 sc1\n"
    "v_mfma_f32_32x32x16_bf16 v[48:63], v[196:199], a[60:63], v[48:63]\n"
    "ds_read_b128 v[188:191], v2 offset:23552\n"
    "s_waitcnt vmcnt(11) lgkmcnt(4)\n"
    "v_mfma_f32_32x32x16_bf16 v[16:31], v[200:203], a[64:67], v[16:31]\n"
    "ds_read_b128 v[192:195], v2 offset:24576\n"
    "v_mfma_f32_32x32x16_bf16 v[32:47], v[204:207], a[64:67], v[32:47]\n"
    "ds_read_b128 v[196:199], v2 offset:25600\n"
    "s_add_u32 s15, s38, 28672\n"
    "buffer_load_dwordx4 a[112:115], v1, s[16:19], s15 offen offset:0 sc1\n"
    "v_mfma_f32_32x32x16_bf16 v[48:63], v[168:171], a[64:67], v[48:63]\n"
    "ds_read_b128 v[200:203], v2 offset:26624\n"
    "s_waitcnt vmcnt(11)\n"
    "v_mfma_f32_32x32x16_bf16 v[16:31], v[172:175], a[68:71], v[16:31]\n"
    "ds_read_b128 v[204:207], v2 offset:27648\n"
    "s_waitcnt lgkmcnt(4)\n"
    "v_mfma_f32_32x32x16_bf16 v[32:47], v[176:179], a[68:71], v[32:47]\n"
    "ds_read_b128 v[168:171], v2 offset:28672\n"
    "buffer_load_dwordx4 a[116:119], v1, s[16:19], s15 offen offset:1024 sc1\n"
    "v_mfma_f32_32x32x16_bf16 v[48:63], v[180:183], a[68:71], v[48:63]\n"
    "ds_read_b128 v[172:175], v2 offset:29696\n"
    "s_waitcnt vmcnt(11)\n"
    "v_mfma_f32_32x32x16_bf16 v[16:31], v[184:187], a[72:75], v[16:31]\n"
    "ds_read_b128 v[176:179], v2 offset:30720\n"
    "v_mfma_f32_32x32x16_bf16 v[32:47], v[188:191], a[72:75], v[32:47]\n"
    "ds_read_b128 v[180:183], v2 offset:31744\n"
    "buffer_load_dwordx4 a[120:123], v1, s[16:19], s15 offen offset:2048 sc1\n"
    "s_waitcnt lgkmcnt(4)\n"
    "v_mfma_f32_32x32x16_bf16 v[48:63], v[192:195], a[72:75], v[48:63]\n"
    "ds_read_b128 v[184:187], v2 offset:32768\n"
    "s_waitcnt vmcnt(11)\n"
    "v_mfma_f32_32x32x16_bf16 v[16:31], v[196:199], a[76:79], v[16:31]\n"
    "ds_read_b128 v[188:191], v2 offset:33792\n"
    "v_mfma_f32_32x32x16_bf16 v[32:47], v[200:203], a[76:79], v[32:47]\n"
    "ds_read_b128 v[192:195], v2 offset:34816\n"
    "buffer_load_dwordx4 a[124:127], v1, s[16:19], s15 offen offset:3072 sc1\n"
    "v_mfma_f32_32x32x16_bf16 v[48:63], v[204:207], a[76:79], v[48:63]\n"
    "ds_read_b128 v[196:199], v2 offset:35840\n"
    "s_waitcnt vmcnt(11) lgkmcnt(4)\n"
    "v_mfma_f32_32x32x16_bf16 v[16:31], v[168:171], a[80:83], v[16:31]\n"
    "ds_read_b128 v[200:203], v2 offset:36864\n"
    "v_mfma_f32_32x32x16_bf16 v[32:47], v[172:175], a[80:83], v[32:47]\n"
    "ds_read_b128 v[204:207], v2 offset:37888\n"
    "s_add_u32 s15, s38, 32768\n"
    "buffer_load_dwordx4 a[128:131], v1, s[16:19], s15 offen offset:0 sc1\n"
    "v_mfma_f32_32x32x16_bf16 v[48:63], v[176:179], a[80:83], v[48:63]\n"
    "ds_read_b128 v[168:171], v2 offset:38912\n"
    "s_waitcnt vmcnt(11)\n"
    "v_mfma_f32_32x32x16_bf16 v[16:31], v[180:183], a[84:87], v[16:31]\n"
    "ds_read_b128 v[172:175], v2 offset:39936\n"
    "s_waitcnt lgkmcnt(4)\n"
    "v_mfma_f32_32x32x16_bf16 v[32:47], v[184:187], a[84:87], v[32:47]\n"
    "ds_read_b128 v[176:179], v2 offset:40960\n"
    "buffer_load_dwordx4 a[132:135], v1, s[16:19], s15 offen offset:1024 sc1\n"
    "v_mfma_f32_32x32x16_bf16 v[48:63], v[188:191], a[84:87], v[48:63]\n"
    "ds_read_b128 v[180:183], v2 offset:41984\n"
    "s_waitcnt vmcnt(11)\n"
    "v_mfma_f32_32x32x16_bf16 v[16:31], v[192:195], a[88:91], v[16:31]\n"
    "ds_read_b128 v[184:187], v2 offset:43008\n"
    "v_mfma_f32_32x32x16_bf16 v[32:47], v[196:199], a[88:91], v[32:47]\n"
    "ds_read_b128 v[188:191], v2 offset:44032\n"
    "buffer_load_dwordx4 a[136:139], v1, s[16:19], s15 offen offset:2048 sc1\n"
    "s_waitcnt lgkmcnt(4)\n"
    "v_mfma_f32_32x32x16_bf16 v[48:63], v[200:203], a[88:91], v[48:63]\n"
    "ds_read_b128 v[192:195], v2 offset:45056\n"
    "s_waitcnt vmcnt(11)\n"
    "v_mfma_f32_32x32x16_bf16 v[16:31], v[204:207], a[92:95], v[16:31]\n"
    "ds_read_b128 v[196:199], v2 offset:46080\n"
    "v_mfma_f32_32x32x16_bf16 v[32:47], v[168:171], a[92:95], v[32:47]\n"
    "ds_read_b128 v[200:203], v2 offset:47104\n"
    "buffer_load_dwordx4 a[140:143], v1, s[16:19], s15 offen offset:3072 sc1\n"
    "v_mfma_f32_32x32x16_bf16 v[48:63], v[172:175], a[92:95], v[48:63]\n"
    "ds_read_b128 v[204:207], v2 offset:48128\n"
    "s_waitcnt vmcnt(11) lgkmcnt(4)\n"
    "v_mfma_f32_32x32x16_bf16 v[16:31], v[176:179], a[96:99], v[16:31]\n"
    "ds_read_b128 v[168:171], v2 offset:49152\n"
    "v_mfma_f32_32x32x16_bf16 v[32:47], v[180:183], a[96:99], v[32:47]\n"
    "ds_read_b128 v[172:175], v2 offset:50176\n"
    "s_add_u32 s15, s38, 36864\n"
    "buffer_load_dwordx4 a[144:147], v1, s[16:19], s15 offen offset:0 sc1\n"
    "v_mfma_f32_32x32x16_bf16 v[48:63], v[184:187], a[96:99], v[48:63]\n"
    "ds_read_b128 v[176:179], v2 offset:51200\n"
    "s_waitcnt vmcnt(11)\n"
    "v_mfma_f32_32x32x16_bf16 v[16:31], v[188:191], a[100:103], v[16:31]\n"
    "ds_read_b128 v[180:183], v2 offset:52224\n"
    "s_waitcnt lgkmcnt(4)\n"
    "v_mfma_f32_32x32x16_bf16 v[32:47], v[192:195], a[100:103], v[32:47]\n"
    "ds_read_b128 v[184:187], v2 offset:53248\n"
    "buffer_load_dwordx4 a[148:151], v1, s[16:19], s15 offen offset:1024 sc1\n"
    "v_mfma_f32_32x32x16_bf16 v[48:63], v[196:199], a[100:103], v[48:63]\n"
    "ds_read_b128 v[188:191], v2 offset:54272\n"
    "s_waitcnt vmcnt(11)\n"
    "v_mfma_f32_32x32x16_bf16 v[16:31], v[200:203], a[104:107], v[16:31]\n"
    "ds_read_b128 v[192:195], v2 offset:55296\n"
    "v_mfma_f32_32x32x16_bf16 v[32:47], v[204:207], a[104:107], v[32:47]\n"
    "ds_read_b128 v[196:199], v2 offset:56320\n"
    "buffer_load_dwordx4 a[152:155], v1, s[16:19], s15 offen offset:2048 sc1\n"
    "s_waitcnt lgkmcnt(4)\n"
    "v_mfma_f32_32x32x16_bf16 v[48:63], v[168:171], a[104:107], v[48:63]\n"
    "ds_read_b128 v[200:203], v2 offset:57344\n"
    "s_waitcnt vmcnt(11)\n"
    "v_mfma_f32_32x32x16_bf16 v[16:31], v[172:175], a[108:111], v[16:31]\n"
    "ds_read_b128 v[204:207], v2 offset:58368\n"
    "v_mfma_f32_32x32x16_bf16 v[32:47], v[176:179], a[108:111], v[32:47]\n"
    "ds_read_b128 v[168:171], v2 offset:59392\n"
    "buffer_load_dwordx4 a[156:159], v1, s[16:19], s15 offen offset:3072 sc1\n"
    "v_mfma_f32_32x32x16_bf16 v[48:63], v[180:183], a[108:111], v[48:63]\n"
    "ds_read_b128 v[172:175], v2 offset:60416\n"
    "s_lshl_b32 s12, s27, 12\n"
    "s_add_u32 s12, s12, s41\n"
    "s_add_u32 s12, s12, 21123072\n"
    "buffer_load_dwordx2 v[136:137], v7, s[16:19], s12 offen offset:0 sc1\n"
    "buffer_load_dwordx2 v[138:139], v7, s[16:19], s12 offen offset:256 sc1\n"
    "buffer_load_dwordx2 v[140:141], v7, s[16:19], s12 offen offset:512 sc1\n"
    "buffer_load_dwordx2 v[142:143], v7, s[16:19], s12 offen offset:768 sc1\n"
    "buffer_load_dwordx2 v[144:145], v7, s[16:19], s12 offen offset:1024 sc1\n"
    "buffer_load_dwordx2 v[146:147], v7, s[16:19], s12 offen offset:1280 sc1\n"
    "buffer_load_dwordx2 v[148:149], v7, s[16:19], s12 offen offset:1536 sc1\n"
    "buffer_load_dwordx2 v[150:151], v7, s[16:19], s12 offen offset:1792 sc1\n"
    "s_waitcnt vmcnt(19) lgkmcnt(4)\n"
    "v_mfma_f32_32x32x16_bf16 v[16:31], v[184:187], a[112:115], v[16:31]\n"
    "ds_read_b128 v[176:179], v2 offset:61440\n"
    "v_mfma_f32_32x32x16_bf16 v[32:47], v[188:191], a[112:115], v[32:47]\n"
    "ds_read_b128 v[180:183], v2 offset:62464\n"
    "v_mfma_f32_32x32x16_bf16 v[48:63], v[192:195], a[112:115], v[48:63]\n"
    "ds_read_b128 v[184:187], v2 offset:63488\n"
    "s_add_u32 m0, s29, 16384\n"
    "s_add_u32 s14, s37, 131072\n"
    "buffer_load_dwordx4 v1, s[16:19], s14 offen offset:0 sc1 lds\n"
    "s_waitcnt vmcnt(19)\n"
    "v_mfma_f32_32x32x16_bf16 v[16:31], v[196:199], a[116:119], v[16:31]\n"
    "ds_read_b128 v[188:191], v2 offset:64512\n"
    "s_waitcnt lgkmcnt(4)\n"
    "v_mfma_f32_32x32x16_bf16 v[32:47], v[200:203], a[116:119], v[32:47]\n"
    "ds_read_b128 v[192:195], v3 offset:0\n"
    "buffer_load_dwordx4 v1, s[16:19], s14 offen offset:1024 sc1 lds\n"
    "v_mfma_f32_32x32x16_bf16 v[48:63], v[204:207], a[116:119], v[48:63]\n"
    "ds_read_b128 v[196:199], v3 offset:1024\n"
    "s_waitcnt vmcnt(19)\n"
    "v_mfma_f32_32x32x16_bf16 v[16:31], v[168:171], a[120:123], v[16:31]\n"
    "ds_read_b128 v[200:203], v3 offset:2048\n"
    "buffer_load_dwordx4 v1, s[16:19], s14 offen offset:2048 sc1 lds\n"
    "v_mfma_f32_32x32x16_bf16 v[32:47], v[172:175], a[120:123], v[32:47]\n"
    "ds_read_b128 v[204:207], v3 offset:3072\n"
    "s_waitcnt lgkmcnt(4)\n"
    "v_mfma_f32_32x32x16_bf16 v[48:63], v[176:179], a[120:123], v[48:63]\n"
    "ds_read_b128 v[168:171], v3 offset:4096\n"
    "buffer_load_dwordx4 v1, s[16:19], s14 offen offset:3072 sc1 lds\n"
    "s_waitcnt vmcnt(20)\n"
    "v_mfma_f32_32x32x16_bf16 v[16:31], v[180:183], a[124:127], v[16:31]\n"
    "ds_read_b128 v[172:175], v3 offset:5120\n"
    "v_mfma_f32_32x32x16_bf16 v[32:47], v[184:187], a[124:127], v[32:47]\n"
    "ds_read_b128 v[176:179], v3 offset:6144\n"
    "s_add_u32 m0, s29, 32768\n"
    "s_add_u32 s14, s37, 147456\n"
    "buffer_load_dwordx4 v1, s[16:19], s14 offen offset:0 sc1 lds\n"
    "v_mfma_f32_32x32x16_bf16 v[48:63], v[188:191], a[124:127], v[48:63]\n"
    "ds_read_b128 v[180:183], v3 offset:7168\n"
    "s_waitcnt vmcnt(20) lgkmcnt(4)\n"
    "v_mfma_f32_32x32x16_bf16 v[16:31], v[192:195], a[128:131], v[16:31]\n"
    "ds_read_b128 v[184:187], v3 offset:8192\n"
    "buffer_load_dwordx4 v1, s[16:19], s14 offen offset:1024 sc1 lds\n"
    "v_mfma_f32_32x32x16_bf16 v[32:47], v[196:199], a[128:131], v[32:47]\n"
    "ds_read_b128 v[188:191], v3 offset:9216\n"
    "v_mfma_f32_32x32x16_bf16 v[48:63], v[200:203], a[128:131], v[48:63]\n"
    "ds_read_b128 v[192:195], v3 offset:10240\n"
    "buffer_load_dwordx4 v1, s[16:19], s14 offen offset:2048 sc1 lds\n"
    "ds_read_b128 v[104:107], v10 offset:0\n"
    "ds_read_b128 v[108:111], v10 offset:16\n"
    "ds_read_b128 v[112:115], v10 offset:32\n"
    "s_waitcnt vmcnt(21)\n"
    "v_mfma_f32_32x32x16_bf16 v[16:31], v[204:207], a[132:135], v[16:31]\n"
    "ds_read_b128 v[196:199], v3 offset:11264\n"
    "ds_read_b128 v[116:119], v10 offset:48\n"
    "ds_read_b128 v[120:123], v10 offset:128\n"
    "ds_read_b128 v[124:127], v10 offset:144\n"
    "ds_read_b128 v[128:131], v10 offset:160\n"
    "s_waitcnt lgkmcnt(11)\n"
    "v_mfma_f32_32x32x16_bf16 v[32:47], v[168:171], a[132:135], v[32:47]\n"
    "ds_read_b128 v[200:203], v3 offset:12288\n"
    "buffer_load_dwordx4 v1, s[16:19], s14 offen offset:3072 sc1 lds\n"
    "ds_read_b128 v[132:135], v10 offset:176\n"
    "ds_read_b128 v[80:83], v10 offset:256\n"
    "ds_read_b128 v[84:87], v10 offset:272\n"
    "v_mfma_f32_32x32x16_bf16 v[48:63], v[172:175], a[132:135], v[48:63]\n"
    "ds_read_b128 v[204:207], v3 offset:13312\n"
    "ds_read_b128 v[88:91], v10 offset:288\n"
    "ds_read_b128 v[92:95], v10 offset:304\n"
    "s_waitcnt vmcnt(21)\n"
    "v_mfma_f32_32x32x16_bf16 v[16:31], v[176:179], a[136:139], v[16:31]\n"
    "ds_read_b128 v[168:171], v3 offset:14336\n"
    "s_mul_i32 s51, s26, 6144\n"
    "v_add_u32 v160, s51, v3\n"
    "s_lshl_b32 s39, s24, 4\n"
    "s_add_u32 s39, s39, s25\n"
    "s_mul_i32 s39, s39, 24576\n"
    "s_add_u32 s39, s39, s51\n"
    "s_add_u32 s39, s39, s44\n"
    "s_add_u32 s39, s39, 41570304\n"
    "s_add_u32 s13, s39, 4096\n"
    "v_mfma_f32_32x32x16_bf16 v[32:47], v[180:183], a[136:139], v[32:47]\n"
    "ds_read_b128 v[172:175], v3 offset:15360\n"
    "s_waitcnt vmcnt(8)\n"
    "v_add_f32 v136, v136, v138\n"
    "v_add_f32 v137, v137, v139\n"
    "v_add_f32 v136, v136, v140\n"
    "s_waitcnt lgkmcnt(13)\n"
    "v_mfma_f32_32x32x16_bf16 v[48:63], v[184:187], a[136:139], v[48:63]\n"
    "ds_read_b128 v[176:179], v1 offset:0\n"
    "v_add_f32 v137, v137, v141\n"
    "v_add_f32 v136, v136, v142\n"
    "v_add_f32 v137, v137, v143\n"
    "v_add_f32 v136, v136, v144\n"
    "v_add_f32 v137, v137, v145\n"
    "v_mfma_f32_32x32x16_bf16 v[16:31], v[188:191], a[140:143], v[16:31]\n"
    "ds_read_b128 v[180:183], v1 offset:1024\n"
    "v_add_f32 v136, v136, v146\n"
    "v_add_f32 v137, v137, v147\n"
    "v_add_f32 v136, v136, v148\n"
    "v_add_f32 v137, v137, v149\n"
    "v_add_f32 v136, v136, v150\n"
    "v_mfma_f32_32x32x16_bf16 v[32:47], v[192:195], a[140:143], v[32:47]\n"
    "ds_read_b128 v[184:187], v1 offset:2048\n"
    "v_add_f32 v137, v137, v151\n"
    "v_mov_b32 v138, v136\n"
    "v_mov_b32 v139, v137\n"
    "s_nop 0\n"
    "v_permlane32_swap_b32 v136, v138\n"
    "v_permlane32_swap_b32 v137, v139\n"
    "v_mfma_f32_32x32x16_bf16 v[48:63], v[196:199], a[140:143], v[48:63]\n"
    "ds_read_b128 v[188:191], v1 offset:3072\n"
    "v_add_f32 v136, v136, v138\n"
    "v_add_f32 v137, v137, v139\n"
    "v_mul_f32 v152, 0x3acccccd, v136\n"
    "v_mul_f32 v137, 0x3acccccd, v137\n"
    "s_waitcnt lgkmcnt(4)\n"
    "v_mfma_f32_32x32x16_bf16 v[16:31], v[200:203], a[144:147], v[16:31]\n"
    "ds_read_b128 v[192:195], v1 offset:4096\n"
    "v_fma_f32 v153, -v152, v152, v137\n"
    "v_max_f32 v153, 0, v153\n"
    "v_add_f32 v153, 0x3727c5ac, v153\n"
    "v_rsq_f32 v153, v153\n"
    "v_mfma_f32_32x32x16_bf16 v[32:47], v[204:207], a[144:147], v[32:47]\n"
    "ds_read_b128 v[196:199], v1 offset:5120\n"
    "v_sub_f32 v152, 0, v152\n"
    "v_mul_f32 v154, 0x3ea52b5c, v153\n"
    "v_mfma_f32_32x32x16_bf16 v[48:63], v[168:171], a[144:147], v[48:63]\n"
    "ds_read_b128 v[200:203], v1 offset:6144\n"
    "v_mfma_f32_32x32x16_bf16 v[16:31], v[172:175], a[148:151], v[16:31]\n"
    "ds_read_b128 v[204:207], v1 offset:7168\n"
    "s_waitcnt lgkmcnt(4)\n"
    "v_mfma_f32_32x32x16_bf16 v[32:47], v[176:179], a[148:151], v[32:47]\n"
    "ds_read_b128 v[168:171], v1 offset:8192\n"
    "v_mfma_f32_32x32x16_bf16 v[48:63], v[180:183], a[148:151], v[48:63]\n"
    "ds_read_b128 v[172:175], v1 offset:9216\n"
    "v_mfma_f32_32x32x16_bf16 v[16:31], v[184:187], a[152:155], v[16:31]\n"
    "ds_read_b128 v[176:179], v1 offset:10240\n"
    "v_mfma_f32_32x32x16_bf16 v[32:47], v[188:191], a[152:155], v[32:47]\n"
    "ds_read_b128 v[180:183], v1 offset:11264\n"
    "s_waitcnt lgkmcnt(4)\n"
    "v_mfma_f32_32x32x16_bf16 v[48:63], v[192:195], a[152:155], v[48:63]\n"
    "ds_read_b128 v[184:187], v1 offset:12288\n"
    "v_mfma_f32_32x32x16_bf16 v[16:31], v[196:199], a[156:159], v[16:31]\n"
    "ds_read_b128 v[188:191], v1 offset:13312\n"
    "v_mfma_f32_32x32x16_bf16 v[32:47], v[200:203], a[156:159], v[32:47]\n"
    "ds_read_b128 v[192:195], v1 offset:14336\n"
    "v_mfma_f32_32x32x16_bf16 v[48:63], v[204:207], a[156:159], v[48:63]\n"
    "ds_read_b128 v[196:199], v1 offset:15360\n"
    "s_barrier\n"
    "s_nop 11\n"
    "v_pk_fma_f32 v[16:17], v[104:105], v[152:153], v[16:17] op_sel_hi:[1,0,1]\n"
    "v_pk_fma_f32 v[18:19], v[106:107], v[152:153], v[18:19] op_sel_hi:[1,0,1]\n"
    "v_pk_fma_f32 v[20:21], v[108:109], v[152:153], v[20:21] op_sel_hi:[1,0,1]\n"
    "v_pk_fma_f32 v[22:23], v[110:111], v[152:153], v[22:23] op_sel_hi:[1,0,1]\n"
    "v_pk_fma_f32 v[24:25], v[112:113], v[152:153], v[24:25] op_sel_hi:[1,0,1]\n"
    "v_pk_fma_f32 v[26:27], v[114:115], v[152:153], v[26:27] op_sel_hi:[1,0,1]\n"
    "v_pk_fma_f32 v[28:29], v[116:117], v[152:153], v[28:29] op_sel_hi:[1,0,1]\n"
    "v_pk_fma_f32 v[30:31], v[118:119], v[152:153], v[30:31] op_sel_hi:[1,0,1]\n"
    "v_pk_mul_f32 v[16:17], v[16:17], v[152:153] op_sel:[0,1] op_sel_hi:[1,1]\n"
    "v_pk_mul_f32 v[18:19], v[18:19], v[152:153] op_sel:[0,1] op_sel_hi:[1,1]\n"
    "v_pk_mul_f32 v[20:21], v[20:21], v[152:153] op_sel:[0,1] op_sel_hi:[1,1]\n"
    "v_pk_mul_f32 v[22:23], v[22:23], v[152:153] op_sel:[0,1] op_sel_hi:[1,1]\n"
    "v_pk_mul_f32 v[24:25], v[24:25], v[152:153] op_sel:[0,1] op_sel_hi:[1,1]\n"
    "v_pk_mul_f32 v[26:27], v[26:27], v[152:153] op_sel:[0,1] op_sel_hi:[1,1]\n"
    "v_pk_mul_f32 v[28:29], v[28:29], v[152:153] op_sel:[0,1] op_sel_hi:[1,1]\n"
    "v_pk_mul_f32 v[30:31], v[30:31], v[152:153] op_sel:[0,1] op_sel_hi:[1,1]\n"
    "v_cvt_pk_bf16_f32 v104, v16, v17\n"
    "v_cvt_pk_bf16_f32 v105, v18, v19\n"
    "v_cvt_pk_bf16_f32 v106, v20, v21\n"
    "v_cvt_pk_bf16_f32 v107, v22, v23\n"
    "v_cvt_pk_bf16_f32 v108, v24, v25\n"
    "v_cvt_pk_bf16_f32 v109, v26, v27\n"
    "v_cvt_pk_bf16_f32 v110, v28, v29\n"
    "v_cvt_pk_bf16_f32 v111, v30, v31\n"
    "ds_write_b128 v12, v[104:107] offset:0\n"
    "ds_write_b128 v12, v[108:111] offset:3072\n"
    "v_pk_fma_f32 v[32:33], v[120:121], v[152:153], v[32:33] op_sel_hi:[1,0,1]\n"
    "v_pk_fma_f32 v[34:35], v[122:123], v[152:153], v[34:35] op_sel_hi:[1,0,1]\n"
    "v_pk_fma_f32 v[36:37], v[124:125], v[152:153], v[36:37] op_sel_hi:[1,0,1]\n"
    "v_pk_fma_f32 v[38:39], v[126:127], v[152:153], v[38:39] op_sel_hi:[1,0,1]\n"
    "v_pk_fma_f32 v[40:41], v[128:129], v[152:153], v[40:41] op_sel_hi:[1,0,1]\n"
    "v_pk_fma_f32 v[42:43], v[130:131], v[152:153], v[42:43] op_sel_hi:[1,0,1]\n"
    "v_pk_fma_f32 v[44:45], v[132:133], v[152:153], v[44:45] op_sel_hi:[1,0,1]\n"
    "v_pk_fma_f32 v[46:47], v[134:135], v[152:153], v[46:47] op_sel_hi:[1,0,1]\n"
    "v_pk_mul_f32 v[32:33], v[32:33], v[152:153] op_sel:[0,1] op_sel_hi:[1,1]\n"
    "v_pk_mul_f32 v[34:35], v[34:35], v[152:153] op_sel:[0,1] op_sel_hi:[1,1]\n"
    "v_pk_mul_f32 v[36:37], v[36:37], v[152:153] op_sel:[0,1] op_sel_hi:[1,1]\n"
    "v_pk_mul_f32 v[38:39], v[38:39], v[152:153] op_sel:[0,1] op_sel_hi:[1,1]\n"
    "v_pk_mul_f32 v[40:41], v[40:41], v[152:153] op_sel:[0,1] op_sel_hi:[1,1]\n"
    "v_pk_mul_f32 v[42:43], v[42:43], v[152:153] op_sel:[0,1] op_sel_hi:[1,1]\n"
    "v_pk_mul_f32 v[44:45], v[44:45], v[152:153] op_sel:[0,1] op_sel_hi:[1,1]\n"
    "v_pk_mul_f32 v[46:47], v[46:47], v[152:153] op_sel:[0,1] op_sel_hi:[1,1]\n"
    "v_cvt_pk_bf16_f32 v120, v32, v33\n"
    "v_cvt_pk_bf16_f32 v121, v34, v35\n"
    "v_cvt_pk_bf16_f32 v122, v36, v37\n"
    "v_cvt_pk_bf16_f32 v123, v38, v39\n"
    "v_cvt_pk_bf16_f32 v124, v40, v41\n"
    "v_cvt_pk_bf16_f32 v125, v42, v43\n"
    "v_cvt_pk_bf16_f32 v126, v44, v45\n"
    "v_cvt_pk_bf16_f32 v127, v46, v47\n"
    "ds_write_b128 v12, v[120:123] offset:1536\n"
    "ds_write_b128 v12, v[124:127] offset:4608\n"
    "v_pk_fma_f32 v[48:49], v[80:81], v[152:153], v[48:49] op_sel_hi:[1,0,1]\n"
    "v_pk_fma_f32 v[50:51], v[82:83], v[152:153], v[50:51] op_sel_hi:[1,0,1]\n"
    "v_pk_fma_f32 v[52:53], v[84:85], v[152:153], v[52:53] op_sel_hi:[1,0,1]\n"
    "v_pk_fma_f32 v[54:55], v[86:87], v[152:153], v[54:55] op_sel_hi:[1,0,1]\n"
    "v_pk_mul_f32 v[48:49], v[48:49], v[152:153] op_sel:[0,1] op_sel_hi:[1,1]\n"
    "v_pk_mul_f32 v[50:51], v[50:51], v[152:153] op_sel:[0,1] op_sel_hi:[1,1]\n"
    "v_pk_mul_f32 v[52:53], v[52:53], v[152:153] op_sel:[0,1] op_sel_hi:[1,1]\n"
    "v_pk_mul_f32 v[54:55], v[54:55], v[152:153] op_sel:[0,1] op_sel_hi:[1,1]\n"
    "v_cvt_pk_bf16_f32 v208, v48, v49\n"
    "v_cvt_pk_bf16_f32 v209, v50, v51\n"
    "v_cvt_pk_bf16_f32 v212, v52, v53\n"
    "v_cvt_pk_bf16_f32 v213, v54, v55\n"
    "v_mov_b32 v214, 0x00003f80\n"
    "v_mov_b32 v215, 0\n"
    "v_mov_b32 v211, 0\n"
    "ds_write_b128 v13, v[212:215] offset:1536\n"
    "v_dot2_f32_bf16 v156, v104, v104, 0\n"
    "v_dot2_f32_bf16 v157, v108, v108, 0\n"
    "v_dot2_f32_bf16 v158, v208, v208, 0\n"
    "v_dot2_f32_bf16 v156, v105, v105, v156\n"
    "v_dot2_f32_bf16 v157, v109, v109, v157\n"
    "v_dot2_f32_bf16 v158, v209, v209, v158\n"
    "v_dot2_f32_bf16 v156, v106, v106, v156\n"
    "v_dot2_f32_bf16 v157, v110, v110, v157\n"
    "v_dot2_f32_bf16 v156, v107, v107, v156\n"
    "v_dot2_f32_bf16 v157, v111, v111, v157\n"
    "v_pk_fma_f32 v[56:57], v[88:89], v[152:153], v[56:57] op_sel_hi:[1,0,1]\n"
    "v_pk_fma_f32 v[58:59], v[90:91], v[152:153], v[58:59] op_sel_hi:[1,0,1]\n"
    "v_pk_fma_f32 v[60:61], v[92:93], v[152:153], v[60:61] op_sel_hi:[1,0,1]\n"
    "v_pk_fma_f32 v[62:63], v[94:95], v[152:153], v[62:63] op_sel_hi:[1,0,1]\n"
    "v_pk_mul_f32 v[56:57], v[56:57], v[154:155] op_sel_hi:[1,0]\n"
    "v_pk_mul_f32 v[58:59], v[58:59], v[154:155] op_sel_hi:[1,0]\n"
    "v_pk_mul_f32 v[60:61], v[60:61], v[154:155] op_sel_hi:[1,0]\n"
    "v_pk_mul_f32 v[62:63], v[62:63], v[154:155] op_sel_hi:[1,0]\n"
    "v_permlane32_swap_b32 v156, v157\n"
    "v_cvt_pk_bf16_f32 v228, v56, v57\n"
    "v_cvt_pk_bf16_f32 v229, v58, v59\n"
    "v_cvt_pk_bf16_f32 v236, v60, v61\n"
    "v_cvt_pk_bf16_f32 v237, v62, v63\n"
    "v_add_f32 v156, v156, v157\n"
    "v_add_f32 v156, v156, v158\n"
    "v_min_f32 v156, 0x7f000000, v156\n"
    "v_add_u32 v156, 0xffff, v156\n"
    "v_and_b32 v210, 0xffff0000, v156\n"
    "ds_write_b128 v13, v[208:211] offset:0\n"
    "s_waitcnt lgkmcnt(0)\n"
    "ds_read_b128 v[16:19], v160 offset:4096\n"
    "ds_read_b128 v[20:23], v160 offset:5120\n"
    "ds_read_b128 v[24:27], v160 offset:6144\n"
    "ds_read_b128 v[28:31], v160 offset:7168\n"
    "ds_read_b128 v[32:35], v160 offset:8192\n"
    "ds_read_b128 v[36:39], v160 offset:9216\n"
    "s_waitcnt lgkmcnt(5)\n"
    "buffer_store_dwordx4 v[16:19], v1, s[16:19], s39 offen offset:0 sc1\n"
    "s_waitcnt lgkmcnt(4)\n"
    "buffer_store_dwordx4 v[20:23], v1, s[16:19], s39 offen offset:1024 sc1\n"
    "s_waitcnt lgkmcnt(3)\n"
    "buffer_store_dwordx4 v[24:27], v1, s[16:19], s39 offen offset:2048 sc1\n"
    "s_waitcnt lgkmcnt(2)\n"
    "buffer_store_dwordx4 v[28:31], v1, s[16:19], s39 offen offset:3072 sc1\n"
    "s_waitcnt lgkmcnt(1)\n"
    "buffer_store_dwordx4 v[32:35], v1, s[16:19], s13 offen offset:0 sc1\n"
    "s_waitcnt lgkmcnt(0)\n"
    "buffer_store_dwordx4 v[36:39], v1, s[16:19], s13 offen offset:1024 sc1\n"
    "v_mov_b32 v230, 0\n"
    "v_mov_b32 v231, 0\n"
    "v_mov_b32 v238, 0\n"
    "v_mov_b32 v239, 0\n"
    "v_mfma_f32_32x32x16_bf16 v[64:79], v[168:171], a[0:3], 0\n"
    "s_waitcnt vmcnt(10)\n"
    "s_barrier\n"
    "ds_read_b128 v[200:203], v1 offset:16384\n"
    "ds_read_b128 v[104:107], v10 offset:384\n"
    "ds_read_b128 v[108:111], v10 offset:400\n"
    "ds_read_b128 v[112:115], v10 offset:416\n"
    "v_mfma_f32_32x32x16_bf16 v[64:79], v[172:175], a[4:7], v[64:79]\n"
    "ds_read_b128 v[204:207], v1 offset:17408\n"
    "ds_read_b128 v[116:119], v10 offset:432\n"
    "v_mfma_f32_32x32x16_bf16 v[64:79], v[176:179], a[8:11], v[64:79]\n"
    "ds_read_b128 v[168:171], v1 offset:18432\n"
    "v_mfma_f32_32x32x16_bf16 v[64:79], v[180:183], a[12:15], v[64:79]\n"
    "ds_read_b128 v[172:175], v1 offset:19456\n"
    "v_mfma_f32_32x32x16_bf16 v[64:79], v[184:187], a[16:19], v[64:79]\n"
    "ds_read_b128 v[176:179], v1 offset:20480\n"
    "v_mfma_f32_32x32x16_bf16 v[64:79], v[188:191], a[20:23], v[64:79]\n"
    "ds_read_b128 v[180:183], v1 offset:21504\n"
    "v_mfma_f32_32x32x16_bf16 v[64:79], v[192:195], a[24:27], v[64:79]\n"
    "ds_read_b128 v[184:187], v1 offset:22528\n"
    "v_mfma_f32_32x32x16_bf16 v[64:79], v[196:199], a[28:31], v[64:79]\n"
    "ds_read_b128 v[188:191], v1 offset:23552\n"
    "s_waitcnt lgkmcnt(4)\n"
    "v_mfma_f32_32x32x16_bf16 v[64:79], v[200:203], a[32:35], v[64:79]\n"
    "ds_read_b128 v[192:195], v1 offset:24576\n"
    "v_mfma_f32_32x32x16_bf16 v[64:79], v[204:207], a[36:39], v[64:79]\n"
    "ds_read_b128 v[196:199], v1 offset:25600\n"
    "v_mfma_f32_32x32x16_bf16 v[64:79], v[168:171], a[40:43], v[64:79]\n"
    "ds_read_b128 v[200:203], v1 offset:26624\n"
    "v_mfma_f32_32x32x16_bf16 v[64:79], v[172:175], a[44:47], v[64:79]\n"
    "ds_read_b128 v[204:207], v1 offset:27648\n"
    "s_waitcnt lgkmcnt(4)\n"
    "v_mfma_f32_32x32x16_bf16 v[64:79], v[176:179], a[48:51], v[64:79]\n"
    "ds_read_b128 v[168:171], v1 offset:28672\n"
    "v_mfma_f32_32x32x16_bf16 v[64:79], v[180:183], a[52:55], v[64:79]\n"
    "ds_read_b128 v[172:175], v1 offset:29696\n"
    "v_mfma_f32_32x32x16_bf16 v[64:79], v[184:187], a[56:59], v[64:79]\n"
    "ds_read_b128 v[176:179], v1 offset:30720\n"
    "v_mfma_f32_32x32x16_bf16 v[64:79], v[188:191], a[60:63], v[64:79]\n"
    "ds_read_b128 v[180:183], v1 offset:31744\n"
    "s_waitcnt lgkmcnt(4)\n"
    "v_mfma_f32_32x32x16_bf16 v[64:79], v[192:195], a[64:67], v[64:79]\n"
    "s_waitcnt vmcnt(6)\n"
    "s_barrier\n"
    "ds_read_b128 v[184:187], v1 offset:32768\n"
    "v_mfma_f32_32x32x16_bf16 v[64:79], v[196:199], a[68:71], v[64:79]\n"
    "ds_read_b128 v[188:191], v1 offset:33792\n"
    "v_mfma_f32_32x32x16_bf16 v[64:79], v[200:203], a[72:75], v[64:79]\n"
    "ds_read_b128 v[192:195], v1 offset:34816\n"
    "v_mfma_f32_32x32x16_bf16 v[64:79], v[204:207], a[76:79], v[64:79]\n"
    "ds_read_b128 v[196:199], v1 offset:35840\n"
    "s_waitcnt lgkmcnt(4)\n"
    "v_mfma_f32_32x32x16_bf16 v[64:79], v[168:171], a[80:83], v[64:79]\n"
    "ds_read_b128 v[200:203], v1 offset:36864\n"
    "v_mfma_f32_32x32x16_bf16 v[64:79], v[172:175], a[84:87], v[64:79]\n"
    "ds_read_b128 v[204:207], v1 offset:37888\n"
    "v_mfma_f32_32x32x16_bf16 v[64:79], v[176:179], a[88:91], v[64:79]\n"
    "ds_read_b128 v[168:171], v1 offset:38912\n"
    "s_waitcnt vmcnt(0)\n"
    "s_barrier\n"
    "s_cmp_lg_u32 s26, 0\n"
    "s_cbranch_scc1 Lpub_23_%=\n"
    "s_lshl_b32 s51, s24, 4\n"
    "s_add_u32 s51, s51, s25\n"
    "s_lshl_b32 s51, s51, 7\n"
    "s_add_u32 s51, s51, s46\n"
    "s_add_u32 s51, s51, 131072\n"
    "v_mov_b32 v156, 0\n"
    "v_mov_b32 v157, 1\n"
    "s_mov_b64 exec, 1\n"
    "buffer_atomic_add v157, v156, s[16:19], s51 offen\n"
    "s_mov_b64 exec, -1\n"
    "Lpub_23_%=:\n"
    "v_mfma_f32_32x32x16_bf16 v[64:79], v[180:183], a[92:95], v[64:79]\n"
    "ds_read_b128 v[172:175], v1 offset:39936\n"
    "s_waitcnt lgkmcnt(4)\n"
    "v_mfma_f32_32x32x16_bf16 v[64:79], v[184:187], a[96:99], v[64:79]\n"
    "ds_read_b128 v[176:179], v1 offset:40960\n"
    "v_mfma_f32_32x32x16_bf16 v[64:79], v[188:191], a[100:103], v[64:79]\n"
    "ds_read_b128 v[180:183], v1 offset:41984\n"
    "v_mfma_f32_32x32x16_bf16 v[64:79], v[192:195], a[104:107], v[64:79]\n"
    "ds_read_b128 v[184:187], v1 offset:43008\n"
    "v_mfma_f32_32x32x16_bf16 v[64:79], v[196:199], a[108:111], v[64:79]\n"
    "ds_read_b128 v[188:191], v1 offset:44032\n"
    "s_waitcnt lgkmcnt(4)\n"
    "v_mfma_f32_32x32x16_bf16 v[64:79], v[200:203], a[112:115], v[64:79]\n"
    "ds_read_b128 v[192:195], v1 offset:45056\n"
    "v_mfma_f32_32x32x16_bf16 v[64:79], v[204:207], a[116:119], v[64:79]\n"
    "ds_read_b128 v[196:199], v1 offset:46080\n"
    "v_mfma_f32_32x32x16_bf16 v[64:79], v[168:171], a[120:123], v[64:79]\n"
    "ds_read_b128 v[200:203], v1 offset:47104\n"
    "v_mfma_f32_32x32x16_bf16 v[64:79], v[172:175], a[124:127], v[64:79]\n"
    "ds_read_b128 v[204:207], v1 offset:48128\n"
    "s_waitcnt lgkmcnt(4)\n"
    "v_mfma_f32_32x32x16_bf16 v[64:79], v[176:179], a[128:131], v[64:79]\n"
    "v_mfma_f32_32x32x16_bf16 v[64:79], v[180:183], a[132:135], v[64:79]\n"
    "v_mfma_f32_32x32x16_bf16 v[64:79], v[184:187], a[136:139], v[64:79]\n"
    "v_mfma_f32_32x32x16_bf16 v[64:79], v[188:191], a[140:143], v[64:79]\n"
    "s_waitcnt lgkmcnt(0)\n"
    "v_mfma_f32_32x32x16_bf16 v[64:79], v[192:195], a[144:147], v[64:79]\n"
    "v_mfma_f32_32x32x16_bf16 v[64:79], v[196:199], a[148:151], v[64:79]\n"
    "v_mfma_f32_32x32x16_bf16 v[64:79], v[200:203], a[152:155], v[64:79]\n"
    "v_mfma_f32_32x32x16_bf16 v[64:79], v[204:207], a[156:159], v[64:79]\n"
    "s_cmp_lg_u32 s26, 0\n"
    "s_cbranch_scc1 Lkvi_24_%=\n"
    "s_lshl_b32 s31, s24, 4\n"
    "s_add_u32 s31, s31, s25\n"
    "s_lshl_b32 s35, s31, 7\n"
    "s_add_u32 s35, s35, s46\n"
    "s_add_u32 s35, s35, 131072\n"
    "s_sub_u32 s51, s35, 2048\n"
    "s_cmp_ge_u32 s24, 1\n"
    "s_cselect_b32 s51, s51, s35\n"
    "v_mov_b32 v210, s51\n"
    "s_sub_u32 s51, s35, 4096\n"
    "s_cmp_ge_u32 s24, 2\n"
    "s_cselect_b32 s51, s51, s35\n"
    "v_mov_b32 v211, s51\n"
    "v_cmp_eq_u32 vcc, 0, v6\n"
    "s_nop 1\n"
    "v_cndmask_b32 v212, v211, v210, vcc\n"
    "v_mov_b32 v213, 0\n"
    "s_mov_b64 exec, 3\n"
    "buffer_atomic_add v213, v212, s[16:19], 0 offen sc0\n"
    "s_mov_b64 exec, -1\n"
    "Lkvi_24_%=:\n"
    "v_pk_fma_f32 v[64:65], v[104:105], v[152:153], v[64:65] op_sel_hi:[1,0,1]\n"
    "v_pk_fma_f32 v[66:67], v[106:107], v[152:153], v[66:67] op_sel_hi:[1,0,1]\n"
    "v_pk_fma_f32 v[68:69], v[108:109], v[152:153], v[68:69] op_sel_hi:[1,0,1]\n"
    "v_pk_fma_f32 v[70:71], v[110:111], v[152:153], v[70:71] op_sel_hi:[1,0,1]\n"
    "v_pk_fma_f32 v[72:73], v[112:113], v[152:153], v[72:73] op_sel_hi:[1,0,1]\n"
    "v_pk_fma_f32 v[74:75], v[114:115], v[152:153], v[74:75] op_sel_hi:[1,0,1]\n"
    "v_pk_fma_f32 v[76:77], v[116:117], v[152:153], v[76:77] op_sel_hi:[1,0,1]\n"
    "v_pk_fma_f32 v[78:79], v[118:119], v[152:153], v[78:79] op_sel_hi:[1,0,1]\n"
    "v_pk_mul_f32 v[64:65], v[64:65], v[154:155] op_sel_hi:[1,0]\n"
    "v_pk_mul_f32 v[66:67], v[66:67], v[154:155] op_sel_hi:[1,0]\n"
    "v_pk_mul_f32 v[68:69], v[68:69], v[154:155] op_sel_hi:[1,0]\n"
    "v_pk_mul_f32 v[70:71], v[70:71], v[154:155] op_sel_hi:[1,0]\n"
    "v_pk_mul_f32 v[72:73], v[72:73], v[154:155] op_sel_hi:[1,0]\n"
    "v_pk_mul_f32 v[74:75], v[74:75], v[154:155] op_sel_hi:[1,0]\n"
    "v_pk_mul_f32 v[76:77], v[76:77], v[154:155] op_sel_hi:[1,0]\n"
    "v_pk_mul_f32 v[78:79], v[78:79], v[154:155] op_sel_hi:[1,0]\n"
    "v_cvt_pk_bf16_f32 v224, v64, v65\n"
    "v_cvt_pk_bf16_f32 v225, v66, v67\n"
    "v_cvt_pk_bf16_f32 v226, v68, v69\n"
    "v_cvt_pk_bf16_f32 v227, v70, v71\n"
    "v_cvt_pk_bf16_f32 v232, v72, v73\n"
    "v_cvt_pk_bf16_f32 v233, v74, v75\n"
    "v_cvt_pk_bf16_f32 v234, v76, v77\n"
    "v_cvt_pk_bf16_f32 v235, v78, v79\n"
    "v_dot2_f32_bf16 v220, v224, v224, 0\n"
    "v_dot2_f32_bf16 v220, v225, v225, v220\n"
    "v_dot2_f32_bf16 v220, v226, v226, v220\n"
    "v_dot2_f32_bf16 v220, v227, v227, v220\n"
    "v_dot2_f32_bf16 v220, v228, v228, v220\n"
    "v_dot2_f32_bf16 v220, v229, v229, v220\n"
    "v_dot2_f32_bf16 v221, v232, v232, 0\n"
    "v_dot2_f32_bf16 v221, v233, v233, v221\n"
    "v_dot2_f32_bf16 v221, v234, v234, v221\n"
    "v_dot2_f32_bf16 v221, v235, v235, v221\n"
    "v_dot2_f32_bf16 v221, v236, v236, v221\n"
    "v_dot2_f32_bf16 v221, v237, v237, v221\n"
    "v_mov_b32 v156, v220\n"
    "s_nop 1\n"
    "v_permlane32_swap_b32 v220, v156\n"
    "v_add_f32 v220, v220, v156\n"
    "v_mov_b32 v157, v221\n"
    "s_nop 1\n"
    "v_permlane32_swap_b32 v221, v157\n"
    "v_add_f32 v221, v221, v157\n"
    "s_lshl_b32 s31, s24, 4\n"
    "s_add_u32 s31, s31, s25\n"
    "s_sub_u32 s51, s31, 16\n"
    "s_mul_i32 s51, s51, 24576\n"
    "s_add_u32 s51, s51, s44\n"
    "s_add_u32 s51, s51, 41570304\n"
    "s_mul_i32 s30, s26, 6144\n"
    "s_add_u32 s51, s51, s30\n"
    "s_cmp_ge_u32 s24, 1\n"
    "s_cselect_b32 s90, s51, 196608\n"
    "s_sub_u32 s51, s31, 32\n"
    "s_mul_i32 s51, s51, 24576\n"
    "s_add_u32 s51, s51, s44\n"
    "s_add_u32 s51, s51, 41570304\n"
    "s_mul_i32 s30, s26, 6144\n"
    "s_add_u32 s51, s51, s30\n"
    "s_cmp_ge_u32 s24, 2\n"
    "s_cselect_b32 s91, s51, 196608\n"
    "s_cmp_lg_u32 s26, 0\n"
    "s_cbranch_scc1 Lkvw_25_%=\n"
    "s_mov_b32 s50, 0\n"
    "s_mov_b64 exec, 3\n"
    "Lkvp_26_%=:\n"
    "s_waitcnt vmcnt(0)\n"
    "v_cmp_ne_u32 vcc, 1, v213\n"
    "s_add_u32 s50, s50, 1\n"
    "s_nop 4\n"
    "s_cbranch_vccz Lkvpd_27_%=\n"
    "v_mov_b32 v213, 0\n"
    "buffer_atomic_add v213, v212, s[16:19], 0 offen sc0\n"
    "s_cmp_lt_u32 s50, 20000\n"
    "s_cbranch_scc1 Lkvp_26_%=\n"
    "s_waitcnt vmcnt(0)\n"
    "Lkvpd_27_%=:\n"
    "s_mov_b64 exec, -1\n"
    "Lkvw_25_%=:\n"
    "s_barrier\n"
    "s_mul_i32 s30, s26, 6144\n"
    "v_mov_b32 v208, 0\n"
    "v_mov_b32 v209, 0\n"
    "v_mov_b32 v210, 0\n"
    "v_mov_b32 v211, 0\n"
    "s_cmp_lt_u32 s24, 1\n"
    "s_cbranch_scc1 Lkz_28_%=\n"
    "s_add_u32 m0, s30, 110592\n"
    "s_add_u32 s14, s90, 0\n"
    "buffer_load_dwordx4 v1, s[16:19], s14 offen offset:0 sc1 lds\n"
    "buffer_load_dwordx4 v1, s[16:19], s14 offen offset:1024 sc1 lds\n"
    "buffer_load_dwordx4 v1, s[16:19], s14 offen offset:2048 sc1 lds\n"
    "buffer_load_dwordx4 v1, s[16:19], s14 offen offset:3072 sc1 lds\n"
    "s_add_u32 m0, s30, 114688\n"
    "s_add_u32 s14, s90, 4096\n"
    "buffer_load_dwordx4 v1, s[16:19], s14 offen offset:0 sc1 lds\n"
    "buffer_load_dwordx4 v1, s[16:19], s14 offen offset:1024 sc1 lds\n"
    "s_branch Lkj_29_%=\n"
    "Lkz_28_%=:\n"
    "v_add_u32 v212, s30, v2\n"
    "ds_write_b128 v212, v[208:211] offset:45056\n"
    "ds_write_b128 v212, v[208:211] offset:46080\n"
    "ds_write_b128 v212, v[208:211] offset:47104\n"
    "ds_write_b128 v212, v[208:211] offset:48128\n"
    "ds_write_b128 v212, v[208:211] offset:49152\n"
    "ds_write_b128 v212, v[208:211] offset:50176\n"
    "s_waitcnt lgkmcnt(0)\n"
    "Lkj_29_%=:\n"
    "s_cmp_lt_u32 s24, 2\n"
    "s_cbranch_scc1 Lkz_30_%=\n"
    "s_add_u32 m0, s30, 86016\n"
    "s_add_u32 s14, s91, 0\n"
    "buffer_load_dwordx4 v1, s[16:19], s14 offen offset:0 sc1 lds\n"
    "buffer_load_dwordx4 v1, s[16:19], s14 offen offset:1024 sc1 lds\n"
    "buffer_load_dwordx4 v1, s[16:19], s14 offen offset:2048 sc1 lds\n"
    "buffer_load_dwordx4 v1, s[16:19], s14 offen offset:3072 sc1 lds\n"
    "s_add_u32 m0, s30, 90112\n"
    "s_add_u32 s14, s91, 4096\n"
    "buffer_load_dwordx4 v1, s[16:19], s14 offen offset:0 sc1 lds\n"
    "buffer_load_dwordx4 v1, s[16:19], s14 offen offset:1024 sc1 lds\n"
    "s_branch Lkj_31_%=\n"
    "Lkz_30_%=:\n"
    "v_add_u32 v212, s30, v2\n"
    "ds_write_b128 v212, v[208:211] offset:20480\n"
    "ds_write_b128 v212, v[208:211] offset:21504\n"
    "ds_write_b128 v212, v[208:211] offset:22528\n"
    "ds_write_b128 v212, v[208:211] offset:23552\n"
    "ds_write_b128 v212, v[208:211] offset:24576\n"
    "ds_write_b128 v212, v[208:211] offset:25600\n"
    "s_waitcnt lgkmcnt(0)\n"
    "Lkj_31_%=:\n"
    "s_or_b32 s51, s26, s28\n"
    "s_cmp_lg_u32 s51, 0\n"
    "s_cbranch_scc1 Ls2_32_%=\n"
    "s_mov_b32 s50, 0\n"
    "v_lshlrev_b32 v16, 7, v6\n"
    "v_add_u32 v16, 54185984, v16\n"
    "v_lshlrev_b32 v17, 7, v6\n"
    "v_add_u32 v17, 54194176, v17\n"
    "v_lshlrev_b32 v18, 7, v6\n"
    "v_add_u32 v18, 54202368, v18\n"
    "v_lshlrev_b32 v19, 7, v6\n"
    "v_add_u32 v19, 54210560, v19\n"
    "Lpa_33_%=:\n"
    "buffer_load_dwordx2 v[20:21], v16, s[16:19], 0 offen sc1\n"
    "buffer_load_dwordx2 v[22:23], v17, s[16:19], 0 offen sc1\n"
    "buffer_load_dwordx2 v[24:25], v18, s[16:19], 0 offen sc1\n"
    "buffer_load_dwordx2 v[26:27], v19, s[16:19], 0 offen sc1\n"
    "s_waitcnt vmcnt(0)\n"
    "s_mov_b64 s[90:91], 0\n"
    "v_cmp_ne_u32 vcc, s47, v20\n"
    "s_nop 1\n"
    "s_or_b64 s[90:91], s[90:91], vcc\n"
    "v_cmp_ne_u32 vcc, s97, v21\n"
    "s_nop 1\n"
    "s_or_b64 s[90:91], s[90:91], vcc\n"
    "v_cmp_ne_u32 vcc, s47, v22\n"
    "s_nop 1\n"
    "s_or_b64 s[90:91], s[90:91], vcc\n"
    "v_cmp_ne_u32 vcc, s97, v23\n"
    "s_nop 1\n"
    "s_or_b64 s[90:91], s[90:91], vcc\n"
    "v_cmp_ne_u32 vcc, s47, v24\n"
    "s_nop 1\n"
    "s_or_b64 s[90:91], s[90:91], vcc\n"
    "v_cmp_ne_u32 vcc, s97, v25\n"
    "s_nop 1\n"
    "s_or_b64 s[90:91], s[90:91], vcc\n"
    "v_cmp_ne_u32 vcc, s47, v26\n"
    "s_nop 1\n"
    "s_or_b64 s[90:91], s[90:91], vcc\n"
    "v_cmp_ne_u32 vcc, s97, v27\n"
    "s_nop 1\n"
    "s_or_b64 s[90:91], s[90:91], vcc\n"
    "s_add_u32 s50, s50, 1\n"
    "s_cmp_eq_u64 s[90:91], 0\n"
    "s_cbranch_scc1 Lpad_34_%=\n"
    "s_cmp_lt_u32 s50, 20000\n"
    "s_cbranch_scc1 Lpa_33_%=\n"
    "Lpad_34_%=:\n"
    "s_or_b32 s51, s24, s25\n"
    "s_cmp_lg_u32 s51, 0\n"
    "s_cbranch_scc1 Leu_35_%=\n"
    "s_add_u32 s51, s47, 1\n"
    "v_mov_b32 v210, s51\n"
    "v_mov_b32 v211, s97\n"
    "v_mov_b32 v208, 54218752\n"
    "s_mov_b64 exec, 1\n"
    "buffer_store_dwordx2 v[210:211], v208, s[16:19], 0 offen sc1\n"
    "s_mov_b64 exec, -1\n"
    "Leu_35_%=:\n"
    "s_mul_i32 s31, s25, 1024\n"
    "s_add_u32 s31, s31, 10571776\n"
    "buffer_load_dwordx4 v[16:19], v1, s[16:19], s31 offen sc1\n"
    "s_add_u32 s31, s31, 16384\n"
    "buffer_load_dwordx4 v[20:23], v1, s[16:19], s31 offen sc1\n"
    "s_add_u32 s31, s31, 16384\n"
    "buffer_load_dwordx4 v[24:27], v1, s[16:19], s31 offen sc1\n"
    "s_add_u32 s31, s31, 16384\n"
    "buffer_load_dwordx4 v[28:31], v1, s[16:19], s31 offen sc1\n"
    "s_waitcnt vmcnt(0)\n"
    "v_add_f32 v16, v16, v20\n"
    "v_add_f32 v24, v24, v28\n"
    "v_add_f32 v16, v16, v24\n"
    "v_add_f32 v17, v17, v21\n"
    "v_add_f32 v25, v25, v29\n"
    "v_add_f32 v17, v17, v25\n"
    "v_add_f32 v18, v18, v22\n"
    "v_add_f32 v26, v26, v30\n"
    "v_add_f32 v18, v18, v26\n"
    "v_add_f32 v19, v19, v23\n"
    "v_add_f32 v27, v27, v31\n"
    "v_add_f32 v19, v19, v27\n"
    "v_add_u32 v208, 160768, v1\n"
    "ds_write_b128 v208, v[16:19]\n"
    "s_waitcnt lgkmcnt(0)\n"
    "Ls2_32_%=:\n"
    "s_waitcnt vmcnt(0)\n"
    "s_waitcnt lgkmcnt(0)\n"
    "s_barrier\n"
    "s_mul_i32 s37, s25, 286720\n"
    "s_add_u32 s37, s37, 3706880\n"
    "s_add_u32 s37, s37, s29\n"
    "s_mul_i32 s94, s25, 51200\n"
    "s_add_u32 s94, s94, 2883584\n"
    "s_add_u32 s94, s94, s93\n"
    "v_mov_b32 v104, 0\n"
    "v_mov_b32 v106, 0\n"
    "ds_read_b128 v[48:51], v14 offset:0\n"
    "ds_read_b128 v[52:55], v15 offset:0\n"
    "ds_read_b128 v[56:59], v14 offset:6144\n"
    "ds_read_b128 v[60:63], v15 offset:6144\n"
    "ds_read_b64_tr_b16 v[64:65], v216 offset:1536\n"
    "ds_read_b64_tr_b16 v[66:67], v216 offset:1920\n"
    "ds_read_b64_tr_b16 v[68:69], v216 offset:2304\n"
    "ds_read_b64_tr_b16 v[70:71], v216 offset:2688\n"
    "s_waitcnt lgkmcnt(6)\n"
    "v_mfma_f32_32x32x16_bf16 v[16:31], v[48:51], v[224:227], 0\n"
    "v_mfma_f32_32x32x16_bf16 v[16:31], v[52:55], v[228:231], v[16:31]\n"
    "s_nop 11\n"
    "v_cndmask_b32 v16, v16, v219, s[58:59]\n"
    "v_cndmask_b32 v17, v17, v219, s[60:61]\n"
    "v_cndmask_b32 v18, v18, v219, s[62:63]\n"
    "v_cndmask_b32 v19, v19, v219, s[64:65]\n"
    "v_exp_f32 v16, v16\n"
    "v_exp_f32 v17, v17\n"
    "v_exp_f32 v18, v18\n"
    "v_exp_f32 v19, v19\n"
    "s_waitcnt lgkmcnt(4)\n"
    "v_mfma_f32_32x32x16_bf16 v[32:47], v[56:59], v[224:227], 0\n"
    "v_max_f32 v104, v104, v54\n"
    "v_cndmask_b32 v20, v20, v219, s[66:67]\n"
    "v_cndmask_b32 v21, v21, v219, s[68:69]\n"
    "v_cndmask_b32 v22, v22, v219, s[70:71]\n"
    "v_cndmask_b32 v23, v23, v219, s[72:73]\n"
    "v_exp_f32 v20, v20\n"
    "v_exp_f32 v21, v21\n"
    "v_exp_f32 v22, v22\n"
    "v_exp_f32 v23, v23\n"
    "v_mfma_f32_32x32x16_bf16 v[32:47], v[60:63], v[228:231], v[32:47]\n"
    "ds_read_b128 v[48:51], v14 offset:12288\n"
    "ds_read_b128 v[52:55], v15 offset:12288\n"
    "v_cvt_pk_bf16_f32 v96, v16, v17\n"
    "v_cvt_pk_bf16_f32 v97, v18, v19\n"
    "v_cvt_pk_bf16_f32 v98, v20, v21\n"
    "v_cvt_pk_bf16_f32 v99, v22, v23\n"
    "ds_read_b64_tr_b16 v[80:81], v216 offset:7680\n"
    "ds_read_b64_tr_b16 v[82:83], v216 offset:8064\n"
    "v_cndmask_b32 v24, v24, v219, s[74:75]\n"
    "v_cndmask_b32 v25, v25, v219, s[76:77]\n"
    "v_cndmask_b32 v26, v26, v219, s[78:79]\n"
    "v_cndmask_b32 v27, v27, v219, s[80:81]\n"
    "v_exp_f32 v24, v24\n"
    "v_exp_f32 v25, v25\n"
    "v_exp_f32 v26, v26\n"
    "v_exp_f32 v27, v27\n"
    "s_waitcnt lgkmcnt(4)\n"
    "v_mfma_f32_32x32x16_bf16 v[112:127], v[64:67], v[96:99], 0\n"
    "v_cndmask_b32 v28, v28, v219, s[82:83]\n"
    "v_cndmask_b32 v29, v29, v219, s[84:85]\n"
    "v_cndmask_b32 v30, v30, v219, s[86:87]\n"
    "v_cndmask_b32 v31, v31, v219, s[88:89]\n"
    "v_exp_f32 v28, v28\n"
    "v_exp_f32 v29, v29\n"
    "v_exp_f32 v30, v30\n"
    "v_exp_f32 v31, v31\n"
    "s_add_u32 m0, s93, 32768\n"
    "s_add_u32 s92, s94, 0\n"
    "buffer_load_dwordx4 v1, s[16:19], s92 offen sc1 lds\n"
    "v_cvt_pk_bf16_f32 v100, v24, v25\n"
    "v_cvt_pk_bf16_f32 v101, v26, v27\n"
    "v_cvt_pk_bf16_f32 v102, v28, v29\n"
    "v_cvt_pk_bf16_f32 v103, v30, v31\n"
    "ds_read_b64_tr_b16 v[84:85], v216 offset:8448\n"
    "ds_read_b64_tr_b16 v[86:87], v216 offset:8832\n"
    "v_mfma_f32_32x32x16_bf16 v[112:127], v[68:71], v[100:103], v[112:127]\n"
    "v_exp_f32 v32, v32\n"
    "v_exp_f32 v33, v33\n"
    "v_exp_f32 v34, v34\n"
    "v_exp_f32 v35, v35\n"
    "s_waitcnt lgkmcnt(4)\n"
    "v_mfma_f32_32x32x16_bf16 v[16:31], v[48:51], v[224:227], 0\n"
    "v_max_f32 v104, v104, v62\n"
    "v_exp_f32 v36, v36\n"
    "v_exp_f32 v37, v37\n"
    "v_exp_f32 v38, v38\n"
    "v_exp_f32 v39, v39\n"
    "v_mfma_f32_32x32x16_bf16 v[16:31], v[52:55], v[228:231], v[16:31]\n"
    "ds_read_b128 v[56:59], v14 offset:18432\n"
    "ds_read_b128 v[60:63], v15 offset:18432\n"
    "v_cvt_pk_bf16_f32 v96, v32, v33\n"
    "v_cvt_pk_bf16_f32 v97, v34, v35\n"
    "v_cvt_pk_bf16_f32 v98, v36, v37\n"
    "v_cvt_pk_bf16_f32 v99, v38, v39\n"
    "ds_read_b64_tr_b16 v[64:65], v216 offset:13824\n"
    "ds_read_b64_tr_b16 v[66:67], v216 offset:14208\n"
    "v_exp_f32 v40, v40\n"
    "v_exp_f32 v41, v41\n"
    "v_exp_f32 v42, v42\n"
    "v_exp_f32 v43, v43\n"
    "s_waitcnt lgkmcnt(4)\n"
    "v_mfma_f32_32x32x16_bf16 v[112:127], v[80:83], v[96:99], v[112:127]\n"
    "v_exp_f32 v44, v44\n"
    "v_exp_f32 v45, v45\n"
    "v_exp_f32 v46, v46\n"
    "v_exp_f32 v47, v47\n"
    "s_add_u32 m0, s93, 36864\n"
    "s_add_u32 s92, s94, 4096\n"
    "buffer_load_dwordx4 v1, s[16:19], s92 offen sc1 lds\n"
    "v_cvt_pk_bf16_f32 v100, v40, v41\n"
    "v_cvt_pk_bf16_f32 v101, v42, v43\n"
    "v_cvt_pk_bf16_f32 v102, v44, v45\n"
    "v_cvt_pk_bf16_f32 v103, v46, v47\n"
    "ds_read_b64_tr_b16 v[68:69], v216 offset:14592\n"
    "ds_read_b64_tr_b16 v[70:71], v216 offset:14976\n"
    "v_mfma_f32_32x32x16_bf16 v[112:127], v[84:87], v[100:103], v[112:127]\n"
    "v_exp_f32 v16, v16\n"
    "v_exp_f32 v17, v17\n"
    "v_exp_f32 v18, v18\n"
    "v_exp_f32 v19, v19\n"
    "s_waitcnt lgkmcnt(4)\n"
    "v_mfma_f32_32x32x16_bf16 v[32:47], v[56:59], v[224:227], 0\n"
    "v_max_f32 v104, v104, v54\n"
    "v_exp_f32 v20, v20\n"
    "v_exp_f32 v21, v21\n"
    "v_exp_f32 v22, v22\n"
    "v_exp_f32 v23, v23\n"
    "v_mfma_f32_32x32x16_bf16 v[32:47], v[60:63], v[228:231], v[32:47]\n"
    "ds_read_b128 v[48:51], v14 offset:24576\n"
    "ds_read_b128 v[52:55], v15 offset:24576\n"
    "v_cvt_pk_bf16_f32 v96, v16, v17\n"
    "v_cvt_pk_bf16_f32 v97, v18, v19\n"
    "v_cvt_pk_bf16_f32 v98, v20, v21\n"
    "v_cvt_pk_bf16_f32 v99, v22, v23\n"
    "ds_read_b64_tr_b16 v[80:81], v216 offset:19968\n"
    "ds_read_b64_tr_b16 v[82:83], v216 offset:20352\n"
    "v_exp_f32 v24, v24\n"
    "v_exp_f32 v25, v25\n"
    "v_exp_f32 v26, v26\n"
    "v_exp_f32 v27, v27\n"
    "s_waitcnt lgkmcnt(4)\n"
    "v_mfma_f32_32x32x16_bf16 v[112:127], v[64:67], v[96:99], v[112:127]\n"
    "v_exp_f32 v28, v28\n"
    "v_exp_f32 v29, v29\n"
    "v_exp_f32 v30, v30\n"
    "v_exp_f32 v31, v31\n"
    "s_add_u32 m0, s93, 40960\n"
    "s_add_u32 s92, s94, 8192\n"
    "buffer_load_dwordx4 v1, s[16:19], s92 offen sc1 lds\n"
    "v_cvt_pk_bf16_f32 v100, v24, v25\n"
    "v_cvt_pk_bf16_f32 v101, v26, v27\n"
    "v_cvt_pk_bf16_f32 v102, v28, v29\n"
    "v_cvt_pk_bf16_f32 v103, v30, v31\n"
    "ds_read_b64_tr_b16 v[84:85], v216 offset:20736\n"
    "ds_read_b64_tr_b16 v[86:87], v216 offset:21120\n"
    "v_mfma_f32_32x32x16_bf16 v[112:127], v[68:71], v[100:103], v[112:127]\n"
    "v_exp_f32 v32, v32\n"
    "v_exp_f32 v33, v33\n"
    "v_exp_f32 v34, v34\n"
    "v_exp_f32 v35, v35\n"
    "s_waitcnt lgkmcnt(4)\n"
    "v_mfma_f32_32x32x16_bf16 v[16:31], v[48:51], v[224:227], 0\n"
    "v_max_f32 v104, v104, v62\n"
    "v_exp_f32 v36, v36\n"
    "v_exp_f32 v37, v37\n"
    "v_exp_f32 v38, v38\n"
    "v_exp_f32 v39, v39\n"
    "v_mfma_f32_32x32x16_bf16 v[16:31], v[52:55], v[228:231], v[16:31]\n"
    "ds_read_b128 v[56:59], v14 offset:30720\n"
    "ds_read_b128 v[60:63], v15 offset:30720\n"
    "v_cvt_pk_bf16_f32 v96, v32, v33\n"
    "v_cvt_pk_bf16_f32 v97, v34, v35\n"
    "v_cvt_pk_bf16_f32 v98, v36, v37\n"
    "v_cvt_pk_bf16_f32 v99, v38, v39\n"
    "ds_read_b64_tr_b16 v[64:65], v216 offset:26112\n"
    "ds_read_b64_tr_b16 v[66:67], v216 offset:26496\n"
    "v_exp_f32 v40, v40\n"
    "v_exp_f32 v41, v41\n"
    "v_exp_f32 v42, v42\n"
    "v_exp_f32 v43, v43\n"
    "s_waitcnt lgkmcnt(4)\n"
    "v_mfma_f32_32x32x16_bf16 v[112:127], v[80:83], v[96:99], v[112:127]\n"
    "v_exp_f32 v44, v44\n"
    "v_exp_f32 v45, v45\n"
    "v_exp_f32 v46, v46\n"
    "v_exp_f32 v47, v47\n"
    "s_add_u32 m0, s93, 45056\n"
    "s_add_u32 s92, s94, 12288\n"
    "buffer_load_dwordx4 v1, s[16:19], s92 offen sc1 lds\n"
    "v_cvt_pk_bf16_f32 v100, v40, v41\n"
    "v_cvt_pk_bf16_f32 v101, v42, v43\n"
    "v_cvt_pk_bf16_f32 v102, v44, v45\n"
    "v_cvt_pk_bf16_f32 v103, v46, v47\n"
    "ds_read_b64_tr_b16 v[68:69], v216 offset:26880\n"
    "ds_read_b64_tr_b16 v[70:71], v216 offset:27264\n"
    "v_mfma_f32_32x32x16_bf16 v[112:127], v[84:87], v[100:103], v[112:127]\n"
    "v_exp_f32 v16, v16\n"
    "v_exp_f32 v17, v17\n"
    "v_exp_f32 v18, v18\n"
    "v_exp_f32 v19, v19\n"
    "s_waitcnt lgkmcnt(4)\n"
    "v_mfma_f32_32x32x16_bf16 v[32:47], v[56:59], v[224:227], 0\n"
    "v_max_f32 v104, v104, v54\n"
    "v_exp_f32 v20, v20\n"
    "v_exp_f32 v21, v21\n"
    "v_exp_f32 v22, v22\n"
    "v_exp_f32 v23, v23\n"
    "v_mfma_f32_32x32x16_bf16 v[32:47], v[60:63], v[228:231], v[32:47]\n"
    "ds_read_b128 v[48:51], v14 offset:36864\n"
    "ds_read_b128 v[52:55], v15 offset:36864\n"
    "v_cvt_pk_bf16_f32 v96, v16, v17\n"
    "v_cvt_pk_bf16_f32 v97, v18, v19\n"
    "v_cvt_pk_bf16_f32 v98, v20, v21\n"
    "v_cvt_pk_bf16_f32 v99, v22, v23\n"
    "ds_read_b64_tr_b16 v[80:81], v216 offset:32256\n"
    "ds_read_b64_tr_b16 v[82:83], v216 offset:32640\n"
    "v_exp_f32 v24, v24\n"
    "v_exp_f32 v25, v25\n"
    "v_exp_f32 v26, v26\n"
    "v_exp_f32 v27, v27\n"
    "s_waitcnt lgkmcnt(4)\n"
    "v_mfma_f32_32x32x16_bf16 v[112:127], v[64:67], v[96:99], v[112:127]\n"
    "v_exp_f32 v28, v28\n"
    "v_exp_f32 v29, v29\n"
    "v_exp_f32 v30, v30\n"
    "v_exp_f32 v31, v31\n"
    "s_add_u32 m0, s93, 49152\n"
    "s_add_u32 s92, s94, 16384\n"
    "buffer_load_dwordx4 v1, s[16:19], s92 offen sc1 lds\n"
    "v_cvt_pk_bf16_f32 v100, v24, v25\n"
    "v_cvt_pk_bf16_f32 v101, v26, v27\n"
    "v_cvt_pk_bf16_f32 v102, v28, v29\n"
    "v_cvt_pk_bf16_f32 v103, v30, v31\n"
    "ds_read_b64_tr_b16 v[84:85], v216 offset:33024\n"
    "ds_read_b64_tr_b16 v[86:87], v216 offset:33408\n"
    "v_mfma_f32_32x32x16_bf16 v[112:127], v[68:71], v[100:103], v[112:127]\n"
    "v_exp_f32 v32, v32\n"
    "v_exp_f32 v33, v33\n"
    "v_exp_f32 v34, v34\n"
    "v_exp_f32 v35, v35\n"
    "s_waitcnt lgkmcnt(4)\n"
    "v_mfma_f32_32x32x16_bf16 v[16:31], v[48:51], v[224:227], 0\n"
    "v_max_f32 v104, v104, v62\n"
    "v_exp_f32 v36, v36\n"
    "v_exp_f32 v37, v37\n"
    "v_exp_f32 v38, v38\n"
    "v_exp_f32 v39, v39\n"
    "v_mfma_f32_32x32x16_bf16 v[16:31], v[52:55], v[228:231], v[16:31]\n"
    "ds_read_b128 v[56:59], v14 offset:43008\n"
    "ds_read_b128 v[60:63], v15 offset:43008\n"
    "v_cvt_pk_bf16_f32 v96, v32, v33\n"
    "v_cvt_pk_bf16_f32 v97, v34, v35\n"
    "v_cvt_pk_bf16_f32 v98, v36, v37\n"
    "v_cvt_pk_bf16_f32 v99, v38, v39\n"
    "ds_read_b64_tr_b16 v[64:65], v216 offset:38400\n"
    "ds_read_b64_tr_b16 v[66:67], v216 offset:38784\n"
    "v_exp_f32 v40, v40\n"
    "v_exp_f32 v41, v41\n"
    "v_exp_f32 v42, v42\n"
    "v_exp_f32 v43, v43\n"
    "s_waitcnt lgkmcnt(4)\n"
    "v_mfma_f32_32x32x16_bf16 v[112:127], v[80:83], v[96:99], v[112:127]\n"
    "v_exp_f32 v44, v44\n"
    "v_exp_f32 v45, v45\n"
    "v_exp_f32 v46, v46\n"
    "v_exp_f32 v47, v47\n"
    "s_add_u32 m0, s93, 53248\n"
    "s_add_u32 s92, s94, 20480\n"
    "buffer_load_dwordx4 v1, s[16:19], s92 offen sc1 lds\n"
    "v_cvt_pk_bf16_f32 v100, v40, v41\n"
    "v_cvt_pk_bf16_f32 v101, v42, v43\n"
    "v_cvt_pk_bf16_f32 v102, v44, v45\n"
    "v_cvt_pk_bf16_f32 v103, v46, v47\n"
    "ds_read_b64_tr_b16 v[68:69], v216 offset:39168\n"
    "ds_read_b64_tr_b16 v[70:71], v216 offset:39552\n"
    "v_mfma_f32_32x32x16_bf16 v[112:127], v[84:87], v[100:103], v[112:127]\n"
    "v_exp_f32 v16, v16\n"
    "v_exp_f32 v17, v17\n"
    "v_exp_f32 v18, v18\n"
    "v_exp_f32 v19, v19\n"
    "s_waitcnt lgkmcnt(4)\n"
    "v_mfma_f32_32x32x16_bf16 v[32:47], v[56:59], v[224:227], 0\n"
    "v_max_f32 v104, v104, v54\n"
    "v_exp_f32 v20, v20\n"
    "v_exp_f32 v21, v21\n"
    "v_exp_f32 v22, v22\n"
    "v_exp_f32 v23, v23\n"
    "v_mfma_f32_32x32x16_bf16 v[32:47], v[60:63], v[228:231], v[32:47]\n"
    "ds_read_b128 v[48:51], v14 offset:49152\n"
    "ds_read_b128 v[52:55], v15 offset:49152\n"
    "v_cvt_pk_bf16_f32 v96, v16, v17\n"
    "v_cvt_pk_bf16_f32 v97, v18, v19\n"
    "v_cvt_pk_bf16_f32 v98, v20, v21\n"
    "v_cvt_pk_bf16_f32 v99, v22, v23\n"
    "ds_read_b64_tr_b16 v[80:81], v216 offset:44544\n"
    "ds_read_b64_tr_b16 v[82:83], v216 offset:44928\n"
    "v_exp_f32 v24, v24\n"
    "v_exp_f32 v25, v25\n"
    "v_exp_f32 v26, v26\n"
    "v_exp_f32 v27, v27\n"
    "s_waitcnt lgkmcnt(4)\n"
    "v_mfma_f32_32x32x16_bf16 v[112:127], v[64:67], v[96:99], v[112:127]\n"
    "v_exp_f32 v28, v28\n"
    "v_exp_f32 v29, v29\n"
    "v_exp_f32 v30, v30\n"
    "v_exp_f32 v31, v31\n"
    "s_add_u32 m0, s93, 57344\n"
    "s_add_u32 s92, s94, 24576\n"
    "buffer_load_dwordx4 v1, s[16:19], s92 offen sc1 lds\n"
    "v_cvt_pk_bf16_f32 v100, v24, v25\n"
    "v_cvt_pk_bf16_f32 v101, v26, v27\n"
    "v_cvt_pk_bf16_f32 v102, v28, v29\n"
    "v_cvt_pk_bf16_f32 v103, v30, v31\n"
    "ds_read_b64_tr_b16 v[84:85], v216 offset:45312\n"
    "ds_read_b64_tr_b16 v[86:87], v216 offset:45696\n"
    "v_mfma_f32_32x32x16_bf16 v[112:127], v[68:71], v[100:103], v[112:127]\n"
    "v_exp_f32 v32, v32\n"
    "v_exp_f32 v33, v33\n"
    "v_exp_f32 v34, v34\n"
    "v_exp_f32 v35, v35\n"
    "s_waitcnt lgkmcnt(4)\n"
    "v_mfma_f32_32x32x16_bf16 v[16:31], v[48:51], v[224:227], 0\n"
    "v_max_f32 v104, v104, v62\n"
    "v_exp_f32 v36, v36\n"
    "v_exp_f32 v37, v37\n"
    "v_exp_f32 v38, v38\n"
    "v_exp_f32 v39, v39\n"
    "v_mfma_f32_32x32x16_bf16 v[16:31], v[52:55], v[228:231], v[16:31]\n"
    "ds_read_b128 v[56:59], v14 offset:3072\n"
    "ds_read_b128 v[60:63], v15 offset:3072\n"
    "v_cvt_pk_bf16_f32 v96, v32, v33\n"
    "v_cvt_pk_bf16_f32 v97, v34, v35\n"
    "v_cvt_pk_bf16_f32 v98, v36, v37\n"
    "v_cvt_pk_bf16_f32 v99, v38, v39\n"
    "ds_read_b64_tr_b16 v[64:65], v216 offset:50688\n"
    "ds_read_b64_tr_b16 v[66:67], v216 offset:51072\n"
    "v_exp_f32 v40, v40\n"
    "v_exp_f32 v41, v41\n"
    "v_exp_f32 v42, v42\n"
    "v_exp_f32 v43, v43\n"
    "s_waitcnt lgkmcnt(4)\n"
    "v_mfma_f32_32x32x16_bf16 v[112:127], v[80:83], v[96:99], v[112:127]\n"
    "v_exp_f32 v44, v44\n"
    "v_exp_f32 v45, v45\n"
    "v_exp_f32 v46, v46\n"
    "v_exp_f32 v47, v47\n"
    "s_add_u32 m0, s93, 61440\n"
    "s_add_u32 s92, s94, 28672\n"
    "buffer_load_dwordx4 v1, s[16:19], s92 offen sc1 lds\n"
    "v_cvt_pk_bf16_f32 v100, v40, v41\n"
    "v_cvt_pk_bf16_f32 v101, v42, v43\n"
    "v_cvt_pk_bf16_f32 v102, v44, v45\n"
    "v_cvt_pk_bf16_f32 v103, v46, v47\n"
    "ds_read_b64_tr_b16 v[68:69], v216 offset:51456\n"
    "ds_read_b64_tr_b16 v[70:71], v216 offset:51840\n"
    "v_mfma_f32_32x32x16_bf16 v[112:127], v[84:87], v[100:103], v[112:127]\n"
    "v_cndmask_b32 v16, v219, v16, s[58:59]\n"
    "v_cndmask_b32 v17, v219, v17, s[60:61]\n"
    "v_cndmask_b32 v18, v219, v18, s[62:63]\n"
    "v_cndmask_b32 v19, v219, v19, s[64:65]\n"
    "v_exp_f32 v16, v16\n"
    "v_exp_f32 v17, v17\n"
    "v_exp_f32 v18, v18\n"
    "v_exp_f32 v19, v19\n"
    "s_waitcnt lgkmcnt(4)\n"
    "v_mfma_f32_32x32x16_bf16 v[32:47], v[56:59], v[232:235], 0\n"
    "v_max_f32 v104, v104, v54\n"
    "v_cndmask_b32 v20, v219, v20, s[66:67]\n"
    "v_cndmask_b32 v21, v219, v21, s[68:69]\n"
    "v_cndmask_b32 v22, v219, v22, s[70:71]\n"
    "v_cndmask_b32 v23, v219, v23, s[72:73]\n"
    "v_exp_f32 v20, v20\n"
    "v_exp_f32 v21, v21\n"
    "v_exp_f32 v22, v22\n"
    "v_exp_f32 v23, v23\n"
    "v_mfma_f32_32x32x16_bf16 v[32:47], v[60:63], v[236:239], v[32:47]\n"
    "ds_read_b128 v[48:51], v14 offset:9216\n"
    "ds_read_b128 v[52:55], v15 offset:9216\n"
    "v_cvt_pk_bf16_f32 v96, v16, v17\n"
    "v_cvt_pk_bf16_f32 v97, v18, v19\n"
    "v_cvt_pk_bf16_f32 v98, v20, v21\n"
    "v_cvt_pk_bf16_f32 v99, v22, v23\n"
    "ds_read_b64_tr_b16 v[80:81], v217 offset:4608\n"
    "ds_read_b64_tr_b16 v[82:83], v217 offset:4992\n"
    "v_cndmask_b32 v24, v219, v24, s[74:75]\n"
    "v_cndmask_b32 v25, v219, v25, s[76:77]\n"
    "v_cndmask_b32 v26, v219, v26, s[78:79]\n"
    "v_cndmask_b32 v27, v219, v27, s[80:81]\n"
    "v_exp_f32 v24, v24\n"
    "v_exp_f32 v25, v25\n"
    "v_exp_f32 v26, v26\n"
    "v_exp_f32 v27, v27\n"
    "s_waitcnt lgkmcnt(4)\n"
    "v_mfma_f32_32x32x16_bf16 v[112:127], v[64:67], v[96:99], v[112:127]\n"
    "v_cndmask_b32 v28, v219, v28, s[82:83]\n"
    "v_cndmask_b32 v29, v219, v29, s[84:85]\n"
    "v_cndmask_b32 v30, v219, v30, s[86:87]\n"
    "v_cndmask_b32 v31, v219, v31, s[88:89]\n"
    "v_exp_f32 v28, v28\n"
    "v_exp_f32 v29, v29\n"
    "v_exp_f32 v30, v30\n"
    "v_exp_f32 v31, v31\n"
    "s_add_u32 m0, s93, 65536\n"
    "s_add_u32 s92, s94, 32768\n"
    "buffer_load_dwordx4 v1, s[16:19], s92 offen sc1 lds\n"
    "v_cvt_pk_bf16_f32 v100, v24, v25\n"
    "v_cvt_pk_bf16_f32 v101, v26, v27\n"
    "v_cvt_pk_bf16_f32 v102, v28, v29\n"
    "v_cvt_pk_bf16_f32 v103, v30, v31\n"
    "ds_read_b64_tr_b16 v[84:85], v217 offset:5376\n"
    "ds_read_b64_tr_b16 v[86:87], v217 offset:5760\n"
    "v_mfma_f32_32x32x16_bf16 v[112:127], v[68:71], v[100:103], v[112:127]\n"
    "v_cndmask_b32 v32, v32, v219, s[58:59]\n"
    "v_cndmask_b32 v33, v33, v219, s[60:61]\n"
    "v_cndmask_b32 v34, v34, v219, s[62:63]\n"
    "v_cndmask_b32 v35, v35, v219, s[64:65]\n"
    "v_exp_f32 v32, v32\n"
    "v_exp_f32 v33, v33\n"
    "v_exp_f32 v34, v34\n"
    "v_exp_f32 v35, v35\n"
    "s_waitcnt lgkmcnt(4)\n"
    "v_mfma_f32_32x32x16_bf16 v[16:31], v[48:51], v[232:235], 0\n"
    "v_max_f32 v106, v106, v62\n"
    "v_cndmask_b32 v36, v36, v219, s[66:67]\n"
    "v_cndmask_b32 v37, v37, v219, s[68:69]\n"
    "v_cndmask_b32 v38, v38, v219, s[70:71]\n"
    "v_cndmask_b32 v39, v39, v219, s[72:73]\n"
    "v_exp_f32 v36, v36\n"
    "v_exp_f32 v37, v37\n"
    "v_exp_f32 v38, v38\n"
    "v_exp_f32 v39, v39\n"
    "v_mfma_f32_32x32x16_bf16 v[16:31], v[52:55], v[236:239], v[16:31]\n"
    "ds_read_b128 v[56:59], v14 offset:15360\n"
    "ds_read_b128 v[60:63], v15 offset:15360\n"
    "v_cvt_pk_bf16_f32 v96, v32, v33\n"
    "v_cvt_pk_bf16_f32 v97, v34, v35\n"
    "v_cvt_pk_bf16_f32 v98, v36, v37\n"
    "v_cvt_pk_bf16_f32 v99, v38, v39\n"
    "ds_read_b64_tr_b16 v[64:65], v217 offset:10752\n"
    "ds_read_b64_tr_b16 v[66:67], v217 offset:11136\n"
    "v_cndmask_b32 v40, v40, v219, s[74:75]\n"
    "v_cndmask_b32 v41, v41, v219, s[76:77]\n"
    "v_cndmask_b32 v42, v42, v219, s[78:79]\n"
    "v_cndmask_b32 v43, v43, v219, s[80:81]\n"
    "v_exp_f32 v40, v40\n"
    "v_exp_f32 v41, v41\n"
    "v_exp_f32 v42, v42\n"
    "v_exp_f32 v43, v43\n"
    "s_waitcnt lgkmcnt(4)\n"
    "v_mfma_f32_32x32x16_bf16 v[128:143], v[80:83], v[96:99], 0\n"
    "v_cndmask_b32 v44, v44, v219, s[82:83]\n"
    "v_cndmask_b32 v45, v45, v219, s[84:85]\n"
    "v_cndmask_b32 v46, v46, v219, s[86:87]\n"
    "v_cndmask_b32 v47, v47, v219, s[88:89]\n"
    "v_exp_f32 v44, v44\n"
    "v_exp_f32 v45, v45\n"
    "v_exp_f32 v46, v46\n"
    "v_exp_f32 v47, v47\n"
    "s_add_u32 m0, s93, 69632\n"
    "s_add_u32 s92, s94, 36864\n"
    "buffer_load_dwordx4 v1, s[16:19], s92 offen sc1 lds\n"
    "v_cvt_pk_bf16_f32 v100, v40, v41\n"
    "v_cvt_pk_bf16_f32 v101, v42, v43\n"
    "v_cvt_pk_bf16_f32 v102, v44, v45\n"
    "v_cvt_pk_bf16_f32 v103, v46, v47\n"
    "ds_read_b64_tr_b16 v[68:69], v217 offset:11520\n"
    "ds_read_b64_tr_b16 v[70:71], v217 offset:11904\n"
    "v_mfma_f32_32x32x16_bf16 v[128:143], v[84:87], v[100:103], v[128:143]\n"
    "v_exp_f32 v16, v16\n"
    "v_exp_f32 v17, v17\n"
    "v_exp_f32 v18, v18\n"
    "v_exp_f32 v19, v19\n"
    "s_waitcnt lgkmcnt(4)\n"
    "v_mfma_f32_32x32x16_bf16 v[32:47], v[56:59], v[232:235], 0\n"
    "v_max_f32 v106, v106, v54\n"
    "v_exp_f32 v20, v20\n"
    "v_exp_f32 v21, v21\n"
    "v_exp_f32 v22, v22\n"
    "v_exp_f32 v23, v23\n"
    "v_mfma_f32_32x32x16_bf16 v[32:47], v[60:63], v[236:239], v[32:47]\n"
    "ds_read_b128 v[48:51], v14 offset:21504\n"
    "ds_read_b128 v[52:55], v15 offset:21504\n"
    "v_cvt_pk_bf16_f32 v96, v16, v17\n"
    "v_cvt_pk_bf16_f32 v97, v18, v19\n"
    "v_cvt_pk_bf16_f32 v98, v20, v21\n"
    "v_cvt_pk_bf16_f32 v99, v22, v23\n"
    "ds_read_b64_tr_b16 v[80:81], v217 offset:16896\n"
    "ds_read_b64_tr_b16 v[82:83], v217 offset:17280\n"
    "v_exp_f32 v24, v24\n"
    "v_exp_f32 v25, v25\n"
    "v_exp_f32 v26, v26\n"
    "v_exp_f32 v27, v27\n"
    "s_waitcnt lgkmcnt(4)\n"
    "v_mfma_f32_32x32x16_bf16 v[128:143], v[64:67], v[96:99], v[128:143]\n"
    "v_exp_f32 v28, v28\n"
    "v_exp_f32 v29, v29\n"
    "v_exp_f32 v30, v30\n"
    "v_exp_f32 v31, v31\n"
    "s_add_u32 m0, s93, 73728\n"
    "s_add_u32 s92, s94, 40960\n"
    "buffer_load_dwordx4 v1, s[16:19], s92 offen sc1 lds\n"
    "v_cvt_pk_bf16_f32 v100, v24, v25\n"
    "v_cvt_pk_bf16_f32 v101, v26, v27\n"
    "v_cvt_pk_bf16_f32 v102, v28, v29\n"
    "v_cvt_pk_bf16_f32 v103, v30, v31\n"
    "ds_read_b64_tr_b16 v[84:85], v217 offset:17664\n"
    "ds_read_b64_tr_b16 v[86:87], v217 offset:18048\n"
    "v_mfma_f32_32x32x16_bf16 v[128:143], v[68:71], v[100:103], v[128:143]\n"
    "v_exp_f32 v32, v32\n"
    "v_exp_f32 v33, v33\n"
    "v_exp_f32 v34, v34\n"
    "v_exp_f32 v35, v35\n"
    "s_waitcnt lgkmcnt(4)\n"
    "v_mfma_f32_32x32x16_bf16 v[16:31], v[48:51], v[232:235], 0\n"
    "v_max_f32 v106, v106, v62\n"
    "v_exp_f32 v36, v36\n"
    "v_exp_f32 v37, v37\n"
    "v_exp_f32 v38, v38\n"
    "v_exp_f32 v39, v39\n"
    "v_mfma_f32_32x32x16_bf16 v[16:31], v[52:55], v[236:239], v[16:31]\n"
    "ds_read_b128 v[56:59], v14 offset:27648\n"
    "ds_read_b128 v[60:63], v15 offset:27648\n"
    "v_cvt_pk_bf16_f32 v96, v32, v33\n"
    "v_cvt_pk_bf16_f32 v97, v34, v35\n"
    "v_cvt_pk_bf16_f32 v98, v36, v37\n"
    "v_cvt_pk_bf16_f32 v99, v38, v39\n"
    "ds_read_b64_tr_b16 v[64:65], v217 offset:23040\n"
    "ds_read_b64_tr_b16 v[66:67], v217 offset:23424\n"
    "v_exp_f32 v40, v40\n"
    "v_exp_f32 v41, v41\n"
    "v_exp_f32 v42, v42\n"
    "v_exp_f32 v43, v43\n"
    "s_waitcnt lgkmcnt(4)\n"
    "v_mfma_f32_32x32x16_bf16 v[128:143], v[80:83], v[96:99], v[128:143]\n"
    "v_exp_f32 v44, v44\n"
    "v_exp_f32 v45, v45\n"
    "v_exp_f32 v46, v46\n"
    "v_exp_f32 v47, v47\n"
    "s_add_u32 m0, s93, 77824\n"
    "s_add_u32 s92, s94, 45056\n"
    "buffer_load_dwordx4 v1, s[16:19], s92 offen sc1 lds\n"
    "v_cvt_pk_bf16_f32 v100, v40, v41\n"
    "v_cvt_pk_bf16_f32 v101, v42, v43\n"
    "v_cvt_pk_bf16_f32 v102, v44, v45\n"
    "v_cvt_pk_bf16_f32 v103, v46, v47\n"
    "ds_read_b64_tr_b16 v[68:69], v217 offset:23808\n"
    "ds_read_b64_tr_b16 v[70:71], v217 offset:24192\n"
    "v_mfma_f32_32x32x16_bf16 v[128:143], v[84:87], v[100:103], v[128:143]\n"
    "v_exp_f32 v16, v16\n"
    "v_exp_f32 v17, v17\n"
    "v_exp_f32 v18, v18\n"
    "v_exp_f32 v19, v19\n"
    "s_waitcnt lgkmcnt(4)\n"
    "v_mfma_f32_32x32x16_bf16 v[32:47], v[56:59], v[232:235], 0\n"
    "v_max_f32 v106, v106, v54\n"
    "v_exp_f32 v20, v20\n"
    "v_exp_f32 v21, v21\n"
    "v_exp_f32 v22, v22\n"
    "v_exp_f32 v23, v23\n"
    "v_mfma_f32_32x32x16_bf16 v[32:47], v[60:63], v[236:239], v[32:47]\n"
    "ds_read_b128 v[48:51], v14 offset:33792\n"
    "ds_read_b128 v[52:55], v15 offset:33792\n"
    "v_cvt_pk_bf16_f32 v96, v16, v17\n"
    "v_cvt_pk_bf16_f32 v97, v18, v19\n"
    "v_cvt_pk_bf16_f32 v98, v20, v21\n"
    "v_cvt_pk_bf16_f32 v99, v22, v23\n"
    "ds_read_b64_tr_b16 v[80:81], v217 offset:29184\n"
    "ds_read_b64_tr_b16 v[82:83], v217 offset:29568\n"
    "v_exp_f32 v24, v24\n"
    "v_exp_f32 v25, v25\n"
    "v_exp_f32 v26, v26\n"
    "v_exp_f32 v27, v27\n"
    "s_waitcnt lgkmcnt(4)\n"
    "v_mfma_f32_32x32x16_bf16 v[128:143], v[64:67], v[96:99], v[128:143]\n"
    "v_exp_f32 v28, v28\n"
    "v_exp_f32 v29, v29\n"
    "v_exp_f32 v30, v30\n"
    "v_exp_f32 v31, v31\n"
    "s_add_u32 m0, s93, 81920\n"
    "s_add_u32 s92, s94, 49152\n"
    "buffer_load_dwordx4 v1, s[16:19], s92 offen sc1 lds\n"
    "v_cvt_pk_bf16_f32 v100, v24, v25\n"
    "v_cvt_pk_bf16_f32 v101, v26, v27\n"
    "v_cvt_pk_bf16_f32 v102, v28, v29\n"
    "v_cvt_pk_bf16_f32 v103, v30, v31\n"
    "ds_read_b64_tr_b16 v[84:85], v217 offset:29952\n"
    "ds_read_b64_tr_b16 v[86:87], v217 offset:30336\n"
    "v_mfma_f32_32x32x16_bf16 v[128:143], v[68:71], v[100:103], v[128:143]\n"
    "v_exp_f32 v32, v32\n"
    "v_exp_f32 v33, v33\n"
    "v_exp_f32 v34, v34\n"
    "v_exp_f32 v35, v35\n"
    "s_waitcnt lgkmcnt(4)\n"
    "v_mfma_f32_32x32x16_bf16 v[16:31], v[48:51], v[232:235], 0\n"
    "v_max_f32 v106, v106, v62\n"
    "v_exp_f32 v36, v36\n"
    "v_exp_f32 v37, v37\n"
    "v_exp_f32 v38, v38\n"
    "v_exp_f32 v39, v39\n"
    "v_mfma_f32_32x32x16_bf16 v[16:31], v[52:55], v[236:239], v[16:31]\n"
    "ds_read_b128 v[56:59], v14 offset:39936\n"
    "ds_read_b128 v[60:63], v15 offset:39936\n"
    "v_cvt_pk_bf16_f32 v96, v32, v33\n"
    "v_cvt_pk_bf16_f32 v97, v34, v35\n"
    "v_cvt_pk_bf16_f32 v98, v36, v37\n"
    "v_cvt_pk_bf16_f32 v99, v38, v39\n"
    "ds_read_b64_tr_b16 v[64:65], v217 offset:35328\n"
    "ds_read_b64_tr_b16 v[66:67], v217 offset:35712\n"
    "v_exp_f32 v40, v40\n"
    "v_exp_f32 v41, v41\n"
    "v_exp_f32 v42, v42\n"
    "v_exp_f32 v43, v43\n"
    "s_waitcnt lgkmcnt(4)\n"
    "v_mfma_f32_32x32x16_bf16 v[128:143], v[80:83], v[96:99], v[128:143]\n"
    "v_exp_f32 v44, v44\n"
    "v_exp_f32 v45, v45\n"
    "v_exp_f32 v46, v46\n"
    "v_exp_f32 v47, v47\n"
    "s_add_u32 m0, s29, 0\n"
    "s_add_u32 s14, s37, 0\n"
    "buffer_load_dwordx4 v1, s[16:19], s14 offen offset:0 sc1 lds\n"
    "v_cvt_pk_bf16_f32 v100, v40, v41\n"
    "v_cvt_pk_bf16_f32 v101, v42, v43\n"
    "v_cvt_pk_bf16_f32 v102, v44, v45\n"
    "v_cvt_pk_bf16_f32 v103, v46, v47\n"
    "ds_read_b64_tr_b16 v[68:69], v217 offset:36096\n"
    "ds_read_b64_tr_b16 v[70:71], v217 offset:36480\n"
    "v_mfma_f32_32x32x16_bf16 v[128:143], v[84:87], v[100:103], v[128:143]\n"
    "v_exp_f32 v16, v16\n"
    "v_exp_f32 v17, v17\n"
    "v_exp_f32 v18, v18\n"
    "v_exp_f32 v19, v19\n"
    "s_waitcnt lgkmcnt(4)\n"
    "v_mfma_f32_32x32x16_bf16 v[32:47], v[56:59], v[232:235], 0\n"
    "v_max_f32 v106, v106, v54\n"
    "v_exp_f32 v20, v20\n"
    "v_exp_f32 v21, v21\n"
    "v_exp_f32 v22, v22\n"
    "v_exp_f32 v23, v23\n"
    "v_mfma_f32_32x32x16_bf16 v[32:47], v[60:63], v[236:239], v[32:47]\n"
    "ds_read_b128 v[48:51], v14 offset:46080\n"
    "ds_read_b128 v[52:55], v15 offset:46080\n"
    "v_cvt_pk_bf16_f32 v96, v16, v17\n"
    "v_cvt_pk_bf16_f32 v97, v18, v19\n"
    "v_cvt_pk_bf16_f32 v98, v20, v21\n"
    "v_cvt_pk_bf16_f32 v99, v22, v23\n"
    "ds_read_b64_tr_b16 v[80:81], v217 offset:41472\n"
    "ds_read_b64_tr_b16 v[82:83], v217 offset:41856\n"
    "v_exp_f32 v24, v24\n"
    "v_exp_f32 v25, v25\n"
    "v_exp_f32 v26, v26\n"
    "v_exp_f32 v27, v27\n"
    "s_waitcnt lgkmcnt(4)\n"
    "v_mfma_f32_32x32x16_bf16 v[128:143], v[64:67], v[96:99], v[128:143]\n"
    "v_exp_f32 v28, v28\n"
    "v_exp_f32 v29, v29\n"
    "v_exp_f32 v30, v30\n"
    "v_exp_f32 v31, v31\n"
    "buffer_load_dwordx4 v1, s[16:19], s14 offen offset:1024 sc1 lds\n"
    "v_cvt_pk_bf16_f32 v100, v24, v25\n"
    "v_cvt_pk_bf16_f32 v101, v26, v27\n"
    "v_cvt_pk_bf16_f32 v102, v28, v29\n"
    "v_cvt_pk_bf16_f32 v103, v30, v31\n"
    "ds_read_b64_tr_b16 v[84:85], v217 offset:42240\n"
    "ds_read_b64_tr_b16 v[86:87], v217 offset:42624\n"
    "v_mfma_f32_32x32x16_bf16 v[128:143], v[68:71], v[100:103], v[128:143]\n"
    "v_exp_f32 v32, v32\n"
    "v_exp_f32 v33, v33\n"
    "v_exp_f32 v34, v34\n"
    "v_exp_f32 v35, v35\n"
    "s_waitcnt lgkmcnt(4)\n"
    "v_mfma_f32_32x32x16_bf16 v[16:31], v[48:51], v[232:235], 0\n"
    "v_max_f32 v106, v106, v62\n"
    "v_exp_f32 v36, v36\n"
    "v_exp_f32 v37, v37\n"
    "v_exp_f32 v38, v38\n"
    "v_exp_f32 v39, v39\n"
    "v_mfma_f32_32x32x16_bf16 v[16:31], v[52:55], v[236:239], v[16:31]\n"
    "ds_read_b128 v[56:59], v14 offset:52224\n"
    "ds_read_b128 v[60:63], v15 offset:52224\n"
    "v_cvt_pk_bf16_f32 v96, v32, v33\n"
    "v_cvt_pk_bf16_f32 v97, v34, v35\n"
    "v_cvt_pk_bf16_f32 v98, v36, v37\n"
    "v_cvt_pk_bf16_f32 v99, v38, v39\n"
    "ds_read_b64_tr_b16 v[64:65], v217 offset:47616\n"
    "ds_read_b64_tr_b16 v[66:67], v217 offset:48000\n"
    "v_exp_f32 v40, v40\n"
    "v_exp_f32 v41, v41\n"
    "v_exp_f32 v42, v42\n"
    "v_exp_f32 v43, v43\n"
    "s_waitcnt lgkmcnt(4)\n"
    "v_mfma_f32_32x32x16_bf16 v[128:143], v[80:83], v[96:99], v[128:143]\n"
    "v_exp_f32 v44, v44\n"
    "v_exp_f32 v45, v45\n"
    "v_exp_f32 v46, v46\n"
    "v_exp_f32 v47, v47\n"
    "buffer_load_dwordx4 v1, s[16:19], s14 offen offset:2048 sc1 lds\n"
    "v_cvt_pk_bf16_f32 v100, v40, v41\n"
    "v_cvt_pk_bf16_f32 v101, v42, v43\n"
    "v_cvt_pk_bf16_f32 v102, v44, v45\n"
    "v_cvt_pk_bf16_f32 v103, v46, v47\n"
    "ds_read_b64_tr_b16 v[68:69], v217 offset:48384\n"
    "ds_read_b64_tr_b16 v[70:71], v217 offset:48768\n"
    "v_mfma_f32_32x32x16_bf16 v[128:143], v[84:87], v[100:103], v[128:143]\n"
    "v_exp_f32 v16, v16\n"
    "v_exp_f32 v17, v17\n"
    "v_exp_f32 v18, v18\n"
    "v_exp_f32 v19, v19\n"
    "s_waitcnt lgkmcnt(4)\n"
    "v_mfma_f32_32x32x16_bf16 v[32:47], v[56:59], v[232:235], 0\n"
    "v_max_f32 v106, v106, v54\n"
    "v_exp_f32 v20, v20\n"
    "v_exp_f32 v21, v21\n"
    "v_exp_f32 v22, v22\n"
    "v_exp_f32 v23, v23\n"
    "v_mfma_f32_32x32x16_bf16 v[32:47], v[60:63], v[236:239], v[32:47]\n"
    "v_cvt_pk_bf16_f32 v96, v16, v17\n"
    "v_cvt_pk_bf16_f32 v97, v18, v19\n"
    "v_cvt_pk_bf16_f32 v98, v20, v21\n"
    "v_cvt_pk_bf16_f32 v99, v22, v23\n"
    "ds_read_b64_tr_b16 v[80:81], v217 offset:53760\n"
    "ds_read_b64_tr_b16 v[82:83], v217 offset:54144\n"
    "v_exp_f32 v24, v24\n"
    "v_exp_f32 v25, v25\n"
    "v_exp_f32 v26, v26\n"
    "v_exp_f32 v27, v27\n"
    "s_waitcnt lgkmcnt(2)\n"
    "v_mfma_f32_32x32x16_bf16 v[128:143], v[64:67], v[96:99], v[128:143]\n"
    "v_exp_f32 v28, v28\n"
    "v_exp_f32 v29, v29\n"
    "v_exp_f32 v30, v30\n"
    "v_exp_f32 v31, v31\n"
    "buffer_load_dwordx4 v1, s[16:19], s14 offen offset:3072 sc1 lds\n"
    "v_cvt_pk_bf16_f32 v100, v24, v25\n"
    "v_cvt_pk_bf16_f32 v101, v26, v27\n"
    "v_cvt_pk_bf16_f32 v102, v28, v29\n"
    "v_cvt_pk_bf16_f32 v103, v30, v31\n"
    "ds_read_b64_tr_b16 v[84:85], v217 offset:54528\n"
    "ds_read_b64_tr_b16 v[86:87], v217 offset:54912\n"
    "v_mfma_f32_32x32x16_bf16 v[128:143], v[68:71], v[100:103], v[128:143]\n"
    "v_cndmask_b32 v32, v219, v32, s[58:59]\n"
    "v_cndmask_b32 v33, v219, v33, s[60:61]\n"
    "v_cndmask_b32 v34, v219, v34, s[62:63]\n"
    "v_cndmask_b32 v35, v219, v35, s[64:65]\n"
    "v_exp_f32 v32, v32\n"
    "v_exp_f32 v33, v33\n"
    "v_exp_f32 v34, v34\n"
    "v_exp_f32 v35, v35\n"
    "v_max_f32 v106, v106, v62\n"
    "v_cndmask_b32 v36, v219, v36, s[66:67]\n"
    "v_cndmask_b32 v37, v219, v37, s[68:69]\n"
    "v_cndmask_b32 v38, v219, v38, s[70:71]\n"
    "v_cndmask_b32 v39, v219, v39, s[72:73]\n"
    "v_exp_f32 v36, v36\n"
    "v_exp_f32 v37, v37\n"
    "v_exp_f32 v38, v38\n"
    "v_exp_f32 v39, v39\n"
    "v_cvt_pk_bf16_f32 v96, v32, v33\n"
    "v_cvt_pk_bf16_f32 v97, v34, v35\n"
    "v_cvt_pk_bf16_f32 v98, v36, v37\n"
    "v_cvt_pk_bf16_f32 v99, v38, v39\n"
    "v_cndmask_b32 v40, v219, v40, s[74:75]\n"
    "v_cndmask_b32 v41, v219, v41, s[76:77]\n"
    "v_cndmask_b32 v42, v219, v42, s[78:79]\n"
    "v_cndmask_b32 v43, v219, v43, s[80:81]\n"
    "v_exp_f32 v40, v40\n"
    "v_exp_f32 v41, v41\n"
    "v_exp_f32 v42, v42\n"
    "v_exp_f32 v43, v43\n"
    "s_waitcnt lgkmcnt(0)\n"
    "v_mfma_f32_32x32x16_bf16 v[128:143], v[80:83], v[96:99], v[128:143]\n"
    "v_cndmask_b32 v44, v219, v44, s[82:83]\n"
    "v_cndmask_b32 v45, v219, v45, s[84:85]\n"
    "v_cndmask_b32 v46, v219, v46, s[86:87]\n"
    "v_cndmask_b32 v47, v219, v47, s[88:89]\n"
    "v_exp_f32 v44, v44\n"
    "v_exp_f32 v45, v45\n"
    "v_exp_f32 v46, v46\n"
    "v_exp_f32 v47, v47\n"
    "s_add_u32 m0, s29, 16384\n"
    "s_add_u32 s14, s37, 16384\n"
    "buffer_load_dwordx4 v1, s[16:19], s14 offen offset:0 sc1 lds\n"
    "v_cvt_pk_bf16_f32 v100, v40, v41\n"
    "v_cvt_pk_bf16_f32 v101, v42, v43\n"
    "v_cvt_pk_bf16_f32 v102, v44, v45\n"
    "v_cvt_pk_bf16_f32 v103, v46, v47\n"
    "s_nop 1\n"
    "v_mfma_f32_32x32x16_bf16 v[128:143], v[84:87], v[100:103], v[128:143]\n"
    "v_max_f32_dpp v104, v104, v104 row_ror:8 row_mask:0xf bank_mask:0xf\n"
    "v_max_f32_dpp v106, v106, v106 row_ror:8 row_mask:0xf bank_mask:0xf\n"
    "s_nop 0\n"
    "v_max_f32_dpp v104, v104, v104 row_ror:4 row_mask:0xf bank_mask:0xf\n"
    "v_max_f32_dpp v106, v106, v106 row_ror:4 row_mask:0xf bank_mask:0xf\n"
    "s_nop 0\n"
    "v_max_f32_dpp v104, v104, v104 row_ror:2 row_mask:0xf bank_mask:0xf\n"
    "v_max_f32_dpp v106, v106, v106 row_ror:2 row_mask:0xf bank_mask:0xf\n"
    "s_nop 0\n"
    "v_max_f32_dpp v104, v104, v104 row_ror:1 row_mask:0xf bank_mask:0xf\n"
    "v_max_f32_dpp v106, v106, v106 row_ror:1 row_mask:0xf bank_mask:0xf\n"
    "v_mov_b32 v105, v104\n"
    "v_mov_b32 v107, v106\n"
    "s_nop 0\n"
    "v_permlane16_swap_b32 v104, v105\n"
    "v_permlane16_swap_b32 v106, v107\n"
    "v_max_f32 v104, v104, v105\n"
    "v_max_f32 v106, v106, v107\n"
    "v_mul_f32 v104, v104, v220\n"
    "v_mul_f32 v106, v106, v221\n"
    "v_mov_b32 v105, 0x45800000\n"
    "v_cmp_nle_f32 s[90:91], v104, v105\n"
    "v_cmp_nle_f32 s[98:99], v106, v105\n"
    "s_waitcnt lgkmcnt(0)\n"
    "s_cmp_eq_u64 s[90:91], 0\n"
    "s_cbranch_scc1 Lfast_36_%=\n"
    "s_nop 12\n"
    "s_min_u32 s91, s24, 2\n"
    "s_lshl_b32 s91, s91, 2\n"
    "s_add_u32 s91, s91, s26\n"
    "s_sub_i32 s91, 8, s91\n"
    "s_max_i32 s91, s91, 0\n"
    "v_mov_b32 v105, v219\n"
    "s_mov_b32 s90, s91\n"
    "Lmx_37_%=:\n"
    "s_mul_i32 s51, s90, 6144\n"
    "v_add_u32 v106, s51, v14\n"
    "v_add_u32 v107, s51, v15\n"
    "v_add_u32 v108, s51, v216\n"
    "ds_read_b128 v[48:51], v106 offset:0\n"
    "ds_read_b128 v[52:55], v107 offset:0\n"
    "s_waitcnt lgkmcnt(0)\n"
    "v_mfma_f32_32x32x16_bf16 v[16:31], v[48:51], v[224:227], 0\n"
    "v_mfma_f32_32x32x16_bf16 v[16:31], v[52:55], v[228:231], v[16:31]\n"
    "s_nop 13\n"
    "s_cmp_lg_u32 s90, 0\n"
    "s_cbranch_scc1 Lnm0_38_%=\n"
    "v_cndmask_b32 v16, v16, v219, s[58:59]\n"
    "v_cndmask_b32 v17, v17, v219, s[60:61]\n"
    "v_cndmask_b32 v18, v18, v219, s[62:63]\n"
    "v_cndmask_b32 v19, v19, v219, s[64:65]\n"
    "v_cndmask_b32 v20, v20, v219, s[66:67]\n"
    "v_cndmask_b32 v21, v21, v219, s[68:69]\n"
    "v_cndmask_b32 v22, v22, v219, s[70:71]\n"
    "v_cndmask_b32 v23, v23, v219, s[72:73]\n"
    "v_cndmask_b32 v24, v24, v219, s[74:75]\n"
    "v_cndmask_b32 v25, v25, v219, s[76:77]\n"
    "v_cndmask_b32 v26, v26, v219, s[78:79]\n"
    "v_cndmask_b32 v27, v27, v219, s[80:81]\n"
    "v_cndmask_b32 v28, v28, v219, s[82:83]\n"
    "v_cndmask_b32 v29, v29, v219, s[84:85]\n"
    "v_cndmask_b32 v30, v30, v219, s[86:87]\n"
    "v_cndmask_b32 v31, v31, v219, s[88:89]\n"
    "Lnm0_38_%=:\n"
    "s_cmp_lg_u32 s90, 8\n"
    "s_cbranch_scc1 Lnm8_39_%=\n"
    "v_cndmask_b32 v16, v219, v16, s[58:59]\n"
    "v_cndmask_b32 v17, v219, v17, s[60:61]\n"
    "v_cndmask_b32 v18, v219, v18, s[62:63]\n"
    "v_cndmask_b32 v19, v219, v19, s[64:65]\n"
    "v_cndmask_b32 v20, v219, v20, s[66:67]\n"
    "v_cndmask_b32 v21, v219, v21, s[68:69]\n"
    "v_cndmask_b32 v22, v219, v22, s[70:71]\n"
    "v_cndmask_b32 v23, v219, v23, s[72:73]\n"
    "v_cndmask_b32 v24, v219, v24, s[74:75]\n"
    "v_cndmask_b32 v25, v219, v25, s[76:77]\n"
    "v_cndmask_b32 v26, v219, v26, s[78:79]\n"
    "v_cndmask_b32 v27, v219, v27, s[80:81]\n"
    "v_cndmask_b32 v28, v219, v28, s[82:83]\n"
    "v_cndmask_b32 v29, v219, v29, s[84:85]\n"
    "v_cndmask_b32 v30, v219, v30, s[86:87]\n"
    "v_cndmask_b32 v31, v219, v31, s[88:89]\n"
    "Lnm8_39_%=:\n"
    "s_nop 1\n"
    "v_max_f32 v105, v105, v16\n"
    "v_max_f32 v105, v105, v17\n"
    "v_max_f32 v105, v105, v18\n"
    "v_max_f32 v105, v105, v19\n"
    "v_max_f32 v105, v105, v20\n"
    "v_max_f32 v105, v105, v21\n"
    "v_max_f32 v105, v105, v22\n"
    "v_max_f32 v105, v105, v23\n"
    "v_max_f32 v105, v105, v24\n"
    "v_max_f32 v105, v105, v25\n"
    "v_max_f32 v105, v105, v26\n"
    "v_max_f32 v105, v105, v27\n"
    "v_max_f32 v105, v105, v28\n"
    "v_max_f32 v105, v105, v29\n"
    "v_max_f32 v105, v105, v30\n"
    "v_max_f32 v105, v105, v31\n"
    "s_add_u32 s90, s90, 1\n"
    "s_cmp_le_u32 s90, 8\n"
    "s_nop 3\n"
    "s_cbranch_scc1 Lmx_37_%=\n"
    "v_mov_b32 v106, v105\n"
    "s_nop 1\n"
    "v_permlane32_swap_b32 v105, v106\n"
    "s_nop 1\n"
    "v_max_f32 v105, v105, v106\n"
    "v_mov_b32 v112, 0\n"
    "v_mov_b32 v113, 0\n"
    "v_mov_b32 v114, 0\n"
    "v_mov_b32 v115, 0\n"
    "v_mov_b32 v116, 0\n"
    "v_mov_b32 v117, 0\n"
    "v_mov_b32 v118, 0\n"
    "v_mov_b32 v119, 0\n"
    "v_mov_b32 v120, 0\n"
    "v_mov_b32 v121, 0\n"
    "v_mov_b32 v122, 0\n"
    "v_mov_b32 v123, 0\n"
    "v_mov_b32 v124, 0\n"
    "v_mov_b32 v125, 0\n"
    "v_mov_b32 v126, 0\n"
    "v_mov_b32 v127, 0\n"
    "s_mov_b32 s90, s91\n"
    "Lpv_40_%=:\n"
    "s_mul_i32 s51, s90, 6144\n"
    "v_add_u32 v106, s51, v14\n"
    "v_add_u32 v107, s51, v15\n"
    "v_add_u32 v108, s51, v216\n"
    "ds_read_b128 v[48:51], v106 offset:0\n"
    "ds_read_b128 v[52:55], v107 offset:0\n"
    "s_waitcnt lgkmcnt(0)\n"
    "v_mfma_f32_32x32x16_bf16 v[16:31], v[48:51], v[224:227], 0\n"
    "v_mfma_f32_32x32x16_bf16 v[16:31], v[52:55], v[228:231], v[16:31]\n"
    "s_nop 13\n"
    "s_cmp_lg_u32 s90, 0\n"
    "s_cbranch_scc1 Lnm0_41_%=\n"
    "v_cndmask_b32 v16, v16, v219, s[58:59]\n"
    "v_cndmask_b32 v17, v17, v219, s[60:61]\n"
    "v_cndmask_b32 v18, v18, v219, s[62:63]\n"
    "v_cndmask_b32 v19, v19, v219, s[64:65]\n"
    "v_cndmask_b32 v20, v20, v219, s[66:67]\n"
    "v_cndmask_b32 v21, v21, v219, s[68:69]\n"
    "v_cndmask_b32 v22, v22, v219, s[70:71]\n"
    "v_cndmask_b32 v23, v23, v219, s[72:73]\n"
    "v_cndmask_b32 v24, v24, v219, s[74:75]\n"
    "v_cndmask_b32 v25, v25, v219, s[76:77]\n"
    "v_cndmask_b32 v26, v26, v219, s[78:79]\n"
    "v_cndmask_b32 v27, v27, v219, s[80:81]\n"
    "v_cndmask_b32 v28, v28, v219, s[82:83]\n"
    "v_cndmask_b32 v29, v29, v219, s[84:85]\n"
    "v_cndmask_b32 v30, v30, v219, s[86:87]\n"
    "v_cndmask_b32 v31, v31, v219, s[88:89]\n"
    "Lnm0_41_%=:\n"
    "s_cmp_lg_u32 s90, 8\n"
    "s_cbranch_scc1 Lnm8_42_%=\n"
    "v_cndmask_b32 v16, v219, v16, s[58:59]\n"
    "v_cndmask_b32 v17, v219, v17, s[60:61]\n"
    "v_cndmask_b32 v18, v219, v18, s[62:63]\n"
    "v_cndmask_b32 v19, v219, v19, s[64:65]\n"
    "v_cndmask_b32 v20, v219, v20, s[66:67]\n"
    "v_cndmask_b32 v21, v219, v21, s[68:69]\n"
    "v_cndmask_b32 v22, v219, v22, s[70:71]\n"
    "v_cndmask_b32 v23, v219, v23, s[72:73]\n"
    "v_cndmask_b32 v24, v219, v24, s[74:75]\n"
    "v_cndmask_b32 v25, v219, v25, s[76:77]\n"
    "v_cndmask_b32 v26, v219, v26, s[78:79]\n"
    "v_cndmask_b32 v27, v219, v27, s[80:81]\n"
    "v_cndmask_b32 v28, v219, v28, s[82:83]\n"
    "v_cndmask_b32 v29, v219, v29, s[84:85]\n"
    "v_cndmask_b32 v30, v219, v30, s[86:87]\n"
    "v_cndmask_b32 v31, v219, v31, s[88:89]\n"
    "Lnm8_42_%=:\n"
    "s_nop 1\n"
    "ds_read_b64_tr_b16 v[64:65], v108 offset:1536\n"
    "ds_read_b64_tr_b16 v[66:67], v108 offset:1920\n"
    "ds_read_b64_tr_b16 v[68:69], v108 offset:2304\n"
    "ds_read_b64_tr_b16 v[70:71], v108 offset:2688\n"
    "v_sub_f32 v16, v16, v105\n"
    "v_sub_f32 v17, v17, v105\n"
    "v_sub_f32 v18, v18, v105\n"
    "v_sub_f32 v19, v19, v105\n"
    "v_sub_f32 v20, v20, v105\n"
    "v_sub_f32 v21, v21, v105\n"
    "v_sub_f32 v22, v22, v105\n"
    "v_sub_f32 v23, v23, v105\n"
    "v_sub_f32 v24, v24, v105\n"
    "v_sub_f32 v25, v25, v105\n"
    "v_sub_f32 v26, v26, v105\n"
    "v_sub_f32 v27, v27, v105\n"
    "v_sub_f32 v28, v28, v105\n"
    "v_sub_f32 v29, v29, v105\n"
    "v_sub_f32 v30, v30, v105\n"
    "v_sub_f32 v31, v31, v105\n"
    "v_exp_f32 v16, v16\n"
    "v_exp_f32 v17, v17\n"
    "v_exp_f32 v18, v18\n"
    "v_exp_f32 v19, v19\n"
    "v_exp_f32 v20, v20\n"
    "v_exp_f32 v21, v21\n"
    "v_exp_f32 v22, v22\n"
    "v_exp_f32 v23, v23\n"
    "v_exp_f32 v24, v24\n"
    "v_exp_f32 v25, v25\n"
    "v_exp_f32 v26, v26\n"
    "v_exp_f32 v27, v27\n"
    "v_exp_f32 v28, v28\n"
    "v_exp_f32 v29, v29\n"
    "v_exp_f32 v30, v30\n"
    "v_exp_f32 v31, v31\n"
    "s_nop 0\n"
    "v_cvt_pk_bf16_f32 v96, v16, v17\n"
    "v_cvt_pk_bf16_f32 v97, v18, v19\n"
    "v_cvt_pk_bf16_f32 v98, v20, v21\n"
    "v_cvt_pk_bf16_f32 v99, v22, v23\n"
    "v_cvt_pk_bf16_f32 v100, v24, v25\n"
    "v_cvt_pk_bf16_f32 v101, v26, v27\n"
    "v_cvt_pk_bf16_f32 v102, v28, v29\n"
    "v_cvt_pk_bf16_f32 v103, v30, v31\n"
    "s_waitcnt lgkmcnt(0)\n"
    "s_nop 1\n"
    "v_mfma_f32_32x32x16_bf16 v[112:127], v[64:67], v[96:99], v[112:127]\n"
    "v_mfma_f32_32x32x16_bf16 v[112:127], v[68:71], v[100:103], v[112:127]\n"
    "s_add_u32 s90, s90, 1\n"
    "s_cmp_le_u32 s90, 8\n"
    "s_nop 13\n"
    "s_cbranch_scc1 Lpv_40_%=\n"
    "s_nop 12\n"
    "Lfast_36_%=:\n"
    "s_cmp_eq_u64 s[98:99], 0\n"
    "s_cbranch_scc1 Lfast_43_%=\n"
    "s_nop 12\n"
    "s_min_u32 s91, s24, 2\n"
    "s_lshl_b32 s91, s91, 2\n"
    "s_add_u32 s91, s91, s26\n"
    "s_sub_i32 s91, 8, s91\n"
    "s_max_i32 s91, s91, 0\n"
    "v_mov_b32 v105, v219\n"
    "s_mov_b32 s90, s91\n"
    "Lmx_44_%=:\n"
    "s_mul_i32 s51, s90, 6144\n"
    "v_add_u32 v106, s51, v14\n"
    "v_add_u32 v107, s51, v15\n"
    "v_add_u32 v108, s51, v217\n"
    "ds_read_b128 v[48:51], v106 offset:3072\n"
    "ds_read_b128 v[52:55], v107 offset:3072\n"
    "s_waitcnt lgkmcnt(0)\n"
    "v_mfma_f32_32x32x16_bf16 v[16:31], v[48:51], v[232:235], 0\n"
    "v_mfma_f32_32x32x16_bf16 v[16:31], v[52:55], v[236:239], v[16:31]\n"
    "s_nop 13\n"
    "s_cmp_lg_u32 s90, 0\n"
    "s_cbranch_scc1 Lnm0_45_%=\n"
    "v_cndmask_b32 v16, v16, v219, s[58:59]\n"
    "v_cndmask_b32 v17, v17, v219, s[60:61]\n"
    "v_cndmask_b32 v18, v18, v219, s[62:63]\n"
    "v_cndmask_b32 v19, v19, v219, s[64:65]\n"
    "v_cndmask_b32 v20, v20, v219, s[66:67]\n"
    "v_cndmask_b32 v21, v21, v219, s[68:69]\n"
    "v_cndmask_b32 v22, v22, v219, s[70:71]\n"
    "v_cndmask_b32 v23, v23, v219, s[72:73]\n"
    "v_cndmask_b32 v24, v24, v219, s[74:75]\n"
    "v_cndmask_b32 v25, v25, v219, s[76:77]\n"
    "v_cndmask_b32 v26, v26, v219, s[78:79]\n"
    "v_cndmask_b32 v27, v27, v219, s[80:81]\n"
    "v_cndmask_b32 v28, v28, v219, s[82:83]\n"
    "v_cndmask_b32 v29, v29, v219, s[84:85]\n"
    "v_cndmask_b32 v30, v30, v219, s[86:87]\n"
    "v_cndmask_b32 v31, v31, v219, s[88:89]\n"
    "Lnm0_45_%=:\n"
    "s_cmp_lg_u32 s90, 8\n"
    "s_cbranch_scc1 Lnm8_46_%=\n"
    "v_cndmask_b32 v16, v219, v16, s[58:59]\n"
    "v_cndmask_b32 v17, v219, v17, s[60:61]\n"
    "v_cndmask_b32 v18, v219, v18, s[62:63]\n"
    "v_cndmask_b32 v19, v219, v19, s[64:65]\n"
    "v_cndmask_b32 v20, v219, v20, s[66:67]\n"
    "v_cndmask_b32 v21, v219, v21, s[68:69]\n"
    "v_cndmask_b32 v22, v219, v22, s[70:71]\n"
    "v_cndmask_b32 v23, v219, v23, s[72:73]\n"
    "v_cndmask_b32 v24, v219, v24, s[74:75]\n"
    "v_cndmask_b32 v25, v219, v25, s[76:77]\n"
    "v_cndmask_b32 v26, v219, v26, s[78:79]\n"
    "v_cndmask_b32 v27, v219, v27, s[80:81]\n"
    "v_cndmask_b32 v28, v219, v28, s[82:83]\n"
    "v_cndmask_b32 v29, v219, v29, s[84:85]\n"
    "v_cndmask_b32 v30, v219, v30, s[86:87]\n"
    "v_cndmask_b32 v31, v219, v31, s[88:89]\n"
    "Lnm8_46_%=:\n"
    "s_nop 1\n"
    "v_max_f32 v105, v105, v16\n"
    "v_max_f32 v105, v105, v17\n"
    "v_max_f32 v105, v105, v18\n"
    "v_max_f32 v105, v105, v19\n"
    "v_max_f32 v105, v105, v20\n"
    "v_max_f32 v105, v105, v21\n"
    "v_max_f32 v105, v105, v22\n"
    "v_max_f32 v105, v105, v23\n"
    "v_max_f32 v105, v105, v24\n"
    "v_max_f32 v105, v105, v25\n"
    "v_max_f32 v105, v105, v26\n"
    "v_max_f32 v105, v105, v27\n"
    "v_max_f32 v105, v105, v28\n"
    "v_max_f32 v105, v105, v29\n"
    "v_max_f32 v105, v105, v30\n"
    "v_max_f32 v105, v105, v31\n"
    "s_add_u32 s90, s90, 1\n"
    "s_cmp_le_u32 s90, 8\n"
    "s_nop 3\n"
    "s_cbranch_scc1 Lmx_44_%=\n"
    "v_mov_b32 v106, v105\n"
    "s_nop 1\n"
    "v_permlane32_swap_b32 v105, v106\n"
    "s_nop 1\n"
    "v_max_f32 v105, v105, v106\n"
    "v_mov_b32 v128, 0\n"
    "v_mov_b32 v129, 0\n"
    "v_mov_b32 v130, 0\n"
    "v_mov_b32 v131, 0\n"
    "v_mov_b32 v132, 0\n"
    "v_mov_b32 v133, 0\n"
    "v_mov_b32 v134, 0\n"
    "v_mov_b32 v135, 0\n"
    "v_mov_b32 v136, 0\n"
    "v_mov_b32 v137, 0\n"
    "v_mov_b32 v138, 0\n"
    "v_mov_b32 v139, 0\n"
    "v_mov_b32 v140, 0\n"
    "v_mov_b32 v141, 0\n"
    "v_mov_b32 v142, 0\n"
    "v_mov_b32 v143, 0\n"
    "s_mov_b32 s90, s91\n"
    "Lpv_47_%=:\n"
    "s_mul_i32 s51, s90, 6144\n"
    "v_add_u32 v106, s51, v14\n"
    "v_add_u32 v107, s51, v15\n"
    "v_add_u32 v108, s51, v217\n"
    "ds_read_b128 v[48:51], v106 offset:3072\n"
    "ds_read_b128 v[52:55], v107 offset:3072\n"
    "s_waitcnt lgkmcnt(0)\n"
    "v_mfma_f32_32x32x16_bf16 v[16:31], v[48:51], v[232:235], 0\n"
    "v_mfma_f32_32x32x16_bf16 v[16:31], v[52:55], v[236:239], v[16:31]\n"
    "s_nop 13\n"
    "s_cmp_lg_u32 s90, 0\n"
    "s_cbranch_scc1 Lnm0_48_%=\n"
    "v_cndmask_b32 v16, v16, v219, s[58:59]\n"
    "v_cndmask_b32 v17, v17, v219, s[60:61]\n"
    "v_cndmask_b32 v18, v18, v219, s[62:63]\n"
    "v_cndmask_b32 v19, v19, v219, s[64:65]\n"
    "v_cndmask_b32 v20, v20, v219, s[66:67]\n"
    "v_cndmask_b32 v21, v21, v219, s[68:69]\n"
    "v_cndmask_b32 v22, v22, v219, s[70:71]\n"
    "v_cndmask_b32 v23, v23, v219, s[72:73]\n"
    "v_cndmask_b32 v24, v24, v219, s[74:75]\n"
    "v_cndmask_b32 v25, v25, v219, s[76:77]\n"
    "v_cndmask_b32 v26, v26, v219, s[78:79]\n"
    "v_cndmask_b32 v27, v27, v219, s[80:81]\n"
    "v_cndmask_b32 v28, v28, v219, s[82:83]\n"
    "v_cndmask_b32 v29, v29, v219, s[84:85]\n"
    "v_cndmask_b32 v30, v30, v219, s[86:87]\n"
    "v_cndmask_b32 v31, v31, v219, s[88:89]\n"
    "Lnm0_48_%=:\n"
    "s_cmp_lg_u32 s90, 8\n"
    "s_cbranch_scc1 Lnm8_49_%=\n"
    "v_cndmask_b32 v16, v219, v16, s[58:59]\n"
    "v_cndmask_b32 v17, v219, v17, s[60:61]\n"
    "v_cndmask_b32 v18, v219, v18, s[62:63]\n"
    "v_cndmask_b32 v19, v219, v19, s[64:65]\n"
    "v_cndmask_b32 v20, v219, v20, s[66:67]\n"
    "v_cndmask_b32 v21, v219, v21, s[68:69]\n"
    "v_cndmask_b32 v22, v219, v22, s[70:71]\n"
    "v_cndmask_b32 v23, v219, v23, s[72:73]\n"
    "v_cndmask_b32 v24, v219, v24, s[74:75]\n"
    "v_cndmask_b32 v25, v219, v25, s[76:77]\n"
    "v_cndmask_b32 v26, v219, v26, s[78:79]\n"
    "v_cndmask_b32 v27, v219, v27, s[80:81]\n"
    "v_cndmask_b32 v28, v219, v28, s[82:83]\n"
    "v_cndmask_b32 v29, v219, v29, s[84:85]\n"
    "v_cndmask_b32 v30, v219, v30, s[86:87]\n"
    "v_cndmask_b32 v31, v219, v31, s[88:89]\n"
    "Lnm8_49_%=:\n"
    "s_nop 1\n"
    "ds_read_b64_tr_b16 v[64:65], v108 offset:4608\n"
    "ds_read_b64_tr_b16 v[66:67], v108 offset:4992\n"
    "ds_read_b64_tr_b16 v[68:69], v108 offset:5376\n"
    "ds_read_b64_tr_b16 v[70:71], v108 offset:5760\n"
    "v_sub_f32 v16, v16, v105\n"
    "v_sub_f32 v17, v17, v105\n"
    "v_sub_f32 v18, v18, v105\n"
    "v_sub_f32 v19, v19, v105\n"
    "v_sub_f32 v20, v20, v105\n"
    "v_sub_f32 v21, v21, v105\n"
    "v_sub_f32 v22, v22, v105\n"
    "v_sub_f32 v23, v23, v105\n"
    "v_sub_f32 v24, v24, v105\n"
    "v_sub_f32 v25, v25, v105\n"
    "v_sub_f32 v26, v26, v105\n"
    "v_sub_f32 v27, v27, v105\n"
    "v_sub_f32 v28, v28, v105\n"
    "v_sub_f32 v29, v29, v105\n"
    "v_sub_f32 v30, v30, v105\n"
    "v_sub_f32 v31, v31, v105\n"
    "v_exp_f32 v16, v16\n"
    "v_exp_f32 v17, v17\n"
    "v_exp_f32 v18, v18\n"
    "v_exp_f32 v19, v19\n"
    "v_exp_f32 v20, v20\n"
    "v_exp_f32 v21, v21\n"
    "v_exp_f32 v22, v22\n"
    "v_exp_f32 v23, v23\n"
    "v_exp_f32 v24, v24\n"
    "v_exp_f32 v25, v25\n"
    "v_exp_f32 v26, v26\n"
    "v_exp_f32 v27, v27\n"
    "v_exp_f32 v28, v28\n"
    "v_exp_f32 v29, v29\n"
    "v_exp_f32 v30, v30\n"
    "v_exp_f32 v31, v31\n"
    "s_nop 0\n"
    "v_cvt_pk_bf16_f32 v96, v16, v17\n"
    "v_cvt_pk_bf16_f32 v97, v18, v19\n"
    "v_cvt_pk_bf16_f32 v98, v20, v21\n"
    "v_cvt_pk_bf16_f32 v99, v22, v23\n"
    "v_cvt_pk_bf16_f32 v100, v24, v25\n"
    "v_cvt_pk_bf16_f32 v101, v26, v27\n"
    "v_cvt_pk_bf16_f32 v102, v28, v29\n"
    "v_cvt_pk_bf16_f32 v103, v30, v31\n"
    "s_waitcnt lgkmcnt(0)\n"
    "s_nop 1\n"
    "v_mfma_f32_32x32x16_bf16 v[128:143], v[64:67], v[96:99], v[128:143]\n"
    "v_mfma_f32_32x32x16_bf16 v[128:143], v[68:71], v[100:103], v[128:143]\n"
    "s_add_u32 s90, s90, 1\n"
    "s_cmp_le_u32 s90, 8\n"
    "s_nop 13\n"
    "s_cbranch_scc1 Lpv_47_%=\n"
    "s_nop 12\n"
    "Lfast_43_%=:\n"
    "s_nop 12\n"
    "v_mov_b32 v144, v120\n"
    "v_mov_b32 v145, v120\n"
    "v_mov_b32 v146, v136\n"
    "v_mov_b32 v147, v136\n"
    "v_permlane32_swap_b32 v144, v145\n"
    "s_nop 0\n"
    "v_permlane32_swap_b32 v146, v147\n"
    "v_rcp_f32 v145, v145\n"
    "v_rcp_f32 v147, v147\n"
    "v_pk_mul_f32 v[112:113], v[112:113], v[144:145] op_sel:[0,1] op_sel_hi:[1,1]\n"
    "v_pk_mul_f32 v[114:115], v[114:115], v[144:145] op_sel:[0,1] op_sel_hi:[1,1]\n"
    "v_pk_mul_f32 v[116:117], v[116:117], v[144:145] op_sel:[0,1] op_sel_hi:[1,1]\n"
    "v_pk_mul_f32 v[118:119], v[118:119], v[144:145] op_sel:[0,1] op_sel_hi:[1,1]\n"
    "v_pk_mul_f32 v[120:121], v[120:121], v[144:145] op_sel:[0,1] op_sel_hi:[1,1]\n"
    "v_pk_mul_f32 v[122:123], v[122:123], v[144:145] op_sel:[0,1] op_sel_hi:[1,1]\n"
    "v_pk_mul_f32 v[128:129], v[128:129], v[146:147] op_sel:[0,1] op_sel_hi:[1,1]\n"
    "v_pk_mul_f32 v[130:131], v[130:131], v[146:147] op_sel:[0,1] op_sel_hi:[1,1]\n"
    "v_pk_mul_f32 v[132:133], v[132:133], v[146:147] op_sel:[0,1] op_sel_hi:[1,1]\n"
    "v_pk_mul_f32 v[134:135], v[134:135], v[146:147] op_sel:[0,1] op_sel_hi:[1,1]\n"
    "v_pk_mul_f32 v[136:137], v[136:137], v[146:147] op_sel:[0,1] op_sel_hi:[1,1]\n"
    "v_pk_mul_f32 v[138:139], v[138:139], v[146:147] op_sel:[0,1] op_sel_hi:[1,1]\n"
    "v_cvt_pk_bf16_f32 v148, v112, v113\n"
    "v_cvt_pk_bf16_f32 v149, v114, v115\n"
    "v_cvt_pk_bf16_f32 v150, v116, v117\n"
    "v_cvt_pk_bf16_f32 v151, v118, v119\n"
    "v_cvt_pk_bf16_f32 v152, v120, v121\n"
    "v_cvt_pk_bf16_f32 v153, v122, v123\n"
    "v_cvt_pk_bf16_f32 v156, v128, v129\n"
    "v_cvt_pk_bf16_f32 v157, v130, v131\n"
    "v_cvt_pk_bf16_f32 v158, v132, v133\n"
    "v_cvt_pk_bf16_f32 v159, v134, v135\n"
    "v_cvt_pk_bf16_f32 v160, v136, v137\n"
    "v_cvt_pk_bf16_f32 v161, v138, v139\n"
    "v_cndmask_b32 v164, v152, v156, s[48:49]\n"
    "v_cndmask_b32 v165, v153, v157, s[48:49]\n"
    "v_mov_b32 v166, v158\n"
    "v_mov_b32 v167, v159\n"
    "v_mov_b32 v168, v156\n"
    "v_mov_b32 v169, v157\n"
    "v_mov_b32 v170, v160\n"
    "v_mov_b32 v171, v161\n"
    "s_mul_i32 s39, s27, 40960\n"
    "s_add_u32 s39, s39, s53\n"
    "s_add_u32 s39, s39, s42\n"
    "s_add_u32 s39, s39, 22171648\n"
    "buffer_store_dwordx4 v[148:151], v1, s[16:19], s39 offen sc1\n"
    "buffer_store_dwordx4 v[164:167], v1, s[16:19], s39 offen offset:1024 sc1\n"
    "buffer_store_dwordx4 v[168:171], v9, s[16:19], s39 offen offset:2048 sc1\n"
    "buffer_load_dwordx4 v1, s[16:19], s14 offen offset:1024 sc1 lds\n"
    "buffer_load_dwordx4 v1, s[16:19], s14 offen offset:2048 sc1 lds\n"
    "buffer_load_dwordx4 v1, s[16:19], s14 offen offset:3072 sc1 lds\n"
    "s_waitcnt lgkmcnt(0)\n"
    "s_barrier\n"
    "s_cmp_eq_u32 s26, 0\n"
    "s_cbranch_scc1 Lt1_50_%=\n"
    "s_add_u32 s35, s37, 32768\n"
    "s_sub_u32 s35, s35, s29\n"
    "s_sub_u32 s30, s93, 1024\n"
    "s_add_u32 s31, s30, s35\n"
    "s_add_u32 m0, s30, 98304\n"
    "s_add_u32 s14, s31, 0\n"
    "buffer_load_dwordx4 v1, s[16:19], s14 offen sc1 lds\n"
    "s_add_u32 m0, s30, 101376\n"
    "s_add_u32 s14, s31, 3072\n"
    "buffer_load_dwordx4 v1, s[16:19], s14 offen sc1 lds\n"
    "s_add_u32 m0, s30, 104448\n"
    "s_add_u32 s14, s31, 6144\n"
    "buffer_load_dwordx4 v1, s[16:19], s14 offen sc1 lds\n"
    "s_add_u32 m0, s30, 107520\n"
    "s_add_u32 s14, s31, 9216\n"
    "buffer_load_dwordx4 v1, s[16:19], s14 offen sc1 lds\n"
    "s_waitcnt vmcnt(7)\n"
    "s_branch Lt2_51_%=\n"
    "Lt1_50_%=:\n"
    "s_waitcnt vmcnt(0)\n"
    "Lt2_51_%=:\n"
    "s_barrier\n"
    "s_cmp_lg_u32 s26, 0\n"
    "s_cbranch_scc1 Lnotw0_52_%=\n"
    "s_lshl_b32 s36, s24, 10\n"
    "s_add_u32 s36, s36, s45\n"
    "s_add_u32 s36, s36, 0\n"
    "v_mov_b32 v208, 0\n"
    "v_mov_b32 v209, 1\n"
    "s_mov_b64 exec, 1\n"
    "s_mov_b32 s50, 0\n"
    "Lgol_55_%=:\n"
    "buffer_atomic_add v209, v208, s[16:19], s36 offen sc0\n"
    "s_waitcnt vmcnt(0)\n"
    "v_readfirstlane_b32 s52, v209\n"
    "s_cmp_eq_u32 s50, 0\n"
    "s_cselect_b32 s51, 1, 0\n"
    "s_add_u32 s52, s52, s51\n"
    "v_mov_b32 v209, 0\n"
    "s_add_u32 s50, s50, 1\n"
    "s_cmp_ge_u32 s52, 16\n"
    "s_cbranch_scc1 Lgod_54_%=\n"
    "s_cmp_lt_u32 s50, 20000\n"
    "s_cbranch_scc1 Lgol_55_%=\n"
    "Lgod_54_%=:\n"
    "s_mov_b64 exec, -1\n"
    "s_branch Ltj_53_%=\n"
    "Lnotw0_52_%=:\n"
    "s_add_u32 m0, s30, 110592\n"
    "s_add_u32 s14, s31, 12288\n"
    "buffer_load_dwordx4 v1, s[16:19], s14 offen sc1 lds\n"
    "s_add_u32 m0, s30, 113664\n"
    "s_add_u32 s14, s31, 15360\n"
    "buffer_load_dwordx4 v1, s[16:19], s14 offen sc1 lds\n"
    "s_add_u32 m0, s30, 116736\n"
    "s_add_u32 s14, s31, 18432\n"
    "buffer_load_dwordx4 v1, s[16:19], s14 offen sc1 lds\n"
    "s_add_u32 m0, s30, 119808\n"
    "s_add_u32 s14, s31, 21504\n"
    "buffer_load_dwordx4 v1, s[16:19], s14 offen sc1 lds\n"
    "s_add_u32 m0, s30, 122880\n"
    "s_add_u32 s14, s31, 24576\n"
    "buffer_load_dwordx4 v1, s[16:19], s14 offen sc1 lds\n"
    "s_add_u32 m0, s30, 125952\n"
    "s_add_u32 s14, s31, 27648\n"
    "buffer_load_dwordx4 v1, s[16:19], s14 offen sc1 lds\n"
    "s_add_u32 m0, s30, 129024\n"
    "s_add_u32 s14, s31, 30720\n"
    "buffer_load_dwordx4 v1, s[16:19], s14 offen sc1 lds\n"
    "s_add_u32 m0, s30, 132096\n"
    "s_add_u32 s14, s31, 33792\n"
    "buffer_load_dwordx4 v1, s[16:19], s14 offen sc1 lds\n"
    "s_add_u32 m0, s30, 135168\n"
    "s_add_u32 s14, s31, 36864\n"
    "buffer_load_dwordx4 v1, s[16:19], s14 offen sc1 lds\n"
    "s_add_u32 m0, s30, 138240\n"
    "s_add_u32 s14, s31, 39936\n"
    "buffer_load_dwordx4 v1, s[16:19], s14 offen sc1 lds\n"
    "s_add_u32 m0, s30, 141312\n"
    "s_add_u32 s14, s31, 43008\n"
    "buffer_load_dwordx4 v1, s[16:19], s14 offen sc1 lds\n"
    "s_add_u32 m0, s30, 144384\n"
    "s_add_u32 s14, s31, 46080\n"
    "buffer_load_dwordx4 v1, s[16:19], s14 offen sc1 lds\n"
    "s_waitcnt vmcnt(0)\n"
    "Ltj_53_%=:\n"
    "s_barrier\n"
    "s_mul_i32 s38, s27, 40960\n"
    "s_add_u32 s38, s38, s42\n"
    "s_add_u32 s38, s38, 22171648\n"
    "v_add_u32 v100, 32768, v1\n"
    "v_add_u32 v103, 32768, v218\n"
    "v_add_u32 v101, 98304, v1\n"
    "v_add_u32 v104, 98304, v218\n"
    "v_add_u32 v102, 163840, v1\n"
    "v_add_u32 v105, 163840, v218\n"
    "s_add_u32 s91, s38, 0\n"
    "buffer_load_dwordx4 a[0:3], v0, s[16:19], s91 offen offset:0 sc1\n"
    "buffer_load_dwordx4 a[4:7], v0, s[16:19], s91 offen offset:256 sc1\n"
    "buffer_load_dwordx4 a[8:11], v0, s[16:19], s91 offen offset:2048 sc1\n"
    "buffer_load_dwordx4 a[12:15], v0, s[16:19], s91 offen offset:2304 sc1\n"
    "s_add_u32 s91, s38, 4096\n"
    "buffer_load_dwordx4 a[16:19], v0, s[16:19], s91 offen offset:0 sc1\n"
    "buffer_load_dwordx4 a[20:23], v0, s[16:19], s91 offen offset:256 sc1\n"
    "buffer_load_dwordx4 a[24:27], v0, s[16:19], s91 offen offset:2048 sc1\n"
    "buffer_load_dwordx4 a[28:31], v0, s[16:19], s91 offen offset:2304 sc1\n"
    "s_add_u32 s91, s38, 8192\n"
    "buffer_load_dwordx4 a[32:35], v0, s[16:19], s91 offen offset:0 sc1\n"
    "buffer_load_dwordx4 a[36:39], v0, s[16:19], s91 offen offset:256 sc1\n"
    "buffer_load_dwordx4 a[40:43], v0, s[16:19], s91 offen offset:2048 sc1\n"
    "buffer_load_dwordx4 a[44:47], v0, s[16:19], s91 offen offset:2304 sc1\n"
    "s_add_u32 s91, s38, 12288\n"
    "buffer_load_dwordx4 a[48:51], v0, s[16:19], s91 offen offset:0 sc1\n"
    "buffer_load_dwordx4 a[52:55], v0, s[16:19], s91 offen offset:256 sc1\n"
    "v_mov_b32 v48, v240\n"
    "v_mov_b32 v49, v241\n"
    "v_mov_b32 v50, v242\n"
    "v_mov_b32 v51, v243\n"
    "v_mov_b32 v52, v244\n"
    "v_mov_b32 v53, v245\n"
    "v_mov_b32 v54, v246\n"
    "v_mov_b32 v55, v247\n"
    "v_mov_b32 v56, v248\n"
    "v_mov_b32 v57, v249\n"
    "v_mov_b32 v58, v250\n"
    "v_mov_b32 v59, v251\n"
    "v_permlane16_swap_b32 v48, v50\n"
    "v_permlane16_swap_b32 v49, v51\n"
    "v_permlane16_swap_b32 v52, v54\n"
    "v_permlane16_swap_b32 v53, v55\n"
    "v_permlane16_swap_b32 v56, v58\n"
    "v_permlane16_swap_b32 v57, v59\n"
    "v_lshlrev_b32 v16, 16, v48\n"
    "v_and_b32 v17, 0xffff0000, v48\n"
    "v_lshlrev_b32 v18, 16, v49\n"
    "v_and_b32 v19, 0xffff0000, v49\n"
    "v_lshlrev_b32 v20, 16, v50\n"
    "v_and_b32 v21, 0xffff0000, v50\n"
    "v_lshlrev_b32 v22, 16, v51\n"
    "v_and_b32 v23, 0xffff0000, v51\n"
    "v_lshlrev_b32 v24, 16, v52\n"
    "v_and_b32 v25, 0xffff0000, v52\n"
    "v_lshlrev_b32 v26, 16, v53\n"
    "v_and_b32 v27, 0xffff0000, v53\n"
    "v_lshlrev_b32 v28, 16, v54\n"
    "v_and_b32 v29, 0xffff0000, v54\n"
    "v_lshlrev_b32 v30, 16, v55\n"
    "v_and_b32 v31, 0xffff0000, v55\n"
    "v_lshlrev_b32 v32, 16, v56\n"
    "v_and_b32 v33, 0xffff0000, v56\n"
    "v_lshlrev_b32 v34, 16, v57\n"
    "v_and_b32 v35, 0xffff0000, v57\n"
    "v_lshlrev_b32 v36, 16, v58\n"
    "v_and_b32 v37, 0xffff0000, v58\n"
    "v_lshlrev_b32 v38, 16, v59\n"
    "v_and_b32 v39, 0xffff0000, v59\n"
    "ds_read_b128 v[168:171], v100 offset:0\n"
    "ds_read_b128 v[172:175], v100 offset:1024\n"
    "ds_read_b128 v[176:179], v103 offset:0\n"
    "ds_read_b128 v[180:183], v100 offset:2560\n"
    "ds_read_b128 v[184:187], v100 offset:3584\n"
    "ds_read_b128 v[188:191], v103 offset:2560\n"
    "s_waitcnt vmcnt(12) lgkmcnt(3)\n"
    "v_mfma_f32_16x16x32_bf16 v[16:19], v[168:171], a[0:3], v[16:19]\n"
    "ds_read_b128 v[192:195], v100 offset:5120\n"
    "buffer_load_dwordx4 a[56:59], v0, s[16:19], s91 offen offset:2048 sc1\n"
    "v_mfma_f32_16x16x32_bf16 v[20:23], v[168:171], a[4:7], v[20:23]\n"
    "v_mfma_f32_16x16x32_bf16 v[24:27], v[172:175], a[0:3], v[24:27]\n"
    "ds_read_b128 v[196:199], v100 offset:6144\n"
    "buffer_load_dwordx4 a[60:63], v0, s[16:19], s91 offen offset:2304 sc1\n"
    "v_mfma_f32_16x16x32_bf16 v[28:31], v[172:175], a[4:7], v[28:31]\n"
    "v_mfma_f32_16x16x32_bf16 v[32:35], v[176:179], a[0:3], v[32:35]\n"
    "ds_read_b128 v[200:203], v103 offset:5120\n"
    "v_mfma_f32_16x16x32_bf16 v[36:39], v[176:179], a[4:7], v[36:39]\n"
    "s_waitcnt vmcnt(12) lgkmcnt(3)\n"
    "v_mfma_f32_16x16x32_bf16 v[16:19], v[180:183], a[8:11], v[16:19]\n"
    "ds_read_b128 v[204:207], v100 offset:7680\n"
    "s_add_u32 s91, s38, 16384\n"
    "buffer_load_dwordx4 a[64:67], v0, s[16:19], s91 offen offset:0 sc1\n"
    "v_mfma_f32_16x16x32_bf16 v[20:23], v[180:183], a[12:15], v[20:23]\n"
    "v_mfma_f32_16x16x32_bf16 v[24:27], v[184:187], a[8:11], v[24:27]\n"
    "ds_read_b128 v[168:171], v100 offset:8704\n"
    "buffer_load_dwordx4 a[68:71], v0, s[16:19], s91 offen offset:256 sc1\n"
    "v_mfma_f32_16x16x32_bf16 v[28:31], v[184:187], a[12:15], v[28:31]\n"
    "v_mfma_f32_16x16x32_bf16 v[32:35], v[188:191], a[8:11], v[32:35]\n"
    "ds_read_b128 v[172:175], v103 offset:7680\n"
    "v_mfma_f32_16x16x32_bf16 v[36:39], v[188:191], a[12:15], v[36:39]\n"
    "s_waitcnt vmcnt(12) lgkmcnt(3)\n"
    "v_mfma_f32_16x16x32_bf16 v[16:19], v[192:195], a[16:19], v[16:19]\n"
    "ds_read_b128 v[176:179], v100 offset:10240\n"
    "buffer_load_dwordx4 a[72:75], v0, s[16:19], s91 offen offset:2048 sc1\n"
    "v_mfma_f32_16x16x32_bf16 v[20:23], v[192:195], a[20:23], v[20:23]\n"
    "v_mfma_f32_16x16x32_bf16 v[24:27], v[196:199], a[16:19], v[24:27]\n"
    "ds_read_b128 v[180:183], v100 offset:11264\n"
    "buffer_load_dwordx4 a[76:79], v0, s[16:19], s91 offen offset:2304 sc1\n"
    "v_mfma_f32_16x16x32_bf16 v[28:31], v[196:199], a[20:23], v[28:31]\n"
    "v_mfma_f32_16x16x32_bf16 v[32:35], v[200:203], a[16:19], v[32:35]\n"
    "ds_read_b128 v[184:187], v103 offset:10240\n"
    "v_mfma_f32_16x16x32_bf16 v[36:39], v[200:203], a[20:23], v[36:39]\n"
    "s_waitcnt vmcnt(12) lgkmcnt(3)\n"
    "v_mfma_f32_16x16x32_bf16 v[16:19], v[204:207], a[24:27], v[16:19]\n"
    "ds_read_b128 v[188:191], v100 offset:12800\n"
    "s_add_u32 s91, s38, 20480\n"
    "buffer_load_dwordx4 a[80:83], v0, s[16:19], s91 offen offset:0 sc1\n"
    "v_mfma_f32_16x16x32_bf16 v[20:23], v[204:207], a[28:31], v[20:23]\n"
    "v_mfma_f32_16x16x32_bf16 v[24:27], v[168:171], a[24:27], v[24:27]\n"
    "ds_read_b128 v[192:195], v100 offset:13824\n"
    "buffer_load_dwordx4 a[84:87], v0, s[16:19], s91 offen offset:256 sc1\n"
    "v_mfma_f32_16x16x32_bf16 v[28:31], v[168:171], a[28:31], v[28:31]\n"
    "v_mfma_f32_16x16x32_bf16 v[32:35], v[172:175], a[24:27], v[32:35]\n"
    "ds_read_b128 v[196:199], v103 offset:12800\n"
    "v_mfma_f32_16x16x32_bf16 v[36:39], v[172:175], a[28:31], v[36:39]\n"
    "s_waitcnt vmcnt(12) lgkmcnt(3)\n"
    "v_mfma_f32_16x16x32_bf16 v[16:19], v[176:179], a[32:35], v[16:19]\n"
    "ds_read_b128 v[200:203], v100 offset:15360\n"
    "buffer_load_dwordx4 a[88:91], v0, s[16:19], s91 offen offset:2048 sc1\n"
    "v_mfma_f32_16x16x32_bf16 v[20:23], v[176:179], a[36:39], v[20:23]\n"
    "v_mfma_f32_16x16x32_bf16 v[24:27], v[180:183], a[32:35], v[24:27]\n"
    "ds_read_b128 v[204:207], v100 offset:16384\n"
    "buffer_load_dwordx4 a[92:95], v0, s[16:19], s91 offen offset:2304 sc1\n"
    "v_mfma_f32_16x16x32_bf16 v[28:31], v[180:183], a[36:39], v[28:31]\n"
    "v_mfma_f32_16x16x32_bf16 v[32:35], v[184:187], a[32:35], v[32:35]\n"
    "ds_read_b128 v[168:171], v103 offset:15360\n"
    "v_mfma_f32_16x16x32_bf16 v[36:39], v[184:187], a[36:39], v[36:39]\n"
    "s_waitcnt vmcnt(12) lgkmcnt(3)\n"
    "v_mfma_f32_16x16x32_bf16 v[16:19], v[188:191], a[40:43], v[16:19]\n"
    "ds_read_b128 v[172:175], v100 offset:17920\n"
    "s_add_u32 s91, s38, 24576\n"
    "buffer_load_dwordx4 a[96:99], v0, s[16:19], s91 offen offset:0 sc1\n"
    "v_mfma_f32_16x16x32_bf16 v[20:23], v[188:191], a[44:47], v[20:23]\n"
    "v_mfma_f32_16x16x32_bf16 v[24:27], v[192:195], a[40:43], v[24:27]\n"
    "ds_read_b128 v[176:179], v100 offset:18944\n"
    "buffer_load_dwordx4 a[100:103], v0, s[16:19], s91 offen offset:256 sc1\n"
    "v_mfma_f32_16x16x32_bf16 v[28:31], v[192:195], a[44:47], v[28:31]\n"
    "v_mfma_f32_16x16x32_bf16 v[32:35], v[196:199], a[40:43], v[32:35]\n"
    "ds_read_b128 v[180:183], v103 offset:17920\n"
    "v_mfma_f32_16x16x32_bf16 v[36:39], v[196:199], a[44:47], v[36:39]\n"
    "s_waitcnt vmcnt(12) lgkmcnt(3)\n"
    "v_mfma_f32_16x16x32_bf16 v[16:19], v[200:203], a[48:51], v[16:19]\n"
    "ds_read_b128 v[184:187], v100 offset:20480\n"
    "buffer_load_dwordx4 a[104:107], v0, s[16:19], s91 offen offset:2048 sc1\n"
    "v_mfma_f32_16x16x32_bf16 v[20:23], v[200:203], a[52:55], v[20:23]\n"
    "v_mfma_f32_16x16x32_bf16 v[24:27], v[204:207], a[48:51], v[24:27]\n"
    "ds_read_b128 v[188:191], v100 offset:21504\n"
    "buffer_load_dwordx4 a[108:111], v0, s[16:19], s91 offen offset:2304 sc1\n"
    "v_mfma_f32_16x16x32_bf16 v[28:31], v[204:207], a[52:55], v[28:31]\n"
    "v_mfma_f32_16x16x32_bf16 v[32:35], v[168:171], a[48:51], v[32:35]\n"
    "ds_read_b128 v[192:195], v103 offset:20480\n"
    "v_mfma_f32_16x16x32_bf16 v[36:39], v[168:171], a[52:55], v[36:39]\n"
    "s_waitcnt vmcnt(12) lgkmcnt(3)\n"
    "v_mfma_f32_16x16x32_bf16 v[16:19], v[172:175], a[56:59], v[16:19]\n"
    "ds_read_b128 v[196:199], v100 offset:23040\n"
    "s_add_u32 s91, s38, 28672\n"
    "buffer_load_dwordx4 a[112:115], v0, s[16:19], s91 offen offset:0 sc1\n"
    "v_mfma_f32_16x16x32_bf16 v[20:23], v[172:175], a[60:63], v[20:23]\n"
    "v_mfma_f32_16x16x32_bf16 v[24:27], v[176:179], a[56:59], v[24:27]\n"
    "ds_read_b128 v[200:203], v100 offset:24064\n"
    "buffer_load_dwordx4 a[116:119], v0, s[16:19], s91 offen offset:256 sc1\n"
    "v_mfma_f32_16x16x32_bf16 v[28:31], v[176:179], a[60:63], v[28:31]\n"
    "v_mfma_f32_16x16x32_bf16 v[32:35], v[180:183], a[56:59], v[32:35]\n"
    "ds_read_b128 v[204:207], v103 offset:23040\n"
    "v_mfma_f32_16x16x32_bf16 v[36:39], v[180:183], a[60:63], v[36:39]\n"
    "s_waitcnt vmcnt(12) lgkmcnt(3)\n"
    "v_mfma_f32_16x16x32_bf16 v[16:19], v[184:187], a[64:67], v[16:19]\n"
    "ds_read_b128 v[168:171], v100 offset:25600\n"
    "buffer_load_dwordx4 a[120:123], v0, s[16:19], s91 offen offset:2048 sc1\n"
    "v_mfma_f32_16x16x32_bf16 v[20:23], v[184:187], a[68:71], v[20:23]\n"
    "v_mfma_f32_16x16x32_bf16 v[24:27], v[188:191], a[64:67], v[24:27]\n"
    "ds_read_b128 v[172:175], v100 offset:26624\n"
    "buffer_load_dwordx4 a[124:127], v0, s[16:19], s91 offen offset:2304 sc1\n"
    "v_mfma_f32_16x16x32_bf16 v[28:31], v[188:191], a[68:71], v[28:31]\n"
    "v_mfma_f32_16x16x32_bf16 v[32:35], v[192:195], a[64:67], v[32:35]\n"
    "ds_read_b128 v[176:179], v103 offset:25600\n"
    "v_mfma_f32_16x16x32_bf16 v[36:39], v[192:195], a[68:71], v[36:39]\n"
    "s_waitcnt vmcnt(12) lgkmcnt(3)\n"
    "v_mfma_f32_16x16x32_bf16 v[16:19], v[196:199], a[72:75], v[16:19]\n"
    "ds_read_b128 v[180:183], v100 offset:28160\n"
    "s_add_u32 s91, s38, 32768\n"
    "buffer_load_dwordx4 a[128:131], v0, s[16:19], s91 offen offset:0 sc1\n"
    "v_mfma_f32_16x16x32_bf16 v[20:23], v[196:199], a[76:79], v[20:23]\n"
    "v_mfma_f32_16x16x32_bf16 v[24:27], v[200:203], a[72:75], v[24:27]\n"
    "ds_read_b128 v[184:187], v100 offset:29184\n"
    "buffer_load_dwordx4 a[132:135], v0, s[16:19], s91 offen offset:256 sc1\n"
    "v_mfma_f32_16x16x32_bf16 v[28:31], v[200:203], a[76:79], v[28:31]\n"
    "v_mfma_f32_16x16x32_bf16 v[32:35], v[204:207], a[72:75], v[32:35]\n"
    "ds_read_b128 v[188:191], v103 offset:28160\n"
    "v_mfma_f32_16x16x32_bf16 v[36:39], v[204:207], a[76:79], v[36:39]\n"
    "s_waitcnt vmcnt(12) lgkmcnt(3)\n"
    "v_mfma_f32_16x16x32_bf16 v[16:19], v[168:171], a[80:83], v[16:19]\n"
    "ds_read_b128 v[192:195], v100 offset:30720\n"
    "buffer_load_dwordx4 a[136:139], v0, s[16:19], s91 offen offset:2048 sc1\n"
    "v_mfma_f32_16x16x32_bf16 v[20:23], v[168:171], a[84:87], v[20:23]\n"
    "v_mfma_f32_16x16x32_bf16 v[24:27], v[172:175], a[80:83], v[24:27]\n"
    "ds_read_b128 v[196:199], v100 offset:31744\n"
    "buffer_load_dwordx4 a[140:143], v0, s[16:19], s91 offen offset:2304 sc1\n"
    "v_mfma_f32_16x16x32_bf16 v[28:31], v[172:175], a[84:87], v[28:31]\n"
    "v_mfma_f32_16x16x32_bf16 v[32:35], v[176:179], a[80:83], v[32:35]\n"
    "ds_read_b128 v[200:203], v103 offset:30720\n"
    "v_mfma_f32_16x16x32_bf16 v[36:39], v[176:179], a[84:87], v[36:39]\n"
    "s_waitcnt vmcnt(12) lgkmcnt(3)\n"
    "v_mfma_f32_16x16x32_bf16 v[16:19], v[180:183], a[88:91], v[16:19]\n"
    "ds_read_b128 v[204:207], v100 offset:33280\n"
    "s_add_u32 s91, s38, 36864\n"
    "buffer_load_dwordx4 a[144:147], v0, s[16:19], s91 offen offset:0 sc1\n"
    "v_mfma_f32_16x16x32_bf16 v[20:23], v[180:183], a[92:95], v[20:23]\n"
    "v_mfma_f32_16x16x32_bf16 v[24:27], v[184:187], a[88:91], v[24:27]\n"
    "ds_read_b128 v[168:171], v100 offset:34304\n"
    "buffer_load_dwordx4 a[148:151], v0, s[16:19], s91 offen offset:256 sc1\n"
    "v_mfma_f32_16x16x32_bf16 v[28:31], v[184:187], a[92:95], v[28:31]\n"
    "v_mfma_f32_16x16x32_bf16 v[32:35], v[188:191], a[88:91], v[32:35]\n"
    "ds_read_b128 v[172:175], v103 offset:33280\n"
    "v_mfma_f32_16x16x32_bf16 v[36:39], v[188:191], a[92:95], v[36:39]\n"
    "s_waitcnt vmcnt(12) lgkmcnt(3)\n"
    "v_mfma_f32_16x16x32_bf16 v[16:19], v[192:195], a[96:99], v[16:19]\n"
    "ds_read_b128 v[176:179], v100 offset:35840\n"
    "buffer_load_dwordx4 a[152:155], v0, s[16:19], s91 offen offset:2048 sc1\n"
    "v_mfma_f32_16x16x32_bf16 v[20:23], v[192:195], a[100:103], v[20:23]\n"
    "v_mfma_f32_16x16x32_bf16 v[24:27], v[196:199], a[96:99], v[24:27]\n"
    "ds_read_b128 v[180:183], v100 offset:36864\n"
    "buffer_load_dwordx4 a[156:159], v0, s[16:19], s91 offen offset:2304 sc1\n"
    "v_mfma_f32_16x16x32_bf16 v[28:31], v[196:199], a[100:103], v[28:31]\n"
    "v_mfma_f32_16x16x32_bf16 v[32:35], v[200:203], a[96:99], v[32:35]\n"
    "ds_read_b128 v[184:187], v103 offset:35840\n"
    "v_mfma_f32_16x16x32_bf16 v[36:39], v[200:203], a[100:103], v[36:39]\n"
    "s_waitcnt vmcnt(12) lgkmcnt(3)\n"
    "v_mfma_f32_16x16x32_bf16 v[16:19], v[204:207], a[104:107], v[16:19]\n"
    "ds_read_b128 v[188:191], v100 offset:38400\n"
    "v_mfma_f32_16x16x32_bf16 v[20:23], v[204:207], a[108:111], v[20:23]\n"
    "v_mfma_f32_16x16x32_bf16 v[24:27], v[168:171], a[104:107], v[24:27]\n"
    "ds_read_b128 v[192:195], v100 offset:39424\n"
    "v_mfma_f32_16x16x32_bf16 v[28:31], v[168:171], a[108:111], v[28:31]\n"
    "v_mfma_f32_16x16x32_bf16 v[32:35], v[172:175], a[104:107], v[32:35]\n"
    "ds_read_b128 v[196:199], v103 offset:38400\n"
    "v_mfma_f32_16x16x32_bf16 v[36:39], v[172:175], a[108:111], v[36:39]\n"
    "s_waitcnt vmcnt(10) lgkmcnt(3)\n"
    "v_mfma_f32_16x16x32_bf16 v[16:19], v[176:179], a[112:115], v[16:19]\n"
    "ds_read_b128 v[200:203], v100 offset:40960\n"
    "v_mfma_f32_16x16x32_bf16 v[20:23], v[176:179], a[116:119], v[20:23]\n"
    "v_mfma_f32_16x16x32_bf16 v[24:27], v[180:183], a[112:115], v[24:27]\n"
    "ds_read_b128 v[204:207], v100 offset:41984\n"
    "v_mfma_f32_16x16x32_bf16 v[28:31], v[180:183], a[116:119], v[28:31]\n"
    "v_mfma_f32_16x16x32_bf16 v[32:35], v[184:187], a[112:115], v[32:35]\n"
    "ds_read_b128 v[168:171], v103 offset:40960\n"
    "v_mfma_f32_16x16x32_bf16 v[36:39], v[184:187], a[116:119], v[36:39]\n"
    "s_waitcnt vmcnt(8) lgkmcnt(3)\n"
    "v_mfma_f32_16x16x32_bf16 v[16:19], v[188:191], a[120:123], v[16:19]\n"
    "ds_read_b128 v[172:175], v100 offset:43520\n"
    "v_mfma_f32_16x16x32_bf16 v[20:23], v[188:191], a[124:127], v[20:23]\n"
    "v_mfma_f32_16x16x32_bf16 v[24:27], v[192:195], a[120:123], v[24:27]\n"
    "ds_read_b128 v[176:179], v100 offset:44544\n"
    "v_mfma_f32_16x16x32_bf16 v[28:31], v[192:195], a[124:127], v[28:31]\n"
    "v_mfma_f32_16x16x32_bf16 v[32:35], v[196:199], a[120:123], v[32:35]\n"
    "ds_read_b128 v[180:183], v103 offset:43520\n"
    "v_mfma_f32_16x16x32_bf16 v[36:39], v[196:199], a[124:127], v[36:39]\n"
    "s_waitcnt vmcnt(6) lgkmcnt(3)\n"
    "v_mfma_f32_16x16x32_bf16 v[16:19], v[200:203], a[128:131], v[16:19]\n"
    "ds_read_b128 v[184:187], v100 offset:46080\n"
    "v_mfma_f32_16x16x32_bf16 v[20:23], v[200:203], a[132:135], v[20:23]\n"
    "v_mfma_f32_16x16x32_bf16 v[24:27], v[204:207], a[128:131], v[24:27]\n"
    "ds_read_b128 v[188:191], v100 offset:47104\n"
    "v_mfma_f32_16x16x32_bf16 v[28:31], v[204:207], a[132:135], v[28:31]\n"
    "v_mfma_f32_16x16x32_bf16 v[32:35], v[168:171], a[128:131], v[32:35]\n"
    "ds_read_b128 v[192:195], v103 offset:46080\n"
    "v_mfma_f32_16x16x32_bf16 v[36:39], v[168:171], a[132:135], v[36:39]\n"
    "s_waitcnt vmcnt(4) lgkmcnt(3)\n"
    "v_mfma_f32_16x16x32_bf16 v[16:19], v[172:175], a[136:139], v[16:19]\n"
    "ds_read_b128 v[196:199], v100 offset:48640\n"
    "v_mfma_f32_16x16x32_bf16 v[20:23], v[172:175], a[140:143], v[20:23]\n"
    "v_mfma_f32_16x16x32_bf16 v[24:27], v[176:179], a[136:139], v[24:27]\n"
    "ds_read_b128 v[200:203], v100 offset:49664\n"
    "v_mfma_f32_16x16x32_bf16 v[28:31], v[176:179], a[140:143], v[28:31]\n"
    "v_mfma_f32_16x16x32_bf16 v[32:35], v[180:183], a[136:139], v[32:35]\n"
    "ds_read_b128 v[204:207], v103 offset:48640\n"
    "v_mfma_f32_16x16x32_bf16 v[36:39], v[180:183], a[140:143], v[36:39]\n"
    "s_waitcnt vmcnt(2) lgkmcnt(3)\n"
    "v_mfma_f32_16x16x32_bf16 v[16:19], v[184:187], a[144:147], v[16:19]\n"
    "v_mfma_f32_16x16x32_bf16 v[20:23], v[184:187], a[148:151], v[20:23]\n"
    "v_mfma_f32_16x16x32_bf16 v[24:27], v[188:191], a[144:147], v[24:27]\n"
    "v_mfma_f32_16x16x32_bf16 v[28:31], v[188:191], a[148:151], v[28:31]\n"
    "v_mfma_f32_16x16x32_bf16 v[32:35], v[192:195], a[144:147], v[32:35]\n"
    "v_mfma_f32_16x16x32_bf16 v[36:39], v[192:195], a[148:151], v[36:39]\n"
    "s_waitcnt vmcnt(0) lgkmcnt(0)\n"
    "v_mfma_f32_16x16x32_bf16 v[16:19], v[196:199], a[152:155], v[16:19]\n"
    "v_mfma_f32_16x16x32_bf16 v[20:23], v[196:199], a[156:159], v[20:23]\n"
    "v_mfma_f32_16x16x32_bf16 v[24:27], v[200:203], a[152:155], v[24:27]\n"
    "v_mfma_f32_16x16x32_bf16 v[28:31], v[200:203], a[156:159], v[28:31]\n"
    "v_mfma_f32_16x16x32_bf16 v[32:35], v[204:207], a[152:155], v[32:35]\n"
    "v_mfma_f32_16x16x32_bf16 v[36:39], v[204:207], a[156:159], v[36:39]\n"
    "s_nop 2\n"
    "v_cvt_pk_bf16_f32 v240, v16, v17\n"
    "v_cvt_pk_bf16_f32 v241, v18, v19\n"
    "v_cvt_pk_bf16_f32 v242, v20, v21\n"
    "v_cvt_pk_bf16_f32 v243, v22, v23\n"
    "v_cvt_pk_bf16_f32 v244, v24, v25\n"
    "v_cvt_pk_bf16_f32 v245, v26, v27\n"
    "v_cvt_pk_bf16_f32 v246, v28, v29\n"
    "v_cvt_pk_bf16_f32 v247, v30, v31\n"
    "v_cvt_pk_bf16_f32 v248, v32, v33\n"
    "v_cvt_pk_bf16_f32 v249, v34, v35\n"
    "v_cvt_pk_bf16_f32 v250, v36, v37\n"
    "v_cvt_pk_bf16_f32 v251, v38, v39\n"
    "v_cndmask_b32 v248, v248, 0, s[48:49]\n"
    "v_cndmask_b32 v249, v249, 0, s[48:49]\n"
    "v_cndmask_b32 v250, v250, 0, s[48:49]\n"
    "v_cndmask_b32 v251, v251, 0, s[48:49]\n"
    "v_mov_b32 v64, v240\n"
    "v_mov_b32 v65, v241\n"
    "v_mov_b32 v66, v242\n"
    "v_mov_b32 v67, v243\n"
    "v_mov_b32 v68, v244\n"
    "v_mov_b32 v69, v245\n"
    "v_mov_b32 v70, v246\n"
    "v_mov_b32 v71, v247\n"
    "v_mov_b32 v72, v248\n"
    "v_mov_b32 v73, v249\n"
    "v_mov_b32 v74, v250\n"
    "v_mov_b32 v75, v251\n"
    "v_permlane16_swap_b32 v240, v242\n"
    "v_permlane16_swap_b32 v241, v243\n"
    "v_permlane16_swap_b32 v244, v246\n"
    "v_permlane16_swap_b32 v245, v247\n"
    "v_permlane16_swap_b32 v248, v250\n"
    "v_permlane16_swap_b32 v249, v251\n"
    "s_mul_i32 s39, s27, 40960\n"
    "s_add_u32 s39, s39, s53\n"
    "s_add_u32 s39, s39, s40\n"
    "s_add_u32 s39, s39, 13258752\n"
    "buffer_store_dwordx4 v[240:243], v1, s[16:19], s39 offen sc1\n"
    "buffer_store_dwordx4 v[244:247], v1, s[16:19], s39 offen offset:1024 sc1\n"
    "buffer_store_dwordx4 v[248:251], v9, s[16:19], s39 offen offset:2048 sc1\n"
    "v_dot2_f32_bf16 v48, v64, v222, 0\n"
    "v_dot2_f32_bf16 v49, v64, v64, 0\n"
    "v_dot2_f32_bf16 v48, v65, v222, v48\n"
    "v_dot2_f32_bf16 v49, v65, v65, v49\n"
    "v_dot2_f32_bf16 v48, v68, v222, v48\n"
    "v_dot2_f32_bf16 v49, v68, v68, v49\n"
    "v_dot2_f32_bf16 v48, v69, v222, v48\n"
    "v_dot2_f32_bf16 v49, v69, v69, v49\n"
    "v_dot2_f32_bf16 v48, v72, v222, v48\n"
    "v_dot2_f32_bf16 v49, v72, v72, v49\n"
    "v_dot2_f32_bf16 v48, v73, v222, v48\n"
    "v_dot2_f32_bf16 v49, v73, v73, v49\n"
    "v_dot2_f32_bf16 v50, v66, v222, 0\n"
    "v_dot2_f32_bf16 v51, v66, v66, 0\n"
    "v_dot2_f32_bf16 v50, v67, v222, v50\n"
    "v_dot2_f32_bf16 v51, v67, v67, v51\n"
    "v_dot2_f32_bf16 v50, v70, v222, v50\n"
    "v_dot2_f32_bf16 v51, v70, v70, v51\n"
    "v_dot2_f32_bf16 v50, v71, v222, v50\n"
    "v_dot2_f32_bf16 v51, v71, v71, v51\n"
    "v_dot2_f32_bf16 v50, v74, v222, v50\n"
    "v_dot2_f32_bf16 v51, v74, v74, v51\n"
    "v_dot2_f32_bf16 v50, v75, v222, v50\n"
    "v_dot2_f32_bf16 v51, v75, v75, v51\n"
    "v_mov_b32 v52, v48\n"
    "v_mov_b32 v53, v49\n"
    "v_mov_b32 v54, v50\n"
    "v_mov_b32 v55, v51\n"
    "v_permlane32_swap_b32 v48, v52\n"
    "v_permlane32_swap_b32 v49, v53\n"
    "v_permlane32_swap_b32 v50, v54\n"
    "v_permlane32_swap_b32 v51, v55\n"
    "v_add_f32 v48, v48, v52\n"
    "v_add_f32 v49, v49, v53\n"
    "v_add_f32 v50, v50, v54\n"
    "v_add_f32 v51, v51, v55\n"
    "v_mov_b32 v52, v48\n"
    "v_mov_b32 v53, v49\n"
    "v_mov_b32 v54, v50\n"
    "v_mov_b32 v55, v51\n"
    "v_permlane16_swap_b32 v48, v52\n"
    "v_permlane16_swap_b32 v49, v53\n"
    "v_permlane16_swap_b32 v50, v54\n"
    "v_permlane16_swap_b32 v51, v55\n"
    "v_add_f32 v48, v48, v52\n"
    "v_add_f32 v49, v49, v53\n"
    "v_add_f32 v50, v50, v54\n"
    "v_add_f32 v51, v51, v55\n"
    "v_cndmask_b32 v48, v48, v50, s[8:9]\n"
    "v_cndmask_b32 v49, v49, v51, s[8:9]\n"
    "s_lshl_b32 s31, s27, 4\n"
    "s_add_u32 s31, s31, s25\n"
    "s_lshl_b32 s31, s31, 8\n"
    "s_add_u32 s31, s31, s41\n"
    "s_add_u32 s31, s31, 21385216\n"
    "v_lshrrev_b32 v52, 1, v9\n"
    "buffer_store_dwordx2 v[48:49], v52, s[16:19], s31 offen sc1\n"
    "s_waitcnt lgkmcnt(0)\n"
    "s_barrier\n"
    "s_cmp_eq_u32 s26, 0\n"
    "s_cbranch_scc1 Lt1_56_%=\n"
    "s_add_u32 s35, s37, 81920\n"
    "s_sub_u32 s35, s35, s29\n"
    "s_sub_u32 s30, s93, 1024\n"
    "s_add_u32 s31, s30, s35\n"
    "s_add_u32 m0, s30, 32768\n"
    "s_add_u32 s14, s31, 0\n"
    "buffer_load_dwordx4 v1, s[16:19], s14 offen sc1 lds\n"
    "s_add_u32 m0, s30, 35840\n"
    "s_add_u32 s14, s31, 3072\n"
    "buffer_load_dwordx4 v1, s[16:19], s14 offen sc1 lds\n"
    "s_add_u32 m0, s30, 38912\n"
    "s_add_u32 s14, s31, 6144\n"
    "buffer_load_dwordx4 v1, s[16:19], s14 offen sc1 lds\n"
    "s_add_u32 m0, s30, 41984\n"
    "s_add_u32 s14, s31, 9216\n"
    "buffer_load_dwordx4 v1, s[16:19], s14 offen sc1 lds\n"
    "s_waitcnt vmcnt(4)\n"
    "s_branch Lt2_57_%=\n"
    "Lt1_56_%=:\n"
    "s_waitcnt vmcnt(0)\n"
    "Lt2_57_%=:\n"
    "s_barrier\n"
    "s_cmp_lg_u32 s26, 0\n"
    "s_cbranch_scc1 Lnotw0_58_%=\n"
    "s_lshl_b32 s36, s24, 10\n"
    "s_add_u32 s36, s36, s45\n"
    "s_add_u32 s36, s36, 16384\n"
    "v_mov_b32 v208, 0\n"
    "v_mov_b32 v209, 1\n"
    "s_mov_b64 exec, 1\n"
    "s_mov_b32 s50, 0\n"
    "Lgol_61_%=:\n"
    "buffer_atomic_add v209, v208, s[16:19], s36 offen sc0\n"
    "s_waitcnt vmcnt(0)\n"
    "v_readfirstlane_b32 s52, v209\n"
    "s_cmp_eq_u32 s50, 0\n"
    "s_cselect_b32 s51, 1, 0\n"
    "s_add_u32 s52, s52, s51\n"
    "v_mov_b32 v209, 0\n"
    "s_add_u32 s50, s50, 1\n"
    "s_cmp_ge_u32 s52, 16\n"
    "s_cbranch_scc1 Lgod_60_%=\n"
    "s_cmp_lt_u32 s50, 20000\n"
    "s_cbranch_scc1 Lgol_61_%=\n"
    "Lgod_60_%=:\n"
    "s_mov_b64 exec, -1\n"
    "s_branch Ltj_59_%=\n"
    "Lnotw0_58_%=:\n"
    "s_add_u32 m0, s30, 45056\n"
    "s_add_u32 s14, s31, 12288\n"
    "buffer_load_dwordx4 v1, s[16:19], s14 offen sc1 lds\n"
    "s_add_u32 m0, s30, 48128\n"
    "s_add_u32 s14, s31, 15360\n"
    "buffer_load_dwordx4 v1, s[16:19], s14 offen sc1 lds\n"
    "s_add_u32 m0, s30, 51200\n"
    "s_add_u32 s14, s31, 18432\n"
    "buffer_load_dwordx4 v1, s[16:19], s14 offen sc1 lds\n"
    "s_add_u32 m0, s30, 54272\n"
    "s_add_u32 s14, s31, 21504\n"
    "buffer_load_dwordx4 v1, s[16:19], s14 offen sc1 lds\n"
    "s_add_u32 m0, s30, 57344\n"
    "s_add_u32 s14, s31, 24576\n"
    "buffer_load_dwordx4 v1, s[16:19], s14 offen sc1 lds\n"
    "s_add_u32 m0, s30, 60416\n"
    "s_add_u32 s14, s31, 27648\n"
    "buffer_load_dwordx4 v1, s[16:19], s14 offen sc1 lds\n"
    "s_add_u32 m0, s30, 63488\n"
    "s_add_u32 s14, s31, 30720\n"
    "buffer_load_dwordx4 v1, s[16:19], s14 offen sc1 lds\n"
    "s_add_u32 m0, s30, 66560\n"
    "s_add_u32 s14, s31, 33792\n"
    "buffer_load_dwordx4 v1, s[16:19], s14 offen sc1 lds\n"
    "s_add_u32 m0, s30, 69632\n"
    "s_add_u32 s14, s31, 36864\n"
    "buffer_load_dwordx4 v1, s[16:19], s14 offen sc1 lds\n"
    "s_add_u32 m0, s30, 72704\n"
    "s_add_u32 s14, s31, 39936\n"
    "buffer_load_dwordx4 v1, s[16:19], s14 offen sc1 lds\n"
    "s_add_u32 m0, s30, 75776\n"
    "s_add_u32 s14, s31, 43008\n"
    "buffer_load_dwordx4 v1, s[16:19], s14 offen sc1 lds\n"
    "s_add_u32 m0, s30, 78848\n"
    "s_add_u32 s14, s31, 46080\n"
    "buffer_load_dwordx4 v1, s[16:19], s14 offen sc1 lds\n"
    "s_add_u32 m0, s30, 81920\n"
    "s_add_u32 s14, s31, 49152\n"
    "buffer_load_dwordx4 v1, s[16:19], s14 offen sc1 lds\n"
    "s_add_u32 m0, s30, 84992\n"
    "s_add_u32 s14, s31, 52224\n"
    "buffer_load_dwordx4 v1, s[16:19], s14 offen sc1 lds\n"
    "s_add_u32 m0, s30, 88064\n"
    "s_add_u32 s14, s31, 55296\n"
    "buffer_load_dwordx4 v1, s[16:19], s14 offen sc1 lds\n"
    "s_add_u32 m0, s30, 91136\n"
    "s_add_u32 s14, s31, 58368\n"
    "buffer_load_dwordx4 v1, s[16:19], s14 offen sc1 lds\n"
    "s_add_u32 m0, s30, 94208\n"
    "s_add_u32 s14, s31, 61440\n"
    "buffer_load_dwordx4 v1, s[16:19], s14 offen sc1 lds\n"
    "s_add_u32 m0, s30, 95232\n"
    "s_add_u32 s14, s31, 62464\n"
    "buffer_load_dwordx4 v1, s[16:19], s14 offen sc1 lds\n"
    "s_waitcnt vmcnt(0)\n"
    "Ltj_59_%=:\n"
    "s_barrier\n"
    "s_mul_i32 s94, s25, 138240\n"
    "s_add_u32 s94, s94, 8310784\n"
    "s_add_u32 s94, s94, s93\n"
    "s_mul_i32 s38, s27, 40960\n"
    "s_add_u32 s38, s38, s40\n"
    "s_add_u32 s38, s38, 13258752\n"
    "s_mul_i32 s39, s27, 110592\n"
    "s_add_u32 s39, s39, s43\n"
    "s_add_u32 s39, s39, 27414528\n"
    "s_mul_i32 s31, s25, 3\n"
    "s_lshl_b32 s35, s25, 1\n"
    "s_add_u32 s51, s35, 12\n"
    "s_cmp_lt_u32 s25, 12\n"
    "s_cselect_b32 s31, s31, s51\n"
    "s_add_u32 s31, s31, 60\n"
    "s_lshl_b32 s31, s31, 10\n"
    "s_add_u32 s90, s39, s31\n"
    "s_lshl_b32 s35, s35, 10\n"
    "s_add_u32 s39, s39, s35\n"
    "s_lshl_b32 s35, s11, 2\n"
    "s_add_u32 s35, s35, s26\n"
    "s_lshl_b32 s35, s35, 10\n"
    "s_add_u32 s35, s35, 54222848\n"
    "s_add_u32 s31, s90, 6144\n"
    "s_cmp_lt_u32 s25, 12\n"
    "s_cselect_b32 s13, s31, s35\n"
    "s_add_u32 s15, s38, 0\n"
    "buffer_load_dwordx4 a[0:3], v1, s[16:19], s15 offen offset:0 sc1\n"
    "buffer_load_dwordx4 a[4:7], v1, s[16:19], s15 offen offset:1024 sc1\n"
    "buffer_load_dwordx4 a[8:11], v1, s[16:19], s15 offen offset:2048 sc1\n"
    "buffer_load_dwordx4 a[12:15], v1, s[16:19], s15 offen offset:3072 sc1\n"
    "s_add_u32 s15, s38, 4096\n"
    "buffer_load_dwordx4 a[16:19], v1, s[16:19], s15 offen offset:0 sc1\n"
    "buffer_load_dwordx4 a[20:23], v1, s[16:19], s15 offen offset:1024 sc1\n"
    "buffer_load_dwordx4 a[24:27], v1, s[16:19], s15 offen offset:2048 sc1\n"
    "buffer_load_dwordx4 a[28:31], v1, s[16:19], s15 offen offset:3072 sc1\n"
    "s_add_u32 s15, s38, 8192\n"
    "buffer_load_dwordx4 a[32:35], v1, s[16:19], s15 offen offset:0 sc1\n"
    "buffer_load_dwordx4 a[36:39], v1, s[16:19], s15 offen offset:1024 sc1\n"
    "buffer_load_dwordx4 a[40:43], v1, s[16:19], s15 offen offset:2048 sc1\n"
    "buffer_load_dwordx4 a[44:47], v1, s[16:19], s15 offen offset:3072 sc1\n"
    "ds_read_b128 v[168:171], v1 offset:0\n"
    "ds_read_b128 v[172:175], v1 offset:1024\n"
    "ds_read_b128 v[176:179], v1 offset:2048\n"
    "ds_read_b128 v[180:183], v1 offset:3072\n"
    "ds_read_b128 v[184:187], v1 offset:4096\n"
    "ds_read_b128 v[188:191], v1 offset:5120\n"
    "ds_read_b128 v[192:195], v1 offset:6144\n"
    "ds_read_b128 v[196:199], v1 offset:7168\n"
    "s_waitcnt vmcnt(11) lgkmcnt(4)\n"
    "v_mfma_f32_32x32x16_bf16 v[16:31], v[168:171], a[0:3], 0\n"
    "ds_read_b128 v[200:203], v1 offset:8192\n"
    "v_mfma_f32_32x32x16_bf16 v[32:47], v[172:175], a[0:3], 0\n"
    "ds_read_b128 v[204:207], v1 offset:9216\n"
    "s_add_u32 s15, s38, 12288\n"
    "buffer_load_dwordx4 a[48:51], v1, s[16:19], s15 offen offset:0 sc1\n"
    "v_mfma_f32_32x32x16_bf16 v[48:63], v[176:179], a[0:3], 0\n"
    "ds_read_b128 v[168:171], v1 offset:10240\n"
    "s_waitcnt vmcnt(11)\n"
    "v_mfma_f32_32x32x16_bf16 v[16:31], v[180:183], a[4:7], v[16:31]\n"
    "ds_read_b128 v[172:175], v1 offset:11264\n"
    "s_waitcnt lgkmcnt(4)\n"
    "v_mfma_f32_32x32x16_bf16 v[32:47], v[184:187], a[4:7], v[32:47]\n"
    "ds_read_b128 v[176:179], v1 offset:12288\n"
    "buffer_load_dwordx4 a[52:55], v1, s[16:19], s15 offen offset:1024 sc1\n"
    "v_mfma_f32_32x32x16_bf16 v[48:63], v[188:191], a[4:7], v[48:63]\n"
    "ds_read_b128 v[180:183], v1 offset:13312\n"
    "s_waitcnt vmcnt(11)\n"
    "v_mfma_f32_32x32x16_bf16 v[16:31], v[192:195], a[8:11], v[16:31]\n"
    "ds_read_b128 v[184:187], v1 offset:14336\n"
    "v_mfma_f32_32x32x16_bf16 v[32:47], v[196:199], a[8:11], v[32:47]\n"
    "ds_read_b128 v[188:191], v1 offset:15360\n"
    "buffer_load_dwordx4 a[56:59], v1, s[16:19], s15 offen offset:2048 sc1\n"
    "s_waitcnt lgkmcnt(4)\n"
    "v_mfma_f32_32x32x16_bf16 v[48:63], v[200:203], a[8:11], v[48:63]\n"
    "ds_read_b128 v[192:195], v1 offset:16384\n"
    "s_waitcnt vmcnt(11)\n"
    "v_mfma_f32_32x32x16_bf16 v[16:31], v[204:207], a[12:15], v[16:31]\n"
    "ds_read_b128 v[196:199], v1 offset:17408\n"
    "v_mfma_f32_32x32x16_bf16 v[32:47], v[168:171], a[12:15], v[32:47]\n"
    "ds_read_b128 v[200:203], v1 offset:18432\n"
    "buffer_load_dwordx4 a[60:63], v1, s[16:19], s15 offen offset:3072 sc1\n"
    "v_mfma_f32_32x32x16_bf16 v[48:63], v[172:175], a[12:15], v[48:63]\n"
    "ds_read_b128 v[204:207], v1 offset:19456\n"
    "s_waitcnt vmcnt(11) lgkmcnt(4)\n"
    "v_mfma_f32_32x32x16_bf16 v[16:31], v[176:179], a[16:19], v[16:31]\n"
    "ds_read_b128 v[168:171], v1 offset:20480\n"
    "v_mfma_f32_32x32x16_bf16 v[32:47], v[180:183], a[16:19], v[32:47]\n"
    "ds_read_b128 v[172:175], v1 offset:21504\n"
    "s_add_u32 s15, s38, 16384\n"
    "buffer_load_dwordx4 a[64:67], v1, s[16:19], s15 offen offset:0 sc1\n"
    "v_mfma_f32_32x32x16_bf16 v[48:63], v[184:187], a[16:19], v[48:63]\n"
    "ds_read_b128 v[176:179], v1 offset:22528\n"
    "s_waitcnt vmcnt(11)\n"
    "v_mfma_f32_32x32x16_bf16 v[16:31], v[188:191], a[20:23], v[16:31]\n"
    "ds_read_b128 v[180:183], v1 offset:23552\n"
    "s_waitcnt lgkmcnt(4)\n"
    "v_mfma_f32_32x32x16_bf16 v[32:47], v[192:195], a[20:23], v[32:47]\n"
    "ds_read_b128 v[184:187], v1 offset:24576\n"
    "buffer_load_dwordx4 a[68:71], v1, s[16:19], s15 offen offset:1024 sc1\n"
    "v_mfma_f32_32x32x16_bf16 v[48:63], v[196:199], a[20:23], v[48:63]\n"
    "ds_read_b128 v[188:191], v1 offset:25600\n"
    "s_waitcnt vmcnt(11)\n"
    "v_mfma_f32_32x32x16_bf16 v[16:31], v[200:203], a[24:27], v[16:31]\n"
    "ds_read_b128 v[192:195], v1 offset:26624\n"
    "v_mfma_f32_32x32x16_bf16 v[32:47], v[204:207], a[24:27], v[32:47]\n"
    "ds_read_b128 v[196:199], v1 offset:27648\n"
    "buffer_load_dwordx4 a[72:75], v1, s[16:19], s15 offen offset:2048 sc1\n"
    "s_waitcnt lgkmcnt(4)\n"
    "v_mfma_f32_32x32x16_bf16 v[48:63], v[168:171], a[24:27], v[48:63]\n"
    "ds_read_b128 v[200:203], v1 offset:28672\n"
    "s_waitcnt vmcnt(11)\n"
    "v_mfma_f32_32x32x16_bf16 v[16:31], v[172:175], a[28:31], v[16:31]\n"
    "ds_read_b128 v[204:207], v1 offset:29696\n"
    "v_mfma_f32_32x32x16_bf16 v[32:47], v[176:179], a[28:31], v[32:47]\n"
    "ds_read_b128 v[168:171], v1 offset:30720\n"
    "buffer_load_dwordx4 a[76:79], v1, s[16:19], s15 offen offset:3072 sc1\n"
    "v_mfma_f32_32x32x16_bf16 v[48:63], v[180:183], a[28:31], v[48:63]\n"
    "ds_read_b128 v[172:175], v1 offset:31744\n"
    "s_waitcnt vmcnt(11) lgkmcnt(4)\n"
    "v_mfma_f32_32x32x16_bf16 v[16:31], v[184:187], a[32:35], v[16:31]\n"
    "ds_read_b128 v[176:179], v2 offset:32768\n"
    "v_mfma_f32_32x32x16_bf16 v[32:47], v[188:191], a[32:35], v[32:47]\n"
    "ds_read_b128 v[180:183], v2 offset:33792\n"
    "s_add_u32 s15, s38, 20480\n"
    "buffer_load_dwordx4 a[80:83], v1, s[16:19], s15 offen offset:0 sc1\n"
    "v_mfma_f32_32x32x16_bf16 v[48:63], v[192:195], a[32:35], v[48:63]\n"
    "ds_read_b128 v[184:187], v2 offset:34816\n"
    "s_waitcnt vmcnt(11)\n"
    "v_mfma_f32_32x32x16_bf16 v[16:31], v[196:199], a[36:39], v[16:31]\n"
    "ds_read_b128 v[188:191], v2 offset:35840\n"
    "s_waitcnt lgkmcnt(4)\n"
    "v_mfma_f32_32x32x16_bf16 v[32:47], v[200:203], a[36:39], v[32:47]\n"
    "ds_read_b128 v[192:195], v2 offset:36864\n"
    "buffer_load_dwordx4 a[84:87], v1, s[16:19], s15 offen offset:1024 sc1\n"
    "v_mfma_f32_32x32x16_bf16 v[48:63], v[204:207], a[36:39], v[48:63]\n"
    "ds_read_b128 v[196:199], v2 offset:37888\n"
    "s_waitcnt vmcnt(11)\n"
    "v_mfma_f32_32x32x16_bf16 v[16:31], v[168:171], a[40:43], v[16:31]\n"
    "ds_read_b128 v[200:203], v2 offset:38912\n"
    "v_mfma_f32_32x32x16_bf16 v[32:47], v[172:175], a[40:43], v[32:47]\n"
    "ds_read_b128 v[204:207], v2 offset:39936\n"
    "buffer_load_dwordx4 a[88:91], v1, s[16:19], s15 offen offset:2048 sc1\n"
    "s_waitcnt lgkmcnt(4)\n"
    "v_mfma_f32_32x32x16_bf16 v[48:63], v[176:179], a[40:43], v[48:63]\n"
    "ds_read_b128 v[168:171], v2 offset:40960\n"
    "s_waitcnt vmcnt(11)\n"
    "v_mfma_f32_32x32x16_bf16 v[16:31], v[180:183], a[44:47], v[16:31]\n"
    "ds_read_b128 v[172:175], v2 offset:41984\n"
    "v_mfma_f32_32x32x16_bf16 v[32:47], v[184:187], a[44:47], v[32:47]\n"
    "ds_read_b128 v[176:179], v2 offset:43008\n"
    "buffer_load_dwordx4 a[92:95], v1, s[16:19], s15 offen offset:3072 sc1\n"
    "v_mfma_f32_32x32x16_bf16 v[48:63], v[188:191], a[44:47], v[48:63]\n"
    "ds_read_b128 v[180:183], v2 offset:44032\n"
    "s_waitcnt vmcnt(11) lgkmcnt(4)\n"
    "v_mfma_f32_32x32x16_bf16 v[16:31], v[192:195], a[48:51], v[16:31]\n"
    "ds_read_b128 v[184:187], v2 offset:45056\n"
    "v_mfma_f32_32x32x16_bf16 v[32:47], v[196:199], a[48:51], v[32:47]\n"
    "ds_read_b128 v[188:191], v2 offset:46080\n"
    "s_add_u32 s15, s38, 24576\n"
    "buffer_load_dwordx4 a[96:99], v1, s[16:19], s15 offen offset:0 sc1\n"
    "v_mfma_f32_32x32x16_bf16 v[48:63], v[200:203], a[48:51], v[48:63]\n"
    "ds_read_b128 v[192:195], v2 offset:47104\n"
    "s_waitcnt vmcnt(11)\n"
    "v_mfma_f32_32x32x16_bf16 v[16:31], v[204:207], a[52:55], v[16:31]\n"
    "ds_read_b128 v[196:199], v2 offset:48128\n"
    "s_waitcnt lgkmcnt(4)\n"
    "v_mfma_f32_32x32x16_bf16 v[32:47], v[168:171], a[52:55], v[32:47]\n"
    "ds_read_b128 v[200:203], v2 offset:49152\n"
    "buffer_load_dwordx4 a[100:103], v1, s[16:19], s15 offen offset:1024 sc1\n"
    "v_mfma_f32_32x32x16_bf16 v[48:63], v[172:175], a[52:55], v[48:63]\n"
    "ds_read_b128 v[204:207], v2 offset:50176\n"
    "s_waitcnt vmcnt(11)\n"
    "v_mfma_f32_32x32x16_bf16 v[16:31], v[176:179], a[56:59], v[16:31]\n"
    "ds_read_b128 v[168:171], v2 offset:51200\n"
    "v_mfma_f32_32x32x16_bf16 v[32:47], v[180:183], a[56:59], v[32:47]\n"
    "ds_read_b128 v[172:175], v2 offset:52224\n"
    "buffer_load_dwordx4 a[104:107], v1, s[16:19], s15 offen offset:2048 sc1\n"
    "s_waitcnt lgkmcnt(4)\n"
    "v_mfma_f32_32x32x16_bf16 v[48:63], v[184:187], a[56:59], v[48:63]\n"
    "ds_read_b128 v[176:179], v2 offset:53248\n"
    "s_waitcnt vmcnt(11)\n"
    "v_mfma_f32_32x32x16_bf16 v[16:31], v[188:191], a[60:63], v[16:31]\n"
    "ds_read_b128 v[180:183], v2 offset:54272\n"
    "v_mfma_f32_32x32x16_bf16 v[32:47], v[192:195], a[60:63], v[32:47]\n"
    "ds_read_b128 v[184:187], v2 offset:55296\n"
    "buffer_load_dwordx4 a[108:111], v1, s[16:19], s15 offen offset:3072 sc1\n"
    "v_mfma_f32_32x32x16_bf16 v[48:63], v[196:199], a[60:63], v[48:63]\n"
    "ds_read_b128 v[188:191], v2 offset:56320\n"
    "s_waitcnt vmcnt(11) lgkmcnt(4)\n"
    "v_mfma_f32_32x32x16_bf16 v[16:31], v[200:203], a[64:67], v[16:31]\n"
    "ds_read_b128 v[192:195], v2 offset:57344\n"
    "v_mfma_f32_32x32x16_bf16 v[32:47], v[204:207], a[64:67], v[32:47]\n"
    "ds_read_b128 v[196:199], v2 offset:58368\n"
    "s_add_u32 s15, s38, 28672\n"
    "buffer_load_dwordx4 a[112:115], v1, s[16:19], s15 offen offset:0 sc1\n"
    "v_mfma_f32_32x32x16_bf16 v[48:63], v[168:171], a[64:67], v[48:63]\n"
    "ds_read_b128 v[200:203], v2 offset:59392\n"
    "s_waitcnt vmcnt(11)\n"
    "v_mfma_f32_32x32x16_bf16 v[16:31], v[172:175], a[68:71], v[16:31]\n"
    "ds_read_b128 v[204:207], v2 offset:60416\n"
    "s_waitcnt lgkmcnt(4)\n"
    "v_mfma_f32_32x32x16_bf16 v[32:47], v[176:179], a[68:71], v[32:47]\n"
    "ds_read_b128 v[168:171], v2 offset:61440\n"
    "buffer_load_dwordx4 a[116:119], v1, s[16:19], s15 offen offset:1024 sc1\n"
    "v_mfma_f32_32x32x16_bf16 v[48:63], v[180:183], a[68:71], v[48:63]\n"
    "ds_read_b128 v[172:175], v2 offset:62464\n"
    "s_waitcnt vmcnt(11)\n"
    "v_mfma_f32_32x32x16_bf16 v[16:31], v[184:187], a[72:75], v[16:31]\n"
    "ds_read_b128 v[176:179], v2 offset:63488\n"
    "v_mfma_f32_32x32x16_bf16 v[32:47], v[188:191], a[72:75], v[32:47]\n"
    "ds_read_b128 v[180:183], v2 offset:64512\n"
    "buffer_load_dwordx4 a[120:123], v1, s[16:19], s15 offen offset:2048 sc1\n"
    "s_waitcnt lgkmcnt(4)\n"
    "v_mfma_f32_32x32x16_bf16 v[48:63], v[192:195], a[72:75], v[48:63]\n"
    "s_barrier\n"
    "ds_read_b128 v[184:187], v3 offset:0\n"
    "s_waitcnt vmcnt(11)\n"
    "v_mfma_f32_32x32x16_bf16 v[16:31], v[196:199], a[76:79], v[16:31]\n"
    "ds_read_b128 v[188:191], v3 offset:1024\n"
    "v_mfma_f32_32x32x16_bf16 v[32:47], v[200:203], a[76:79], v[32:47]\n"
    "ds_read_b128 v[192:195], v3 offset:2048\n"
    "buffer_load_dwordx4 a[124:127], v1, s[16:19], s15 offen offset:3072 sc1\n"
    "v_mfma_f32_32x32x16_bf16 v[48:63], v[204:207], a[76:79], v[48:63]\n"
    "ds_read_b128 v[196:199], v3 offset:3072\n"
    "s_waitcnt vmcnt(11) lgkmcnt(4)\n"
    "v_mfma_f32_32x32x16_bf16 v[16:31], v[168:171], a[80:83], v[16:31]\n"
    "ds_read_b128 v[200:203], v3 offset:4096\n"
    "v_mfma_f32_32x32x16_bf16 v[32:47], v[172:175], a[80:83], v[32:47]\n"
    "ds_read_b128 v[204:207], v3 offset:5120\n"
    "s_add_u32 s15, s38, 32768\n"
    "buffer_load_dwordx4 a[128:131], v1, s[16:19], s15 offen offset:0 sc1\n"
    "v_mfma_f32_32x32x16_bf16 v[48:63], v[176:179], a[80:83], v[48:63]\n"
    "ds_read_b128 v[168:171], v3 offset:6144\n"
    "s_waitcnt vmcnt(11)\n"
    "v_mfma_f32_32x32x16_bf16 v[16:31], v[180:183], a[84:87], v[16:31]\n"
    "ds_read_b128 v[172:175], v3 offset:7168\n"
    "s_waitcnt lgkmcnt(4)\n"
    "v_mfma_f32_32x32x16_bf16 v[32:47], v[184:187], a[84:87], v[32:47]\n"
    "ds_read_b128 v[176:179], v3 offset:8192\n"
    "buffer_load_dwordx4 a[132:135], v1, s[16:19], s15 offen offset:1024 sc1\n"
    "v_mfma_f32_32x32x16_bf16 v[48:63], v[188:191], a[84:87], v[48:63]\n"
    "ds_read_b128 v[180:183], v3 offset:9216\n"
    "s_waitcnt vmcnt(11)\n"
    "v_mfma_f32_32x32x16_bf16 v[16:31], v[192:195], a[88:91], v[16:31]\n"
    "ds_read_b128 v[184:187], v3 offset:10240\n"
    "v_mfma_f32_32x32x16_bf16 v[32:47], v[196:199], a[88:91], v[32:47]\n"
    "ds_read_b128 v[188:191], v3 offset:11264\n"
    "buffer_load_dwordx4 a[136:139], v1, s[16:19], s15 offen offset:2048 sc1\n"
    "s_waitcnt lgkmcnt(4)\n"
    "v_mfma_f32_32x32x16_bf16 v[48:63], v[200:203], a[88:91], v[48:63]\n"
    "ds_read_b128 v[192:195], v3 offset:12288\n"
    "s_waitcnt vmcnt(11)\n"
    "v_mfma_f32_32x32x16_bf16 v[16:31], v[204:207], a[92:95], v[16:31]\n"
    "ds_read_b128 v[196:199], v3 offset:13312\n"
    "v_mfma_f32_32x32x16_bf16 v[32:47], v[168:171], a[92:95], v[32:47]\n"
    "ds_read_b128 v[200:203], v3 offset:14336\n"
    "buffer_load_dwordx4 a[140:143], v1, s[16:19], s15 offen offset:3072 sc1\n"
    "v_mfma_f32_32x32x16_bf16 v[48:63], v[172:175], a[92:95], v[48:63]\n"
    "ds_read_b128 v[204:207], v3 offset:15360\n"
    "s_waitcnt vmcnt(11) lgkmcnt(4)\n"
    "v_mfma_f32_32x32x16_bf16 v[16:31], v[176:179], a[96:99], v[16:31]\n"
    "ds_read_b128 v[168:171], v1 offset:32768\n"
    "v_mfma_f32_32x32x16_bf16 v[32:47], v[180:183], a[96:99], v[32:47]\n"
    "ds_read_b128 v[172:175], v1 offset:33792\n"
    "s_add_u32 s15, s38, 36864\n"
    "buffer_load_dwordx4 a[144:147], v1, s[16:19], s15 offen offset:0 sc1\n"
    "v_mfma_f32_32x32x16_bf16 v[48:63], v[184:187], a[96:99], v[48:63]\n"
    "ds_read_b128 v[176:179], v1 offset:34816\n"
    "s_waitcnt vmcnt(11)\n"
    "v_mfma_f32_32x32x16_bf16 v[16:31], v[188:191], a[100:103], v[16:31]\n"
    "ds_read_b128 v[180:183], v1 offset:35840\n"
    "s_waitcnt lgkmcnt(4)\n"
    "v_mfma_f32_32x32x16_bf16 v[32:47], v[192:195], a[100:103], v[32:47]\n"
    "ds_read_b128 v[184:187], v1 offset:36864\n"
    "buffer_load_dwordx4 a[148:151], v1, s[16:19], s15 offen offset:1024 sc1\n"
    "v_mfma_f32_32x32x16_bf16 v[48:63], v[196:199], a[100:103], v[48:63]\n"
    "ds_read_b128 v[188:191], v1 offset:37888\n"
    "s_waitcnt vmcnt(11)\n"
    "v_mfma_f32_32x32x16_bf16 v[16:31], v[200:203], a[104:107], v[16:31]\n"
    "ds_read_b128 v[192:195], v1 offset:38912\n"
    "v_mfma_f32_32x32x16_bf16 v[32:47], v[204:207], a[104:107], v[32:47]\n"
    "ds_read_b128 v[196:199], v1 offset:39936\n"
    "buffer_load_dwordx4 a[152:155], v1, s[16:19], s15 offen offset:2048 sc1\n"
    "s_waitcnt lgkmcnt(4)\n"
    "v_mfma_f32_32x32x16_bf16 v[48:63], v[168:171], a[104:107], v[48:63]\n"
    "ds_read_b128 v[200:203], v1 offset:40960\n"
    "s_waitcnt vmcnt(11)\n"
    "v_mfma_f32_32x32x16_bf16 v[16:31], v[172:175], a[108:111], v[16:31]\n"
    "ds_read_b128 v[204:207], v1 offset:41984\n"
    "v_mfma_f32_32x32x16_bf16 v[32:47], v[176:179], a[108:111], v[32:47]\n"
    "ds_read_b128 v[168:171], v1 offset:43008\n"
    "buffer_load_dwordx4 a[156:159], v1, s[16:19], s15 offen offset:3072 sc1\n"
    "v_mfma_f32_32x32x16_bf16 v[48:63], v[180:183], a[108:111], v[48:63]\n"
    "ds_read_b128 v[172:175], v1 offset:44032\n"
    "s_lshl_b32 s12, s27, 12\n"
    "s_add_u32 s12, s12, s41\n"
    "s_add_u32 s12, s12, 21385216\n"
    "buffer_load_dwordx2 v[136:137], v7, s[16:19], s12 offen offset:0 sc1\n"
    "buffer_load_dwordx2 v[138:139], v7, s[16:19], s12 offen offset:256 sc1\n"
    "buffer_load_dwordx2 v[140:141], v7, s[16:19], s12 offen offset:512 sc1\n"
    "buffer_load_dwordx2 v[142:143], v7, s[16:19], s12 offen offset:768 sc1\n"
    "buffer_load_dwordx2 v[144:145], v7, s[16:19], s12 offen offset:1024 sc1\n"
    "buffer_load_dwordx2 v[146:147], v7, s[16:19], s12 offen offset:1280 sc1\n"
    "buffer_load_dwordx2 v[148:149], v7, s[16:19], s12 offen offset:1536 sc1\n"
    "buffer_load_dwordx2 v[150:151], v7, s[16:19], s12 offen offset:1792 sc1\n"
    "s_waitcnt vmcnt(19) lgkmcnt(4)\n"
    "v_mfma_f32_32x32x16_bf16 v[16:31], v[184:187], a[112:115], v[16:31]\n"
    "ds_read_b128 v[176:179], v1 offset:45056\n"
    "v_mfma_f32_32x32x16_bf16 v[32:47], v[188:191], a[112:115], v[32:47]\n"
    "ds_read_b128 v[180:183], v1 offset:46080\n"
    "v_mfma_f32_32x32x16_bf16 v[48:63], v[192:195], a[112:115], v[48:63]\n"
    "ds_read_b128 v[184:187], v1 offset:47104\n"
    "s_waitcnt vmcnt(18)\n"
    "v_mfma_f32_32x32x16_bf16 v[16:31], v[196:199], a[116:119], v[16:31]\n"
    "ds_read_b128 v[188:191], v1 offset:48128\n"
    "s_add_u32 m0, s29, 0\n"
    "s_add_u32 s14, s37, 147456\n"
    "buffer_load_dwordx4 v1, s[16:19], s14 offen offset:0 sc1 lds\n"
    "s_waitcnt lgkmcnt(4)\n"
    "v_mfma_f32_32x32x16_bf16 v[32:47], v[200:203], a[116:119], v[32:47]\n"
    "ds_read_b128 v[192:195], v1 offset:49152\n"
    "v_mfma_f32_32x32x16_bf16 v[48:63], v[204:207], a[116:119], v[48:63]\n"
    "ds_read_b128 v[196:199], v1 offset:50176\n"
    "s_waitcnt vmcnt(18)\n"
    "v_mfma_f32_32x32x16_bf16 v[16:31], v[168:171], a[120:123], v[16:31]\n"
    "ds_read_b128 v[200:203], v1 offset:51200\n"
    "buffer_load_dwordx4 v1, s[16:19], s14 offen offset:1024 sc1 lds\n"
    "v_mfma_f32_32x32x16_bf16 v[32:47], v[172:175], a[120:123], v[32:47]\n"
    "ds_read_b128 v[204:207], v1 offset:52224\n"
    "s_waitcnt lgkmcnt(4)\n"
    "v_mfma_f32_32x32x16_bf16 v[48:63], v[176:179], a[120:123], v[48:63]\n"
    "ds_read_b128 v[168:171], v1 offset:53248\n"
    "s_waitcnt vmcnt(18)\n"
    "v_mfma_f32_32x32x16_bf16 v[16:31], v[180:183], a[124:127], v[16:31]\n"
    "ds_read_b128 v[172:175], v1 offset:54272\n"
    "buffer_load_dwordx4 v1, s[16:19], s14 offen offset:2048 sc1 lds\n"
    "v_mfma_f32_32x32x16_bf16 v[32:47], v[184:187], a[124:127], v[32:47]\n"
    "ds_read_b128 v[176:179], v1 offset:55296\n"
    "v_mfma_f32_32x32x16_bf16 v[48:63], v[188:191], a[124:127], v[48:63]\n"
    "ds_read_b128 v[180:183], v1 offset:56320\n"
    "s_waitcnt vmcnt(18) lgkmcnt(4)\n"
    "v_mfma_f32_32x32x16_bf16 v[16:31], v[192:195], a[128:131], v[16:31]\n"
    "ds_read_b128 v[184:187], v1 offset:57344\n"
    "buffer_load_dwordx4 v1, s[16:19], s14 offen offset:3072 sc1 lds\n"
    "v_mfma_f32_32x32x16_bf16 v[32:47], v[196:199], a[128:131], v[32:47]\n"
    "ds_read_b128 v[188:191], v1 offset:58368\n"
    "v_mfma_f32_32x32x16_bf16 v[48:63], v[200:203], a[128:131], v[48:63]\n"
    "ds_read_b128 v[192:195], v1 offset:59392\n"
    "ds_read_b128 v[104:107], v11 offset:0\n"
    "ds_read_b128 v[108:111], v11 offset:16\n"
    "ds_read_b128 v[112:115], v11 offset:32\n"
    "ds_read_b128 v[116:119], v11 offset:48\n"
    "s_waitcnt vmcnt(4)\n"
    "v_mfma_f32_32x32x16_bf16 v[16:31], v[204:207], a[132:135], v[16:31]\n"
    "ds_read_b128 v[196:199], v1 offset:60416\n"
    "s_add_u32 m0, s29, 16384\n"
    "s_add_u32 s14, s37, 163840\n"
    "buffer_load_dwordx4 v1, s[16:19], s14 offen offset:0 sc1 lds\n"
    "s_waitcnt lgkmcnt(8)\n"
    "v_mfma_f32_32x32x16_bf16 v[32:47], v[168:171], a[132:135], v[32:47]\n"
    "ds_read_b128 v[200:203], v1 offset:61440\n"
    "v_add_f32 v136, v136, v138\n"
    "v_add_f32 v137, v137, v139\n"
    "v_add_f32 v136, v136, v140\n"
    "v_add_f32 v137, v137, v141\n"
    "v_add_f32 v136, v136, v142\n"
    "v_mfma_f32_32x32x16_bf16 v[48:63], v[172:175], a[132:135], v[48:63]\n"
    "ds_read_b128 v[204:207], v1 offset:62464\n"
    "v_add_f32 v137, v137, v143\n"
    "v_add_f32 v136, v136, v144\n"
    "v_add_f32 v137, v137, v145\n"
    "v_add_f32 v136, v136, v146\n"
    "v_add_f32 v137, v137, v147\n"
    "v_mfma_f32_32x32x16_bf16 v[16:31], v[176:179], a[136:139], v[16:31]\n"
    "ds_read_b128 v[168:171], v1 offset:63488\n"
    "buffer_load_dwordx4 v1, s[16:19], s14 offen offset:1024 sc1 lds\n"
    "v_add_f32 v136, v136, v148\n"
    "v_add_f32 v137, v137, v149\n"
    "v_add_f32 v136, v136, v150\n"
    "v_mfma_f32_32x32x16_bf16 v[32:47], v[180:183], a[136:139], v[32:47]\n"
    "ds_read_b128 v[172:175], v1 offset:64512\n"
    "v_add_f32 v137, v137, v151\n"
    "v_mov_b32 v138, v136\n"
    "v_mov_b32 v139, v137\n"
    "s_nop 0\n"
    "v_permlane32_swap_b32 v136, v138\n"
    "s_waitcnt lgkmcnt(4)\n"
    "v_mfma_f32_32x32x16_bf16 v[48:63], v[184:187], a[136:139], v[48:63]\n"
    "ds_read_b128 v[176:179], v2 offset:0\n"
    "v_permlane32_swap_b32 v137, v139\n"
    "v_add_f32 v136, v136, v138\n"
    "v_add_f32 v137, v137, v139\n"
    "v_mul_f32 v152, 0x3acccccd, v136\n"
    "v_mul_f32 v137, 0x3acccccd, v137\n"
    "v_mfma_f32_32x32x16_bf16 v[16:31], v[188:191], a[140:143], v[16:31]\n"
    "ds_read_b128 v[180:183], v2 offset:1024\n"
    "buffer_load_dwordx4 v1, s[16:19], s14 offen offset:2048 sc1 lds\n"
    "v_fma_f32 v154, -v152, v152, v137\n"
    "v_max_f32 v154, 0, v154\n"
    "v_add_f32 v154, 0x3727c5ac, v154\n"
    "v_mfma_f32_32x32x16_bf16 v[32:47], v[192:195], a[140:143], v[32:47]\n"
    "ds_read_b128 v[184:187], v2 offset:2048\n"
    "v_rsq_f32 v153, v154\n"
    "v_sub_f32 v152, 0, v152\n"
    "v_mul_f32 v153, 0xbfb8aa3b, v153\n"
    "v_mfma_f32_32x32x16_bf16 v[48:63], v[196:199], a[140:143], v[48:63]\n"
    "ds_read_b128 v[188:191], v2 offset:3072\n"
    "s_waitcnt lgkmcnt(4)\n"
    "v_mfma_f32_32x32x16_bf16 v[16:31], v[200:203], a[144:147], v[16:31]\n"
    "ds_read_b128 v[192:195], v2 offset:4096\n"
    "buffer_load_dwordx4 v1, s[16:19], s14 offen offset:3072 sc1 lds\n"
    "v_mfma_f32_32x32x16_bf16 v[32:47], v[204:207], a[144:147], v[32:47]\n"
    "ds_read_b128 v[196:199], v2 offset:5120\n"
    "v_mfma_f32_32x32x16_bf16 v[48:63], v[168:171], a[144:147], v[48:63]\n"
    "ds_read_b128 v[200:203], v2 offset:6144\n"
    "v_mfma_f32_32x32x16_bf16 v[16:31], v[172:175], a[148:151], v[16:31]\n"
    "ds_read_b128 v[204:207], v2 offset:7168\n"
    "s_add_u32 m0, s29, 98304\n"
    "s_add_u32 s14, s37, 180224\n"
    "buffer_load_dwordx4 v1, s[16:19], s14 offen offset:0 sc1 lds\n"
    "s_waitcnt lgkmcnt(4)\n"
    "v_mfma_f32_32x32x16_bf16 v[32:47], v[176:179], a[148:151], v[32:47]\n"
    "ds_read_b128 v[168:171], v2 offset:8192\n"
    "v_mfma_f32_32x32x16_bf16 v[48:63], v[180:183], a[148:151], v[48:63]\n"
    "ds_read_b128 v[172:175], v2 offset:9216\n"
    "v_mfma_f32_32x32x16_bf16 v[16:31], v[184:187], a[152:155], v[16:31]\n"
    "ds_read_b128 v[176:179], v2 offset:10240\n"
    "buffer_load_dwordx4 v1, s[16:19], s14 offen offset:1024 sc1 lds\n"
    "v_mfma_f32_32x32x16_bf16 v[32:47], v[188:191], a[152:155], v[32:47]\n"
    "ds_read_b128 v[180:183], v2 offset:11264\n"
    "s_waitcnt lgkmcnt(4)\n"
    "v_mfma_f32_32x32x16_bf16 v[48:63], v[192:195], a[152:155], v[48:63]\n"
    "ds_read_b128 v[184:187], v2 offset:12288\n"
    "v_mfma_f32_32x32x16_bf16 v[16:31], v[196:199], a[156:159], v[16:31]\n"
    "ds_read_b128 v[188:191], v2 offset:13312\n"
    "buffer_load_dwordx4 v1, s[16:19], s14 offen offset:2048 sc1 lds\n"
    "v_mfma_f32_32x32x16_bf16 v[32:47], v[200:203], a[156:159], v[32:47]\n"
    "ds_read_b128 v[192:195], v2 offset:14336\n"
    "v_mfma_f32_32x32x16_bf16 v[48:63], v[204:207], a[156:159], v[48:63]\n"
    "ds_read_b128 v[196:199], v2 offset:15360\n"
    "s_waitcnt lgkmcnt(4)\n"
    "v_mfma_f32_32x32x16_bf16 v[64:79], v[168:171], a[0:3], 0\n"
    "ds_read_b128 v[200:203], v2 offset:16384\n"
    "v_mfma_f32_32x32x16_bf16 v[64:79], v[172:175], a[4:7], v[64:79]\n"
    "ds_read_b128 v[204:207], v2 offset:17408\n"
    "v_mfma_f32_32x32x16_bf16 v[64:79], v[176:179], a[8:11], v[64:79]\n"
    "ds_read_b128 v[168:171], v2 offset:18432\n"
    "buffer_load_dwordx4 v1, s[16:19], s14 offen offset:3072 sc1 lds\n"
    "v_fma_f32 v16, v104, v152, v16\n"
    "v_fma_f32 v24, v112, v152, v24\n"
    "v_mul_f32 v156, v16, v153\n"
    "v_mfma_f32_32x32x16_bf16 v[64:79], v[180:183], a[12:15], v[64:79]\n"
    "ds_read_b128 v[172:175], v2 offset:19456\n"
    "v_fma_f32 v17, v105, v152, v17\n"
    "v_fma_f32 v25, v113, v152, v25\n"
    "v_mul_f32 v157, v17, v153\n"
    "s_waitcnt lgkmcnt(4)\n"
    "v_mfma_f32_32x32x16_bf16 v[64:79], v[184:187], a[16:19], v[64:79]\n"
    "ds_read_b128 v[176:179], v2 offset:20480\n"
    "v_exp_f32 v156, v156\n"
    "v_exp_f32 v157, v157\n"
    "v_mul_f32 v16, v16, v24\n"
    "v_mfma_f32_32x32x16_bf16 v[64:79], v[188:191], a[20:23], v[64:79]\n"
    "ds_read_b128 v[180:183], v2 offset:21504\n"
    "v_mul_f32 v17, v17, v25\n"
    "v_fma_f32 v156, v156, v154, v154\n"
    "v_fma_f32 v157, v157, v154, v154\n"
    "v_rcp_f32 v156, v156\n"
    "v_mfma_f32_32x32x16_bf16 v[64:79], v[192:195], a[24:27], v[64:79]\n"
    "ds_read_b128 v[184:187], v2 offset:22528\n"
    "v_rcp_f32 v157, v157\n"
    "v_mul_f32 v16, v16, v156\n"
    "v_mul_f32 v17, v17, v157\n"
    "v_cvt_pk_bf16_f32 v160, v16, v17\n"
    "v_mfma_f32_32x32x16_bf16 v[64:79], v[196:199], a[28:31], v[64:79]\n"
    "ds_read_b128 v[188:191], v2 offset:23552\n"
    "v_fma_f32 v18, v106, v152, v18\n"
    "v_fma_f32 v26, v114, v152, v26\n"
    "v_mul_f32 v158, v18, v153\n"
    "v_fma_f32 v19, v107, v152, v19\n"
    "s_waitcnt lgkmcnt(4)\n"
    "v_mfma_f32_32x32x16_bf16 v[64:79], v[200:203], a[32:35], v[64:79]\n"
    "ds_read_b128 v[192:195], v2 offset:24576\n"
    "v_fma_f32 v27, v115, v152, v27\n"
    "v_mul_f32 v159, v19, v153\n"
    "v_exp_f32 v158, v158\n"
    "v_mfma_f32_32x32x16_bf16 v[64:79], v[204:207], a[36:39], v[64:79]\n"
    "ds_read_b128 v[196:199], v2 offset:25600\n"
    "v_exp_f32 v159, v159\n"
    "v_mul_f32 v18, v18, v26\n"
    "v_mul_f32 v19, v19, v27\n"
    "v_fma_f32 v158, v158, v154, v154\n"
    "v_mfma_f32_32x32x16_bf16 v[64:79], v[168:171], a[40:43], v[64:79]\n"
    "ds_read_b128 v[200:203], v2 offset:26624\n"
    "v_fma_f32 v159, v159, v154, v154\n"
    "v_rcp_f32 v158, v158\n"
    "v_rcp_f32 v159, v159\n"
    "v_mfma_f32_32x32x16_bf16 v[64:79], v[172:175], a[44:47], v[64:79]\n"
    "ds_read_b128 v[204:207], v2 offset:27648\n"
    "v_mul_f32 v18, v18, v158\n"
    "v_mul_f32 v19, v19, v159\n"
    "v_cvt_pk_bf16_f32 v161, v18, v19\n"
    "ds_read_b128 v[120:123], v11 offset:128\n"
    "s_waitcnt lgkmcnt(5)\n"
    "v_mfma_f32_32x32x16_bf16 v[64:79], v[176:179], a[48:51], v[64:79]\n"
    "ds_read_b128 v[168:171], v2 offset:28672\n"
    "ds_read_b128 v[124:127], v11 offset:144\n"
    "ds_read_b128 v[128:131], v11 offset:160\n"
    "ds_read_b128 v[132:135], v11 offset:176\n"
    "v_fma_f32 v20, v108, v152, v20\n"
    "v_fma_f32 v28, v116, v152, v28\n"
    "v_mfma_f32_32x32x16_bf16 v[64:79], v[180:183], a[52:55], v[64:79]\n"
    "ds_read_b128 v[172:175], v2 offset:29696\n"
    "v_mul_f32 v156, v20, v153\n"
    "v_fma_f32 v21, v109, v152, v21\n"
    "v_fma_f32 v29, v117, v152, v29\n"
    "v_mul_f32 v157, v21, v153\n"
    "v_mfma_f32_32x32x16_bf16 v[64:79], v[184:187], a[56:59], v[64:79]\n"
    "ds_read_b128 v[176:179], v2 offset:30720\n"
    "v_exp_f32 v156, v156\n"
    "v_exp_f32 v157, v157\n"
    "v_mul_f32 v20, v20, v28\n"
    "v_mfma_f32_32x32x16_bf16 v[64:79], v[188:191], a[60:63], v[64:79]\n"
    "ds_read_b128 v[180:183], v2 offset:31744\n"
    "v_mul_f32 v21, v21, v29\n"
    "v_fma_f32 v156, v156, v154, v154\n"
    "v_fma_f32 v157, v157, v154, v154\n"
    "s_waitcnt lgkmcnt(8)\n"
    "v_mfma_f32_32x32x16_bf16 v[64:79], v[192:195], a[64:67], v[64:79]\n"
    "s_waitcnt vmcnt(0)\n"
    "s_barrier\n"
    "ds_read_b128 v[184:187], v1 offset:0\n"
    "v_rcp_f32 v156, v156\n"
    "v_mfma_f32_32x32x16_bf16 v[64:79], v[196:199], a[68:71], v[64:79]\n"
    "ds_read_b128 v[188:191], v1 offset:1024\n"
    "s_add_u32 m0, s29, 114688\n"
    "s_add_u32 s14, s37, 196608\n"
    "buffer_load_dwordx4 v1, s[16:19], s14 offen offset:0 sc1 lds\n"
    "v_mfma_f32_32x32x16_bf16 v[64:79], v[200:203], a[72:75], v[64:79]\n"
    "ds_read_b128 v[192:195], v1 offset:2048\n"
    "v_rcp_f32 v157, v157\n"
    "v_mul_f32 v20, v20, v156\n"
    "v_mul_f32 v21, v21, v157\n"
    "v_cvt_pk_bf16_f32 v162, v20, v21\n"
    "v_mfma_f32_32x32x16_bf16 v[64:79], v[204:207], a[76:79], v[64:79]\n"
    "ds_read_b128 v[196:199], v1 offset:3072\n"
    "v_fma_f32 v22, v110, v152, v22\n"
    "v_fma_f32 v30, v118, v152, v30\n"
    "v_mul_f32 v158, v22, v153\n"
    "v_fma_f32 v23, v111, v152, v23\n"
    "s_waitcnt lgkmcnt(4)\n"
    "v_mfma_f32_32x32x16_bf16 v[64:79], v[168:171], a[80:83], v[64:79]\n"
    "ds_read_b128 v[200:203], v1 offset:4096\n"
    "buffer_load_dwordx4 v1, s[16:19], s14 offen offset:1024 sc1 lds\n"
    "v_fma_f32 v31, v119, v152, v31\n"
    "v_mul_f32 v159, v23, v153\n"
    "v_mfma_f32_32x32x16_bf16 v[64:79], v[172:175], a[84:87], v[64:79]\n"
    "ds_read_b128 v[204:207], v1 offset:5120\n"
    "v_exp_f32 v158, v158\n"
    "v_exp_f32 v159, v159\n"
    "v_mul_f32 v22, v22, v30\n"
    "v_mfma_f32_32x32x16_bf16 v[64:79], v[176:179], a[88:91], v[64:79]\n"
    "ds_read_b128 v[168:171], v1 offset:6144\n"
    "v_mul_f32 v23, v23, v31\n"
    "v_fma_f32 v158, v158, v154, v154\n"
    "v_fma_f32 v159, v159, v154, v154\n"
    "v_rcp_f32 v158, v158\n"
    "v_mfma_f32_32x32x16_bf16 v[64:79], v[180:183], a[92:95], v[64:79]\n"
    "ds_read_b128 v[172:175], v1 offset:7168\n"
    "buffer_load_dwordx4 v1, s[16:19], s14 offen offset:2048 sc1 lds\n"
    "v_rcp_f32 v159, v159\n"
    "s_waitcnt lgkmcnt(4)\n"
    "v_mfma_f32_32x32x16_bf16 v[64:79], v[184:187], a[96:99], v[64:79]\n"
    "ds_read_b128 v[176:179], v1 offset:8192\n"
    "v_mul_f32 v22, v22, v158\n"
    "v_mul_f32 v23, v23, v159\n"
    "v_cvt_pk_bf16_f32 v163, v22, v23\n"
    "v_mfma_f32_32x32x16_bf16 v[64:79], v[188:191], a[100:103], v[64:79]\n"
    "ds_read_b128 v[180:183], v1 offset:9216\n"
    "s_add_u32 s35, s39, 0\n"
    "buffer_store_dwordx4 v[160:163], v1, s[16:19], s35 offen sc1\n"
    "v_fma_f32 v32, v120, v152, v32\n"
    "v_fma_f32 v40, v128, v152, v40\n"
    "v_mfma_f32_32x32x16_bf16 v[64:79], v[192:195], a[104:107], v[64:79]\n"
    "ds_read_b128 v[184:187], v1 offset:10240\n"
    "buffer_load_dwordx4 v1, s[16:19], s14 offen offset:3072 sc1 lds\n"
    "v_mul_f32 v156, v32, v153\n"
    "v_fma_f32 v33, v121, v152, v33\n"
    "v_fma_f32 v41, v129, v152, v41\n"
    "v_mfma_f32_32x32x16_bf16 v[64:79], v[196:199], a[108:111], v[64:79]\n"
    "ds_read_b128 v[188:191], v1 offset:11264\n"
    "v_mul_f32 v157, v33, v153\n"
    "v_exp_f32 v156, v156\n"
    "s_waitcnt lgkmcnt(4)\n"
    "v_mfma_f32_32x32x16_bf16 v[64:79], v[200:203], a[112:115], v[64:79]\n"
    "ds_read_b128 v[192:195], v1 offset:12288\n"
    "v_exp_f32 v157, v157\n"
    "v_mul_f32 v32, v32, v40\n"
    "v_mul_f32 v33, v33, v41\n"
    "v_fma_f32 v156, v156, v154, v154\n"
    "v_mfma_f32_32x32x16_bf16 v[64:79], v[204:207], a[116:119], v[64:79]\n"
    "ds_read_b128 v[196:199], v1 offset:13312\n"
    "s_add_u32 m0, s29, 131072\n"
    "s_add_u32 s14, s37, 212992\n"
    "buffer_load_dwordx4 v1, s[16:19], s14 offen offset:0 sc1 lds\n"
    "v_fma_f32 v157, v157, v154, v154\n"
    "v_mfma_f32_32x32x16_bf16 v[64:79], v[168:171], a[120:123], v[64:79]\n"
    "ds_read_b128 v[200:203], v1 offset:14336\n"
    "v_rcp_f32 v156, v156\n"
    "v_rcp_f32 v157, v157\n"
    "v_mul_f32 v32, v32, v156\n"
    "v_mfma_f32_32x32x16_bf16 v[64:79], v[172:175], a[124:127], v[64:79]\n"
    "ds_read_b128 v[204:207], v1 offset:15360\n"
    "v_mul_f32 v33, v33, v157\n"
    "v_cvt_pk_bf16_f32 v164, v32, v33\n"
    "v_fma_f32 v34, v122, v152, v34\n"
    "v_fma_f32 v42, v130, v152, v42\n"
    "s_waitcnt lgkmcnt(4)\n"
    "v_mfma_f32_32x32x16_bf16 v[64:79], v[176:179], a[128:131], v[64:79]\n"
    "ds_read_b128 v[168:171], v1 offset:16384\n"
    "buffer_load_dwordx4 v1, s[16:19], s14 offen offset:1024 sc1 lds\n"
    "v_mul_f32 v158, v34, v153\n"
    "v_fma_f32 v35, v123, v152, v35\n"
    "v_fma_f32 v43, v131, v152, v43\n"
    "v_mfma_f32_32x32x16_bf16 v[64:79], v[180:183], a[132:135], v[64:79]\n"
    "ds_read_b128 v[172:175], v1 offset:17408\n"
    "v_mul_f32 v159, v35, v153\n"
    "v_exp_f32 v158, v158\n"
    "v_exp_f32 v159, v159\n"
    "v_mfma_f32_32x32x16_bf16 v[64:79], v[184:187], a[136:139], v[64:79]\n"
    "ds_read_b128 v[176:179], v1 offset:18432\n"
    "v_mul_f32 v34, v34, v42\n"
    "v_mul_f32 v35, v35, v43\n"
    "v_fma_f32 v158, v158, v154, v154\n"
    "v_fma_f32 v159, v159, v154, v154\n"
    "v_mfma_f32_32x32x16_bf16 v[64:79], v[188:191], a[140:143], v[64:79]\n"
    "ds_read_b128 v[180:183], v1 offset:19456\n"
    "buffer_load_dwordx4 v1, s[16:19], s14 offen offset:2048 sc1 lds\n"
    "v_rcp_f32 v158, v158\n"
    "s_waitcnt lgkmcnt(4)\n"
    "v_mfma_f32_32x32x16_bf16 v[64:79], v[192:195], a[144:147], v[64:79]\n"
    "ds_read_b128 v[184:187], v1 offset:20480\n"
    "v_rcp_f32 v159, v159\n"
    "v_mul_f32 v34, v34, v158\n"
    "v_mul_f32 v35, v35, v159\n"
    "v_cvt_pk_bf16_f32 v165, v34, v35\n"
    "v_mfma_f32_32x32x16_bf16 v[64:79], v[196:199], a[148:151], v[64:79]\n"
    "ds_read_b128 v[188:191], v1 offset:21504\n"
    "ds_read_b128 v[104:107], v11 offset:256\n"
    "ds_read_b128 v[108:111], v11 offset:272\n"
    "ds_read_b128 v[112:115], v11 offset:288\n"
    "ds_read_b128 v[116:119], v11 offset:304\n"
    "v_fma_f32 v36, v124, v152, v36\n"
    "v_mfma_f32_32x32x16_bf16 v[64:79], v[200:203], a[152:155], v[64:79]\n"
    "ds_read_b128 v[192:195], v1 offset:22528\n"
    "buffer_load_dwordx4 v1, s[16:19], s14 offen offset:3072 sc1 lds\n"
    "v_fma_f32 v44, v132, v152, v44\n"
    "v_mul_f32 v156, v36, v153\n"
    "v_fma_f32 v37, v125, v152, v37\n"
    "v_mfma_f32_32x32x16_bf16 v[64:79], v[204:207], a[156:159], v[64:79]\n"
    "ds_read_b128 v[196:199], v1 offset:23552\n"
    "v_fma_f32 v45, v133, v152, v45\n"
    "v_mul_f32 v157, v37, v153\n"
    "v_exp_f32 v156, v156\n"
    "s_waitcnt lgkmcnt(8)\n"
    "v_mfma_f32_32x32x16_bf16 v[80:95], v[168:171], a[0:3], 0\n"
    "ds_read_b128 v[200:203], v1 offset:24576\n"
    "v_exp_f32 v157, v157\n"
    "v_mul_f32 v36, v36, v44\n"
    "v_mul_f32 v37, v37, v45\n"
    "v_fma_f32 v156, v156, v154, v154\n"
    "v_mfma_f32_32x32x16_bf16 v[80:95], v[172:175], a[4:7], v[80:95]\n"
    "ds_read_b128 v[204:207], v1 offset:25600\n"
    "s_add_u32 m0, s29, 32768\n"
    "s_add_u32 s14, s37, 229376\n"
    "buffer_load_dwordx4 v1, s[16:19], s14 offen offset:0 sc1 lds\n"
    "v_fma_f32 v157, v157, v154, v154\n"
    "v_mfma_f32_32x32x16_bf16 v[80:95], v[176:179], a[8:11], v[80:95]\n"
    "ds_read_b128 v[168:171], v1 offset:26624\n"
    "v_rcp_f32 v156, v156\n"
    "v_rcp_f32 v157, v157\n"
    "v_mul_f32 v36, v36, v156\n"
    "v_mfma_f32_32x32x16_bf16 v[80:95], v[180:183], a[12:15], v[80:95]\n"
    "ds_read_b128 v[172:175], v1 offset:27648\n"
    "v_mul_f32 v37, v37, v157\n"
    "v_cvt_pk_bf16_f32 v166, v36, v37\n"
    "v_fma_f32 v38, v126, v152, v38\n"
    "v_fma_f32 v46, v134, v152, v46\n"
    "s_waitcnt lgkmcnt(4)\n"
    "v_mfma_f32_32x32x16_bf16 v[80:95], v[184:187], a[16:19], v[80:95]\n"
    "ds_read_b128 v[176:179], v1 offset:28672\n"
    "buffer_load_dwordx4 v1, s[16:19], s14 offen offset:1024 sc1 lds\n"
    "v_mul_f32 v158, v38, v153\n"
    "v_fma_f32 v39, v127, v152, v39\n"
    "v_fma_f32 v47, v135, v152, v47\n"
    "v_mfma_f32_32x32x16_bf16 v[80:95], v[188:191], a[20:23], v[80:95]\n"
    "ds_read_b128 v[180:183], v1 offset:29696\n"
    "v_mul_f32 v159, v39, v153\n"
    "v_exp_f32 v158, v158\n"
    "v_exp_f32 v159, v159\n"
    "v_mfma_f32_32x32x16_bf16 v[80:95], v[192:195], a[24:27], v[80:95]\n"
    "ds_read_b128 v[184:187], v1 offset:30720\n"
    "v_mul_f32 v38, v38, v46\n"
    "v_mul_f32 v39, v39, v47\n"
    "v_fma_f32 v158, v158, v154, v154\n"
    "v_fma_f32 v159, v159, v154, v154\n"
    "v_mfma_f32_32x32x16_bf16 v[80:95], v[196:199], a[28:31], v[80:95]\n"
    "ds_read_b128 v[188:191], v1 offset:31744\n"
    "buffer_load_dwordx4 v1, s[16:19], s14 offen offset:2048 sc1 lds\n"
    "v_rcp_f32 v158, v158\n"
    "s_waitcnt lgkmcnt(4)\n"
    "v_mfma_f32_32x32x16_bf16 v[80:95], v[200:203], a[32:35], v[80:95]\n"
    "ds_read_b128 v[192:195], v2 offset:32768\n"
    "v_rcp_f32 v159, v159\n"
    "v_mul_f32 v38, v38, v158\n"
    "v_mul_f32 v39, v39, v159\n"
    "v_cvt_pk_bf16_f32 v167, v38, v39\n"
    "v_mfma_f32_32x32x16_bf16 v[80:95], v[204:207], a[36:39], v[80:95]\n"
    "ds_read_b128 v[196:199], v2 offset:33792\n"
    "s_add_u32 s35, s39, 1024\n"
    "buffer_store_dwordx4 v[164:167], v1, s[16:19], s35 offen sc1\n"
    "v_fma_f32 v48, v104, v152, v48\n"
    "v_fma_f32 v56, v112, v152, v56\n"
    "v_mfma_f32_32x32x16_bf16 v[80:95], v[168:171], a[40:43], v[80:95]\n"
    "ds_read_b128 v[200:203], v2 offset:34816\n"
    "buffer_load_dwordx4 v1, s[16:19], s14 offen offset:3072 sc1 lds\n"
    "v_mul_f32 v156, v48, v153\n"
    "v_fma_f32 v49, v105, v152, v49\n"
    "v_fma_f32 v57, v113, v152, v57\n"
    "v_mfma_f32_32x32x16_bf16 v[80:95], v[172:175], a[44:47], v[80:95]\n"
    "ds_read_b128 v[204:207], v2 offset:35840\n"
    "v_mul_f32 v157, v49, v153\n"
    "v_exp_f32 v156, v156\n"
    "s_waitcnt lgkmcnt(4)\n"
    "v_mfma_f32_32x32x16_bf16 v[80:95], v[176:179], a[48:51], v[80:95]\n"
    "ds_read_b128 v[168:171], v2 offset:36864\n"
    "v_exp_f32 v157, v157\n"
    "v_mul_f32 v48, v48, v56\n"
    "v_mul_f32 v49, v49, v57\n"
    "v_fma_f32 v156, v156, v154, v154\n"
    "v_mfma_f32_32x32x16_bf16 v[80:95], v[180:183], a[52:55], v[80:95]\n"
    "ds_read_b128 v[172:175], v2 offset:37888\n"
    "s_add_u32 m0, s29, 49152\n"
    "s_add_u32 s14, s37, 245760\n"
    "buffer_load_dwordx4 v1, s[16:19], s14 offen offset:0 sc1 lds\n"
    "v_fma_f32 v157, v157, v154, v154\n"
    "v_mfma_f32_32x32x16_bf16 v[80:95], v[184:187], a[56:59], v[80:95]\n"
    "ds_read_b128 v[176:179], v2 offset:38912\n"
    "v_rcp_f32 v156, v156\n"
    "v_rcp_f32 v157, v157\n"
    "v_mul_f32 v48, v48, v156\n"
    "v_mfma_f32_32x32x16_bf16 v[80:95], v[188:191], a[60:63], v[80:95]\n"
    "ds_read_b128 v[180:183], v2 offset:39936\n"
    "v_mul_f32 v49, v49, v157\n"
    "v_cvt_pk_bf16_f32 v160, v48, v49\n"
    "v_fma_f32 v50, v106, v152, v50\n"
    "v_fma_f32 v58, v114, v152, v58\n"
    "s_waitcnt lgkmcnt(4)\n"
    "v_mfma_f32_32x32x16_bf16 v[80:95], v[192:195], a[64:67], v[80:95]\n"
    "ds_read_b128 v[184:187], v2 offset:40960\n"
    "buffer_load_dwordx4 v1, s[16:19], s14 offen offset:1024 sc1 lds\n"
    "v_mul_f32 v158, v50, v153\n"
    "v_fma_f32 v51, v107, v152, v51\n"
    "v_fma_f32 v59, v115, v152, v59\n"
    "v_mfma_f32_32x32x16_bf16 v[80:95], v[196:199], a[68:71], v[80:95]\n"
    "ds_read_b128 v[188:191], v2 offset:41984\n"
    "v_mul_f32 v159, v51, v153\n"
    "v_exp_f32 v158, v158\n"
    "v_exp_f32 v159, v159\n"
    "v_mfma_f32_32x32x16_bf16 v[80:95], v[200:203], a[72:75], v[80:95]\n"
    "ds_read_b128 v[192:195], v2 offset:43008\n"
    "v_mul_f32 v50, v50, v58\n"
    "v_mul_f32 v51, v51, v59\n"
    "v_fma_f32 v158, v158, v154, v154\n"
    "v_fma_f32 v159, v159, v154, v154\n"
    "v_mfma_f32_32x32x16_bf16 v[80:95], v[204:207], a[76:79], v[80:95]\n"
    "ds_read_b128 v[196:199], v2 offset:44032\n"
    "buffer_load_dwordx4 v1, s[16:19], s14 offen offset:2048 sc1 lds\n"
    "v_rcp_f32 v158, v158\n"
    "s_waitcnt lgkmcnt(4)\n"
    "v_mfma_f32_32x32x16_bf16 v[80:95], v[168:171], a[80:83], v[80:95]\n"
    "ds_read_b128 v[200:203], v2 offset:45056\n"
    "v_rcp_f32 v159, v159\n"
    "v_mul_f32 v50, v50, v158\n"
    "v_mul_f32 v51, v51, v159\n"
    "v_cvt_pk_bf16_f32 v161, v50, v51\n"
    "v_mfma_f32_32x32x16_bf16 v[80:95], v[172:175], a[84:87], v[80:95]\n"
    "ds_read_b128 v[204:207], v2 offset:46080\n"
    "ds_read_b128 v[120:123], v11 offset:384\n"
    "ds_read_b128 v[124:127], v11 offset:400\n"
    "ds_read_b128 v[128:131], v11 offset:416\n"
    "ds_read_b128 v[132:135], v11 offset:432\n"
    "v_fma_f32 v52, v108, v152, v52\n"
    "v_mfma_f32_32x32x16_bf16 v[80:95], v[176:179], a[88:91], v[80:95]\n"
    "ds_read_b128 v[168:171], v2 offset:47104\n"
    "buffer_load_dwordx4 v1, s[16:19], s14 offen offset:3072 sc1 lds\n"
    "v_fma_f32 v60, v116, v152, v60\n"
    "v_mul_f32 v156, v52, v153\n"
    "v_fma_f32 v53, v109, v152, v53\n"
    "v_mfma_f32_32x32x16_bf16 v[80:95], v[180:183], a[92:95], v[80:95]\n"
    "ds_read_b128 v[172:175], v2 offset:48128\n"
    "v_fma_f32 v61, v117, v152, v61\n"
    "v_mul_f32 v157, v53, v153\n"
    "v_exp_f32 v156, v156\n"
    "s_waitcnt lgkmcnt(8)\n"
    "v_mfma_f32_32x32x16_bf16 v[80:95], v[184:187], a[96:99], v[80:95]\n"
    "s_waitcnt vmcnt(4)\n"
    "s_barrier\n"
    "ds_read_b128 v[176:179], v2 offset:49152\n"
    "v_exp_f32 v157, v157\n"
    "v_mul_f32 v52, v52, v60\n"
    "v_mfma_f32_32x32x16_bf16 v[80:95], v[188:191], a[100:103], v[80:95]\n"
    "ds_read_b128 v[180:183], v2 offset:50176\n"
    "s_add_u32 m0, s29, 65536\n"
    "s_add_u32 s14, s37, 262144\n"
    "buffer_load_dwordx4 v1, s[16:19], s14 offen offset:0 sc1 lds\n"
    "v_mul_f32 v53, v53, v61\n"
    "v_mfma_f32_32x32x16_bf16 v[80:95], v[192:195], a[104:107], v[80:95]\n"
    "ds_read_b128 v[184:187], v2 offset:51200\n"
    "v_fma_f32 v156, v156, v154, v154\n"
    "v_fma_f32 v157, v157, v154, v154\n"
    "v_rcp_f32 v156, v156\n"
    "v_mfma_f32_32x32x16_bf16 v[80:95], v[196:199], a[108:111], v[80:95]\n"
    "ds_read_b128 v[188:191], v2 offset:52224\n"
    "v_rcp_f32 v157, v157\n"
    "v_mul_f32 v52, v52, v156\n"
    "v_mul_f32 v53, v53, v157\n"
    "s_waitcnt lgkmcnt(4)\n"
    "v_mfma_f32_32x32x16_bf16 v[80:95], v[200:203], a[112:115], v[80:95]\n"
    "ds_read_b128 v[192:195], v2 offset:53248\n"
    "buffer_load_dwordx4 v1, s[16:19], s14 offen offset:1024 sc1 lds\n"
    "v_cvt_pk_bf16_f32 v162, v52, v53\n"
    "v_fma_f32 v54, v110, v152, v54\n"
    "v_fma_f32 v62, v118, v152, v62\n"
    "v_mfma_f32_32x32x16_bf16 v[80:95], v[204:207], a[116:119], v[80:95]\n"
    "ds_read_b128 v[196:199], v2 offset:54272\n"
    "v_mul_f32 v158, v54, v153\n"
    "v_fma_f32 v55, v111, v152, v55\n"
    "v_fma_f32 v63, v119, v152, v63\n"
    "v_mul_f32 v159, v55, v153\n"
    "v_mfma_f32_32x32x16_bf16 v[80:95], v[168:171], a[120:123], v[80:95]\n"
    "ds_read_b128 v[200:203], v2 offset:55296\n"
    "v_exp_f32 v158, v158\n"
    "v_exp_f32 v159, v159\n"
    "v_mul_f32 v54, v54, v62\n"
    "v_mfma_f32_32x32x16_bf16 v[80:95], v[172:175], a[124:127], v[80:95]\n"
    "ds_read_b128 v[204:207], v2 offset:56320\n"
    "buffer_load_dwordx4 v1, s[16:19], s14 offen offset:2048 sc1 lds\n"
    "v_mul_f32 v55, v55, v63\n"
    "v_fma_f32 v158, v158, v154, v154\n"
    "s_waitcnt lgkmcnt(4)\n"
    "v_mfma_f32_32x32x16_bf16 v[80:95], v[176:179], a[128:131], v[80:95]\n"
    "ds_read_b128 v[168:171], v2 offset:57344\n"
    "v_fma_f32 v159, v159, v154, v154\n"
    "v_rcp_f32 v158, v158\n"
    "v_rcp_f32 v159, v159\n"
    "v_mfma_f32_32x32x16_bf16 v[80:95], v[180:183], a[132:135], v[80:95]\n"
    "ds_read_b128 v[172:175], v2 offset:58368\n"
    "v_mul_f32 v54, v54, v158\n"
    "v_mul_f32 v55, v55, v159\n"
    "v_cvt_pk_bf16_f32 v163, v54, v55\n"
    "v_mfma_f32_32x32x16_bf16 v[80:95], v[184:187], a[136:139], v[80:95]\n"
    "ds_read_b128 v[176:179], v2 offset:59392\n"
    "buffer_load_dwordx4 v1, s[16:19], s14 offen offset:3072 sc1 lds\n"
    "s_add_u32 s35, s39, 32768\n"
    "buffer_store_dwordx4 v[160:163], v1, s[16:19], s35 offen sc1\n"
    "v_mfma_f32_32x32x16_bf16 v[80:95], v[188:191], a[140:143], v[80:95]\n"
    "ds_read_b128 v[180:183], v2 offset:60416\n"
    "v_fma_f32 v64, v120, v152, v64\n"
    "v_fma_f32 v72, v128, v152, v72\n"
    "v_mul_f32 v156, v64, v153\n"
    "v_fma_f32 v65, v121, v152, v65\n"
    "s_waitcnt lgkmcnt(4)\n"
    "v_mfma_f32_32x32x16_bf16 v[80:95], v[192:195], a[144:147], v[80:95]\n"
    "ds_read_b128 v[184:187], v2 offset:61440\n"
    "v_fma_f32 v73, v129, v152, v73\n"
    "v_mul_f32 v157, v65, v153\n"
    "v_exp_f32 v156, v156\n"
    "v_mfma_f32_32x32x16_bf16 v[80:95], v[196:199], a[148:151], v[80:95]\n"
    "ds_read_b128 v[188:191], v2 offset:62464\n"
    "s_add_u32 m0, s29, 81920\n"
    "s_add_u32 s14, s37, 278528\n"
    "buffer_load_dwordx4 v1, s[16:19], s14 offen offset:0 sc1 lds\n"
    "v_mfma_f32_32x32x16_bf16 v[80:95], v[200:203], a[152:155], v[80:95]\n"
    "ds_read_b128 v[192:195], v2 offset:63488\n"
    "v_exp_f32 v157, v157\n"
    "v_mul_f32 v64, v64, v72\n"
    "v_mul_f32 v65, v65, v73\n"
    "v_fma_f32 v156, v156, v154, v154\n"
    "v_mfma_f32_32x32x16_bf16 v[80:95], v[204:207], a[156:159], v[80:95]\n"
    "ds_read_b128 v[196:199], v2 offset:64512\n"
    "v_fma_f32 v157, v157, v154, v154\n"
    "v_rcp_f32 v156, v156\n"
    "s_waitcnt lgkmcnt(4)\n"
    "v_mfma_f32_32x32x16_bf16 v[16:31], v[168:171], a[0:3], 0\n"
    "ds_read_b128 v[200:203], v3 offset:0\n"
    "buffer_load_dwordx4 v1, s[16:19], s14 offen offset:1024 sc1 lds\n"
    "v_rcp_f32 v157, v157\n"
    "v_mul_f32 v64, v64, v156\n"
    "v_mfma_f32_32x32x16_bf16 v[16:31], v[172:175], a[4:7], v[16:31]\n"
    "ds_read_b128 v[204:207], v3 offset:1024\n"
    "v_mul_f32 v65, v65, v157\n"
    "v_cvt_pk_bf16_f32 v164, v64, v65\n"
    "v_fma_f32 v66, v122, v152, v66\n"
    "v_fma_f32 v74, v130, v152, v74\n"
    "v_mul_f32 v158, v66, v153\n"
    "v_mfma_f32_32x32x16_bf16 v[16:31], v[176:179], a[8:11], v[16:31]\n"
    "ds_read_b128 v[168:171], v3 offset:2048\n"
    "v_fma_f32 v67, v123, v152, v67\n"
    "v_fma_f32 v75, v131, v152, v75\n"
    "v_mul_f32 v159, v67, v153\n"
    "v_exp_f32 v158, v158\n"
    "v_mfma_f32_32x32x16_bf16 v[16:31], v[180:183], a[12:15], v[16:31]\n"
    "ds_read_b128 v[172:175], v3 offset:3072\n"
    "buffer_load_dwordx4 v1, s[16:19], s14 offen offset:2048 sc1 lds\n"
    "v_exp_f32 v159, v159\n"
    "s_waitcnt lgkmcnt(4)\n"
    "v_mfma_f32_32x32x16_bf16 v[16:31], v[184:187], a[16:19], v[16:31]\n"
    "ds_read_b128 v[176:179], v3 offset:4096\n"
    "v_mul_f32 v66, v66, v74\n"
    "v_mul_f32 v67, v67, v75\n"
    "v_fma_f32 v158, v158, v154, v154\n"
    "v_fma_f32 v159, v159, v154, v154\n"
    "v_mfma_f32_32x32x16_bf16 v[16:31], v[188:191], a[20:23], v[16:31]\n"
    "ds_read_b128 v[180:183], v3 offset:5120\n"
    "v_rcp_f32 v158, v158\n"
    "v_rcp_f32 v159, v159\n"
    "v_mul_f32 v66, v66, v158\n"
    "v_mfma_f32_32x32x16_bf16 v[16:31], v[192:195], a[24:27], v[16:31]\n"
    "ds_read_b128 v[184:187], v3 offset:6144\n"
    "buffer_load_dwordx4 v1, s[16:19], s14 offen offset:3072 sc1 lds\n"
    "v_mul_f32 v67, v67, v159\n"
    "v_cvt_pk_bf16_f32 v165, v66, v67\n"
    "ds_read_b128 v[104:107], v11 offset:512\n"
    "v_mfma_f32_32x32x16_bf16 v[16:31], v[196:199], a[28:31], v[16:31]\n"
    "ds_read_b128 v[188:191], v3 offset:7168\n"
    "ds_read_b128 v[108:111], v11 offset:528\n"
    "ds_read_b128 v[112:115], v11 offset:544\n"
    "ds_read_b128 v[116:119], v11 offset:560\n"
    "v_fma_f32 v68, v124, v152, v68\n"
    "s_waitcnt lgkmcnt(8)\n"
    "v_mfma_f32_32x32x16_bf16 v[16:31], v[200:203], a[32:35], v[16:31]\n"
    "ds_read_b128 v[192:195], v3 offset:8192\n"
    "v_fma_f32 v76, v132, v152, v76\n"
    "v_mul_f32 v156, v68, v153\n"
    "v_fma_f32 v69, v125, v152, v69\n"
    "v_fma_f32 v77, v133, v152, v77\n"
    "v_mul_f32 v157, v69, v153\n"
    "v_mfma_f32_32x32x16_bf16 v[16:31], v[204:207], a[36:39], v[16:31]\n"
    "ds_read_b128 v[196:199], v3 offset:9216\n"
    "s_add_u32 m0, s93, 0\n"
    "s_add_u32 s92, s94, 0\n"
    "buffer_load_dwordx4 v1, s[16:19], s92 offen sc1 lds\n"
    "v_mfma_f32_32x32x16_bf16 v[16:31], v[168:171], a[40:43], v[16:31]\n"
    "ds_read_b128 v[200:203], v3 offset:10240\n"
    "v_exp_f32 v156, v156\n"
    "v_exp_f32 v157, v157\n"
    "v_mul_f32 v68, v68, v76\n"
    "v_mfma_f32_32x32x16_bf16 v[16:31], v[172:175], a[44:47], v[16:31]\n"
    "ds_read_b128 v[204:207], v3 offset:11264\n"
    "v_mul_f32 v69, v69, v77\n"
    "v_fma_f32 v156, v156, v154, v154\n"
    "v_fma_f32 v157, v157, v154, v154\n"
    "s_waitcnt lgkmcnt(7)\n"
    "v_mfma_f32_32x32x16_bf16 v[16:31], v[176:179], a[48:51], v[16:31]\n"
    "ds_read_b128 v[168:171], v3 offset:12288\n"
    "s_add_u32 m0, s93, 4096\n"
    "s_add_u32 s92, s94, 4096\n"
    "buffer_load_dwordx4 v1, s[16:19], s92 offen sc1 lds\n"
    "v_mfma_f32_32x32x16_bf16 v[16:31], v[180:183], a[52:55], v[16:31]\n"
    "ds_read_b128 v[172:175], v3 offset:13312\n"
    "v_rcp_f32 v156, v156\n"
    "v_rcp_f32 v157, v157\n"
    "v_mul_f32 v68, v68, v156\n"
    "v_mfma_f32_32x32x16_bf16 v[16:31], v[184:187], a[56:59], v[16:31]\n"
    "ds_read_b128 v[176:179], v3 offset:14336\n"
    "v_mul_f32 v69, v69, v157\n"
    "v_cvt_pk_bf16_f32 v166, v68, v69\n"
    "v_fma_f32 v70, v126, v152, v70\n"
    "v_fma_f32 v78, v134, v152, v78\n"
    "v_mul_f32 v158, v70, v153\n"
    "v_mfma_f32_32x32x16_bf16 v[16:31], v[188:191], a[60:63], v[16:31]\n"
    "ds_read_b128 v[180:183], v3 offset:15360\n"
    "s_add_u32 m0, s93, 8192\n"
    "s_add_u32 s92, s94, 8192\n"
    "buffer_load_dwordx4 v1, s[16:19], s92 offen sc1 lds\n"
    "s_waitcnt lgkmcnt(4)\n"
    "v_mfma_f32_32x32x16_bf16 v[16:31], v[192:195], a[64:67], v[16:31]\n"
    "ds_read_b128 v[184:187], v1 offset:32768\n"
    "v_fma_f32 v71, v127, v152, v71\n"
    "v_fma_f32 v79, v135, v152, v79\n"
    "v_mul_f32 v159, v71, v153\n"
    "v_exp_f32 v158, v158\n"
    "v_mfma_f32_32x32x16_bf16 v[16:31], v[196:199], a[68:71], v[16:31]\n"
    "ds_read_b128 v[188:191], v1 offset:33792\n"
    "v_exp_f32 v159, v159\n"
    "v_mul_f32 v70, v70, v78\n"
    "v_mul_f32 v71, v71, v79\n"
    "v_fma_f32 v158, v158, v154, v154\n"
    "v_mfma_f32_32x32x16_bf16 v[16:31], v[200:203], a[72:75], v[16:31]\n"
    "ds_read_b128 v[192:195], v1 offset:34816\n"
    "s_add_u32 m0, s93, 12288\n"
    "s_add_u32 s92, s94, 12288\n"
    "buffer_load_dwordx4 v1, s[16:19], s92 offen sc1 lds\n"
    "v_fma_f32 v159, v159, v154, v154\n"
    "v_mfma_f32_32x32x16_bf16 v[16:31], v[204:207], a[76:79], v[16:31]\n"
    "ds_read_b128 v[196:199], v1 offset:35840\n"
    "v_rcp_f32 v158, v158\n"
    "v_rcp_f32 v159, v159\n"
    "s_waitcnt lgkmcnt(4)\n"
    "v_mfma_f32_32x32x16_bf16 v[16:31], v[168:171], a[80:83], v[16:31]\n"
    "ds_read_b128 v[200:203], v1 offset:36864\n"
    "v_mul_f32 v70, v70, v158\n"
    "v_mul_f32 v71, v71, v159\n"
    "v_cvt_pk_bf16_f32 v167, v70, v71\n"
    "v_mfma_f32_32x32x16_bf16 v[16:31], v[172:175], a[84:87], v[16:31]\n"
    "ds_read_b128 v[204:207], v1 offset:37888\n"
    "s_add_u32 m0, s93, 16384\n"
    "s_add_u32 s92, s94, 15360\n"
    "buffer_load_dwordx4 v1, s[16:19], s92 offen sc1 lds\n"
    "v_mfma_f32_32x32x16_bf16 v[16:31], v[176:179], a[88:91], v[16:31]\n"
    "ds_read_b128 v[168:171], v1 offset:38912\n"
    "s_add_u32 s35, s39, 33792\n"
    "buffer_store_dwordx4 v[164:167], v1, s[16:19], s35 offen sc1\n"
    "v_fma_f32 v80, v104, v152, v80\n"
    "v_fma_f32 v88, v112, v152, v88\n"
    "v_mfma_f32_32x32x16_bf16 v[16:31], v[180:183], a[92:95], v[16:31]\n"
    "ds_read_b128 v[172:175], v1 offset:39936\n"
    "v_mul_f32 v156, v80, v153\n"
    "v_fma_f32 v81, v105, v152, v81\n"
    "v_fma_f32 v89, v113, v152, v89\n"
    "v_mul_f32 v157, v81, v153\n"
    "s_waitcnt lgkmcnt(4)\n"
    "v_mfma_f32_32x32x16_bf16 v[16:31], v[184:187], a[96:99], v[16:31]\n"
    "ds_read_b128 v[176:179], v1 offset:40960\n"
    "s_add_u32 m0, s93, 20480\n"
    "s_add_u32 s92, s94, 19456\n"
    "buffer_load_dwordx4 v1, s[16:19], s92 offen sc1 lds\n"
    "v_mfma_f32_32x32x16_bf16 v[16:31], v[188:191], a[100:103], v[16:31]\n"
    "ds_read_b128 v[180:183], v1 offset:41984\n"
    "v_exp_f32 v156, v156\n"
    "v_exp_f32 v157, v157\n"
    "v_mul_f32 v80, v80, v88\n"
    "v_mfma_f32_32x32x16_bf16 v[16:31], v[192:195], a[104:107], v[16:31]\n"
    "ds_read_b128 v[184:187], v1 offset:43008\n"
    "v_mul_f32 v81, v81, v89\n"
    "v_fma_f32 v156, v156, v154, v154\n"
    "v_fma_f32 v157, v157, v154, v154\n"
    "v_rcp_f32 v156, v156\n"
    "v_mfma_f32_32x32x16_bf16 v[16:31], v[196:199], a[108:111], v[16:31]\n"
    "ds_read_b128 v[188:191], v1 offset:44032\n"
    "s_add_u32 m0, s93, 24576\n"
    "s_add_u32 s92, s94, 23552\n"
    "buffer_load_dwordx4 v1, s[16:19], s92 offen sc1 lds\n"
    "s_waitcnt lgkmcnt(4)\n"
    "v_mfma_f32_32x32x16_bf16 v[16:31], v[200:203], a[112:115], v[16:31]\n"
    "ds_read_b128 v[192:195], v1 offset:45056\n"
    "v_rcp_f32 v157, v157\n"
    "v_mul_f32 v80, v80, v156\n"
    "v_mul_f32 v81, v81, v157\n"
    "v_cvt_pk_bf16_f32 v160, v80, v81\n"
    "v_mfma_f32_32x32x16_bf16 v[16:31], v[204:207], a[116:119], v[16:31]\n"
    "ds_read_b128 v[196:199], v1 offset:46080\n"
    "v_fma_f32 v82, v106, v152, v82\n"
    "v_fma_f32 v90, v114, v152, v90\n"
    "v_mul_f32 v158, v82, v153\n"
    "v_fma_f32 v83, v107, v152, v83\n"
    "v_fma_f32 v91, v115, v152, v91\n"
    "v_mfma_f32_32x32x16_bf16 v[16:31], v[168:171], a[120:123], v[16:31]\n"
    "ds_read_b128 v[200:203], v1 offset:47104\n"
    "s_add_u32 m0, s93, 28672\n"
    "s_add_u32 s92, s94, 27648\n"
    "buffer_load_dwordx4 v1, s[16:19], s92 offen sc1 lds\n"
    "v_mul_f32 v159, v83, v153\n"
    "v_mfma_f32_32x32x16_bf16 v[16:31], v[172:175], a[124:127], v[16:31]\n"
    "ds_read_b128 v[204:207], v1 offset:48128\n"
    "v_exp_f32 v158, v158\n"
    "v_exp_f32 v159, v159\n"
    "s_waitcnt lgkmcnt(4)\n"
    "v_mfma_f32_32x32x16_bf16 v[16:31], v[176:179], a[128:131], v[16:31]\n"
    "s_waitcnt vmcnt(14)\n"
    "s_barrier\n"
    "ds_read_b128 v[168:171], v1 offset:49152\n"
    "v_mul_f32 v82, v82, v90\n"
    "v_mul_f32 v83, v83, v91\n"
    "v_fma_f32 v158, v158, v154, v154\n"
    "v_mfma_f32_32x32x16_bf16 v[16:31], v[180:183], a[132:135], v[16:31]\n"
    "ds_read_b128 v[172:175], v1 offset:50176\n"
    "s_add_u32 m0, s93, 98304\n"
    "s_add_u32 s92, s94, 30720\n"
    "buffer_load_dwordx4 v1, s[16:19], s92 offen sc1 lds\n"
    "v_fma_f32 v159, v159, v154, v154\n"
    "v_mfma_f32_32x32x16_bf16 v[16:31], v[184:187], a[136:139], v[16:31]\n"
    "ds_read_b128 v[176:179], v1 offset:51200\n"
    "v_rcp_f32 v158, v158\n"
    "v_rcp_f32 v159, v159\n"
    "v_mul_f32 v82, v82, v158\n"
    "v_mfma_f32_32x32x16_bf16 v[16:31], v[188:191], a[140:143], v[16:31]\n"
    "ds_read_b128 v[180:183], v1 offset:52224\n"
    "v_mul_f32 v83, v83, v159\n"
    "v_cvt_pk_bf16_f32 v161, v82, v83\n"
    "ds_read_b128 v[120:123], v11 offset:640\n"
    "ds_read_b128 v[124:127], v11 offset:656\n"
    "s_waitcnt lgkmcnt(6)\n"
    "v_mfma_f32_32x32x16_bf16 v[16:31], v[192:195], a[144:147], v[16:31]\n"
    "ds_read_b128 v[184:187], v1 offset:53248\n"
    "s_add_u32 m0, s93, 102400\n"
    "s_add_u32 s92, s94, 34816\n"
    "buffer_load_dwordx4 v1, s[16:19], s92 offen sc1 lds\n"
    "ds_read_b128 v[128:131], v11 offset:672\n"
    "v_mfma_f32_32x32x16_bf16 v[16:31], v[196:199], a[148:151], v[16:31]\n"
    "ds_read_b128 v[188:191], v1 offset:54272\n"
    "ds_read_b128 v[132:135], v11 offset:688\n"
    "v_fma_f32 v84, v108, v152, v84\n"
    "v_fma_f32 v92, v116, v152, v92\n"
    "v_mul_f32 v156, v84, v153\n"
    "v_fma_f32 v85, v109, v152, v85\n"
    "v_mfma_f32_32x32x16_bf16 v[16:31], v[200:203], a[152:155], v[16:31]\n"
    "ds_read_b128 v[192:195], v1 offset:55296\n"
    "v_fma_f32 v93, v117, v152, v93\n"
    "v_mul_f32 v157, v85, v153\n"
    "v_exp_f32 v156, v156\n"
    "v_mfma_f32_32x32x16_bf16 v[16:31], v[204:207], a[156:159], v[16:31]\n"
    "ds_read_b128 v[196:199], v1 offset:56320\n"
    "s_add_u32 m0, s93, 106496\n"
    "s_add_u32 s92, s94, 38912\n"
    "buffer_load_dwordx4 v1, s[16:19], s92 offen sc1 lds\n"
    "s_waitcnt lgkmcnt(8)\n"
    "v_mfma_f32_32x32x16_bf16 v[32:47], v[168:171], a[0:3], 0\n"
    "ds_read_b128 v[200:203], v1 offset:57344\n"
    "v_exp_f32 v157, v157\n"
    "v_mul_f32 v84, v84, v92\n"
    "v_mul_f32 v85, v85, v93\n"
    "v_fma_f32 v156, v156, v154, v154\n"
    "v_mfma_f32_32x32x16_bf16 v[32:47], v[172:175], a[4:7], v[32:47]\n"
    "ds_read_b128 v[204:207], v1 offset:58368\n"
    "v_fma_f32 v157, v157, v154, v154\n"
    "v_rcp_f32 v156, v156\n"
    "v_rcp_f32 v157, v157\n"
    "v_mfma_f32_32x32x16_bf16 v[32:47], v[176:179], a[8:11], v[32:47]\n"
    "ds_read_b128 v[168:171], v1 offset:59392\n"
    "s_add_u32 m0, s93, 110592\n"
    "s_add_u32 s92, s94, 43008\n"
    "buffer_load_dwordx4 v1, s[16:19], s92 offen sc1 lds\n"
    "v_mul_f32 v84, v84, v156\n"
    "v_mfma_f32_32x32x16_bf16 v[32:47], v[180:183], a[12:15], v[32:47]\n"
    "ds_read_b128 v[172:175], v1 offset:60416\n"
    "v_mul_f32 v85, v85, v157\n"
    "v_cvt_pk_bf16_f32 v162, v84, v85\n"
    "v_fma_f32 v86, v110, v152, v86\n"
    "v_fma_f32 v94, v118, v152, v94\n"
    "s_waitcnt lgkmcnt(4)\n"
    "v_mfma_f32_32x32x16_bf16 v[32:47], v[184:187], a[16:19], v[32:47]\n"
    "ds_read_b128 v[176:179], v1 offset:61440\n"
    "v_mul_f32 v158, v86, v153\n"
    "v_fma_f32 v87, v111, v152, v87\n"
    "v_fma_f32 v95, v119, v152, v95\n"
    "v_mul_f32 v159, v87, v153\n"
    "v_mfma_f32_32x32x16_bf16 v[32:47], v[188:191], a[20:23], v[32:47]\n"
    "ds_read_b128 v[180:183], v1 offset:62464\n"
    "s_add_u32 m0, s93, 114688\n"
    "s_add_u32 s92, s94, 46080\n"
    "buffer_load_dwordx4 v1, s[16:19], s92 offen sc1 lds\n"
    "v_mfma_f32_32x32x16_bf16 v[32:47], v[192:195], a[24:27], v[32:47]\n"
    "ds_read_b128 v[184:187], v1 offset:63488\n"
    "v_exp_f32 v158, v158\n"
    "v_exp_f32 v159, v159\n"
    "v_mul_f32 v86, v86, v94\n"
    "v_mfma_f32_32x32x16_bf16 v[32:47], v[196:199], a[28:31], v[32:47]\n"
    "ds_read_b128 v[188:191], v1 offset:64512\n"
    "v_mul_f32 v87, v87, v95\n"
    "v_fma_f32 v158, v158, v154, v154\n"
    "v_fma_f32 v159, v159, v154, v154\n"
    "s_waitcnt lgkmcnt(4)\n"
    "v_mfma_f32_32x32x16_bf16 v[32:47], v[200:203], a[32:35], v[32:47]\n"
    "ds_read_b128 v[192:195], v2 offset:0\n"
    "s_add_u32 m0, s93, 118784\n"
    "s_add_u32 s92, s94, 50176\n"
    "buffer_load_dwordx4 v1, s[16:19], s92 offen sc1 lds\n"
    "v_mfma_f32_32x32x16_bf16 v[32:47], v[204:207], a[36:39], v[32:47]\n"
    "ds_read_b128 v[196:199], v2 offset:1024\n"
    "v_rcp_f32 v158, v158\n"
    "v_rcp_f32 v159, v159\n"
    "v_mul_f32 v86, v86, v158\n"
    "v_mfma_f32_32x32x16_bf16 v[32:47], v[168:171], a[40:43], v[32:47]\n"
    "ds_read_b128 v[200:203], v2 offset:2048\n"
    "v_mul_f32 v87, v87, v159\n"
    "v_cvt_pk_bf16_f32 v163, v86, v87\n"
    "s_add_u32 s35, s90, 4096\n"
    "buffer_store_dwordx4 v[160:163], v1, s[16:19], s35 offen sc1\n"
    "v_mfma_f32_32x32x16_bf16 v[32:47], v[172:175], a[44:47], v[32:47]\n"
    "ds_read_b128 v[204:207], v2 offset:3072\n"
    "s_add_u32 m0, s93, 122880\n"
    "s_add_u32 s92, s94, 54272\n"
    "buffer_load_dwordx4 v1, s[16:19], s92 offen sc1 lds\n"
    "s_waitcnt lgkmcnt(4)\n"
    "v_mfma_f32_32x32x16_bf16 v[32:47], v[176:179], a[48:51], v[32:47]\n"
    "ds_read_b128 v[168:171], v2 offset:4096\n"
    "v_fma_f32 v16, v120, v152, v16\n"
    "v_fma_f32 v24, v128, v152, v24\n"
    "v_mul_f32 v156, v16, v153\n"
    "v_fma_f32 v17, v121, v152, v17\n"
    "v_fma_f32 v25, v129, v152, v25\n"
    "v_mfma_f32_32x32x16_bf16 v[32:47], v[180:183], a[52:55], v[32:47]\n"
    "ds_read_b128 v[172:175], v2 offset:5120\n"
    "v_mul_f32 v157, v17, v153\n"
    "v_exp_f32 v156, v156\n"
    "v_exp_f32 v157, v157\n"
    "v_mfma_f32_32x32x16_bf16 v[32:47], v[184:187], a[56:59], v[32:47]\n"
    "ds_read_b128 v[176:179], v2 offset:6144\n"
    "s_add_u32 m0, s93, 126976\n"
    "s_add_u32 s92, s94, 58368\n"
    "buffer_load_dwordx4 v1, s[16:19], s92 offen sc1 lds\n"
    "v_mul_f32 v16, v16, v24\n"
    "v_mfma_f32_32x32x16_bf16 v[32:47], v[188:191], a[60:63], v[32:47]\n"
    "ds_read_b128 v[180:183], v2 offset:7168\n"
    "v_mul_f32 v17, v17, v25\n"
    "v_fma_f32 v156, v156, v154, v154\n"
    "v_fma_f32 v157, v157, v154, v154\n"
    "s_waitcnt lgkmcnt(4)\n"
    "v_mfma_f32_32x32x16_bf16 v[32:47], v[192:195], a[64:67], v[32:47]\n"
    "ds_read_b128 v[184:187], v2 offset:8192\n"
    "v_rcp_f32 v156, v156\n"
    "v_rcp_f32 v157, v157\n"
    "v_mul_f32 v16, v16, v156\n"
    "v_mfma_f32_32x32x16_bf16 v[32:47], v[196:199], a[68:71], v[32:47]\n"
    "ds_read_b128 v[188:191], v2 offset:9216\n"
    "s_add_u32 m0, s93, 131072\n"
    "s_add_u32 s92, s94, 61440\n"
    "buffer_load_dwordx4 v1, s[16:19], s92 offen sc1 lds\n"
    "v_mul_f32 v17, v17, v157\n"
    "v_mfma_f32_32x32x16_bf16 v[32:47], v[200:203], a[72:75], v[32:47]\n"
    "ds_read_b128 v[192:195], v2 offset:10240\n"
    "v_cvt_pk_bf16_f32 v164, v16, v17\n"
    "v_fma_f32 v18, v122, v152, v18\n"
    "v_fma_f32 v26, v130, v152, v26\n"
    "v_mul_f32 v158, v18, v153\n"
    "v_fma_f32 v19, v123, v152, v19\n"
    "v_mfma_f32_32x32x16_bf16 v[32:47], v[204:207], a[76:79], v[32:47]\n"
    "ds_read_b128 v[196:199], v2 offset:11264\n"
    "v_fma_f32 v27, v131, v152, v27\n"
    "v_mul_f32 v159, v19, v153\n"
    "v_exp_f32 v158, v158\n"
    "s_waitcnt lgkmcnt(4)\n"
    "v_mfma_f32_32x32x16_bf16 v[32:47], v[168:171], a[80:83], v[32:47]\n"
    "ds_read_b128 v[200:203], v2 offset:12288\n"
    "s_add_u32 m0, s93, 135168\n"
    "s_add_u32 s92, s94, 65536\n"
    "buffer_load_dwordx4 v1, s[16:19], s92 offen sc1 lds\n"
    "v_mfma_f32_32x32x16_bf16 v[32:47], v[172:175], a[84:87], v[32:47]\n"
    "ds_read_b128 v[204:207], v2 offset:13312\n"
    "v_exp_f32 v159, v159\n"
    "v_mul_f32 v18, v18, v26\n"
    "v_mul_f32 v19, v19, v27\n"
    "v_fma_f32 v158, v158, v154, v154\n"
    "v_mfma_f32_32x32x16_bf16 v[32:47], v[176:179], a[88:91], v[32:47]\n"
    "ds_read_b128 v[168:171], v2 offset:14336\n"
    "v_fma_f32 v159, v159, v154, v154\n"
    "v_rcp_f32 v158, v158\n"
    "v_rcp_f32 v159, v159\n"
    "v_mfma_f32_32x32x16_bf16 v[32:47], v[180:183], a[92:95], v[32:47]\n"
    "ds_read_b128 v[172:175], v2 offset:15360\n"
    "s_add_u32 m0, s93, 139264\n"
    "s_add_u32 s92, s94, 69632\n"
    "buffer_load_dwordx4 v1, s[16:19], s92 offen sc1 lds\n"
    "s_waitcnt lgkmcnt(4)\n"
    "v_mfma_f32_32x32x16_bf16 v[32:47], v[184:187], a[96:99], v[32:47]\n"
    "s_waitcnt vmcnt(21)\n"
    "s_barrier\n"
    "ds_read_b128 v[176:179], v2 offset:16384\n"
    "v_mul_f32 v18, v18, v158\n"
    "v_mul_f32 v19, v19, v159\n"
    "v_cvt_pk_bf16_f32 v165, v18, v19\n"
    "v_mfma_f32_32x32x16_bf16 v[32:47], v[188:191], a[100:103], v[32:47]\n"
    "ds_read_b128 v[180:183], v2 offset:17408\n"
    "ds_read_b128 v[104:107], v11 offset:768\n"
    "ds_read_b128 v[108:111], v11 offset:784\n"
    "ds_read_b128 v[112:115], v11 offset:800\n"
    "ds_read_b128 v[116:119], v11 offset:816\n"
    "v_fma_f32 v20, v124, v152, v20\n"
    "v_mfma_f32_32x32x16_bf16 v[32:47], v[192:195], a[104:107], v[32:47]\n"
    "ds_read_b128 v[184:187], v2 offset:18432\n"
    "s_add_u32 m0, s93, 143360\n"
    "s_add_u32 s92, s94, 73728\n"
    "buffer_load_dwordx4 v1, s[16:19], s92 offen sc1 lds\n"
    "v_fma_f32 v28, v132, v152, v28\n"
    "v_mfma_f32_32x32x16_bf16 v[32:47], v[196:199], a[108:111], v[32:47]\n"
    "ds_read_b128 v[188:191], v2 offset:19456\n"
    "v_mul_f32 v156, v20, v153\n"
    "v_fma_f32 v21, v125, v152, v21\n"
    "v_fma_f32 v29, v133, v152, v29\n"
    "v_mul_f32 v157, v21, v153\n"
    "s_waitcnt lgkmcnt(8)\n"
    "v_mfma_f32_32x32x16_bf16 v[32:47], v[200:203], a[112:115], v[32:47]\n"
    "ds_read_b128 v[192:195], v2 offset:20480\n"
    "v_exp_f32 v156, v156\n"
    "v_exp_f32 v157, v157\n"
    "v_mul_f32 v20, v20, v28\n"
    "v_mfma_f32_32x32x16_bf16 v[32:47], v[204:207], a[116:119], v[32:47]\n"
    "ds_read_b128 v[196:199], v2 offset:21504\n"
    "s_add_u32 m0, s93, 32768\n"
    "s_add_u32 s92, s94, 76800\n"
    "buffer_load_dwordx4 v1, s[16:19], s92 offen sc1 lds\n"
    "v_mul_f32 v21, v21, v29\n"
    "v_mfma_f32_32x32x16_bf16 v[32:47], v[168:171], a[120:123], v[32:47]\n"
    "ds_read_b128 v[200:203], v2 offset:22528\n"
    "v_fma_f32 v156, v156, v154, v154\n"
    "v_fma_f32 v157, v157, v154, v154\n"
    "v_rcp_f32 v156, v156\n"
    "v_mfma_f32_32x32x16_bf16 v[32:47], v[172:175], a[124:127], v[32:47]\n"
    "ds_read_b128 v[204:207], v2 offset:23552\n"
    "v_rcp_f32 v157, v157\n"
    "v_mul_f32 v20, v20, v156\n"
    "v_mul_f32 v21, v21, v157\n"
    "s_waitcnt lgkmcnt(4)\n"
    "v_mfma_f32_32x32x16_bf16 v[32:47], v[176:179], a[128:131], v[32:47]\n"
    "s_add_u32 m0, s93, 36864\n"
    "s_add_u32 s92, s94, 80896\n"
    "buffer_load_dwordx4 v1, s[16:19], s92 offen sc1 lds\n"
    "v_cvt_pk_bf16_f32 v166, v20, v21\n"
    "v_fma_f32 v22, v126, v152, v22\n"
    "v_mfma_f32_32x32x16_bf16 v[32:47], v[180:183], a[132:135], v[32:47]\n"
    "v_fma_f32 v30, v134, v152, v30\n"
    "v_mul_f32 v158, v22, v153\n"
    "v_fma_f32 v23, v127, v152, v23\n"
    "v_fma_f32 v31, v135, v152, v31\n"
    "v_mul_f32 v159, v23, v153\n"
    "v_mfma_f32_32x32x16_bf16 v[32:47], v[184:187], a[136:139], v[32:47]\n"
    "v_exp_f32 v158, v158\n"
    "v_exp_f32 v159, v159\n"
    "v_mul_f32 v22, v22, v30\n"
    "v_mul_f32 v23, v23, v31\n"
    "v_mfma_f32_32x32x16_bf16 v[32:47], v[188:191], a[140:143], v[32:47]\n"
    "s_add_u32 m0, s93, 40960\n"
    "s_add_u32 s92, s94, 84992\n"
    "buffer_load_dwordx4 v1, s[16:19], s92 offen sc1 lds\n"
    "v_fma_f32 v158, v158, v154, v154\n"
    "s_waitcnt lgkmcnt(0)\n"
    "v_mfma_f32_32x32x16_bf16 v[32:47], v[192:195], a[144:147], v[32:47]\n"
    "v_fma_f32 v159, v159, v154, v154\n"
    "v_rcp_f32 v158, v158\n"
    "v_rcp_f32 v159, v159\n"
    "v_mul_f32 v22, v22, v158\n"
    "v_mfma_f32_32x32x16_bf16 v[32:47], v[196:199], a[148:151], v[32:47]\n"
    "v_mul_f32 v23, v23, v159\n"
    "v_cvt_pk_bf16_f32 v167, v22, v23\n"
    "s_add_u32 s35, s90, 5120\n"
    "buffer_store_dwordx4 v[164:167], v1, s[16:19], s35 offen sc1\n"
    "v_mfma_f32_32x32x16_bf16 v[32:47], v[200:203], a[152:155], v[32:47]\n"
    "s_add_u32 m0, s93, 45056\n"
    "s_add_u32 s92, s94, 89088\n"
    "buffer_load_dwordx4 v1, s[16:19], s92 offen sc1 lds\n"
    "v_mfma_f32_32x32x16_bf16 v[32:47], v[204:207], a[156:159], v[32:47]\n"
    "s_nop 3\n"
    "s_nop 7\n"
    "v_pk_fma_f32 v[32:33], v[104:105], v[152:153], v[32:33] op_sel_hi:[1,0,1]\n"
    "v_pk_fma_f32 v[34:35], v[106:107], v[152:153], v[34:35] op_sel_hi:[1,0,1]\n"
    "v_pk_fma_f32 v[36:37], v[108:109], v[152:153], v[36:37] op_sel_hi:[1,0,1]\n"
    "v_pk_fma_f32 v[38:39], v[110:111], v[152:153], v[38:39] op_sel_hi:[1,0,1]\n"
    "v_pk_fma_f32 v[40:41], v[112:113], v[152:153], v[40:41] op_sel_hi:[1,0,1]\n"
    "v_pk_fma_f32 v[42:43], v[114:115], v[152:153], v[42:43] op_sel_hi:[1,0,1]\n"
    "v_pk_fma_f32 v[44:45], v[116:117], v[152:153], v[44:45] op_sel_hi:[1,0,1]\n"
    "v_pk_fma_f32 v[46:47], v[118:119], v[152:153], v[46:47] op_sel_hi:[1,0,1]\n"
    "v_pk_mul_f32 v[208:209], v[32:33], v[152:153] op_sel:[0,1] op_sel_hi:[1,1]\n"
    "v_pk_mul_f32 v[210:211], v[34:35], v[152:153] op_sel:[0,1] op_sel_hi:[1,1]\n"
    "v_pk_mul_f32 v[212:213], v[36:37], v[152:153] op_sel:[0,1] op_sel_hi:[1,1]\n"
    "v_pk_mul_f32 v[214:215], v[38:39], v[152:153] op_sel:[0,1] op_sel_hi:[1,1]\n"
    "v_exp_f32 v208, v208\n"
    "v_exp_f32 v209, v209\n"
    "v_exp_f32 v210, v210\n"
    "v_exp_f32 v211, v211\n"
    "v_exp_f32 v212, v212\n"
    "v_exp_f32 v213, v213\n"
    "v_exp_f32 v214, v214\n"
    "v_exp_f32 v215, v215\n"
    "v_pk_mul_f32 v[32:33], v[32:33], v[40:41]\n"
    "v_pk_mul_f32 v[34:35], v[34:35], v[42:43]\n"
    "v_pk_mul_f32 v[36:37], v[36:37], v[44:45]\n"
    "v_pk_mul_f32 v[38:39], v[38:39], v[46:47]\n"
    "v_pk_fma_f32 v[208:209], v[208:209], v[154:155], v[154:155] op_sel_hi:[1,0,0]\n"
    "v_pk_fma_f32 v[210:211], v[210:211], v[154:155], v[154:155] op_sel_hi:[1,0,0]\n"
    "v_pk_fma_f32 v[212:213], v[212:213], v[154:155], v[154:155] op_sel_hi:[1,0,0]\n"
    "v_pk_fma_f32 v[214:215], v[214:215], v[154:155], v[154:155] op_sel_hi:[1,0,0]\n"
    "v_rcp_f32 v208, v208\n"
    "v_rcp_f32 v209, v209\n"
    "v_rcp_f32 v210, v210\n"
    "v_rcp_f32 v211, v211\n"
    "v_rcp_f32 v212, v212\n"
    "v_rcp_f32 v213, v213\n"
    "v_rcp_f32 v214, v214\n"
    "v_rcp_f32 v215, v215\n"
    "v_pk_mul_f32 v[32:33], v[32:33], v[208:209]\n"
    "v_pk_mul_f32 v[34:35], v[34:35], v[210:211]\n"
    "v_pk_mul_f32 v[36:37], v[36:37], v[212:213]\n"
    "v_pk_mul_f32 v[38:39], v[38:39], v[214:215]\n"
    "v_cvt_pk_bf16_f32 v160, v32, v33\n"
    "v_cvt_pk_bf16_f32 v161, v34, v35\n"
    "v_cvt_pk_bf16_f32 v162, v36, v37\n"
    "v_cvt_pk_bf16_f32 v163, v38, v39\n"
    "buffer_store_dwordx4 v[160:163], v1, s[16:19], s13 offen sc1\n"
    "s_waitcnt lgkmcnt(0)\n"
    "s_barrier\n"
    "s_cmp_eq_u32 s26, 0\n"
    "s_cbranch_scc1 Lt1_62_%=\n"
    "s_sub_u32 s35, s94, s93\n"
    "s_add_u32 s35, s35, 92160\n"
    "s_sub_u32 s30, s93, 1024\n"
    "s_add_u32 s31, s30, s35\n"
    "s_add_u32 m0, s30, 49152\n"
    "s_add_u32 s14, s31, 0\n"
    "buffer_load_dwordx4 v1, s[16:19], s14 offen sc1 lds\n"
    "s_add_u32 m0, s30, 52224\n"
    "s_add_u32 s14, s31, 3072\n"
    "buffer_load_dwordx4 v1, s[16:19], s14 offen sc1 lds\n"
    "s_add_u32 m0, s30, 55296\n"
    "s_add_u32 s14, s31, 6144\n"
    "buffer_load_dwordx4 v1, s[16:19], s14 offen sc1 lds\n"
    "s_add_u32 m0, s30, 58368\n"
    "s_add_u32 s14, s31, 9216\n"
    "buffer_load_dwordx4 v1, s[16:19], s14 offen sc1 lds\n"
    "s_waitcnt vmcnt(4)\n"
    "s_branch Lt2_63_%=\n"
    "Lt1_62_%=:\n"
    "s_waitcnt vmcnt(0)\n"
    "Lt2_63_%=:\n"
    "s_barrier\n"
    "s_cmp_lg_u32 s26, 0\n"
    "s_cbranch_scc1 Lnotw0_64_%=\n"
    "s_lshl_b32 s36, s24, 10\n"
    "s_add_u32 s36, s36, s45\n"
    "s_add_u32 s36, s36, 32768\n"
    "v_mov_b32 v208, 0\n"
    "v_mov_b32 v209, 1\n"
    "s_mov_b64 exec, 1\n"
    "s_mov_b32 s50, 0\n"
    "Lgol_67_%=:\n"
    "buffer_atomic_add v209, v208, s[16:19], s36 offen sc0\n"
    "s_waitcnt vmcnt(0)\n"
    "v_readfirstlane_b32 s52, v209\n"
    "s_cmp_eq_u32 s50, 0\n"
    "s_cselect_b32 s51, 1, 0\n"
    "s_add_u32 s52, s52, s51\n"
    "v_mov_b32 v209, 0\n"
    "s_add_u32 s50, s50, 1\n"
    "s_cmp_ge_u32 s52, 16\n"
    "s_cbranch_scc1 Lgod_66_%=\n"
    "s_cmp_lt_u32 s50, 20000\n"
    "s_cbranch_scc1 Lgol_67_%=\n"
    "Lgod_66_%=:\n"
    "s_mov_b64 exec, -1\n"
    "s_branch Ltj_65_%=\n"
    "Lnotw0_64_%=:\n"
    "s_add_u32 m0, s30, 61440\n"
    "s_add_u32 s14, s31, 12288\n"
    "buffer_load_dwordx4 v1, s[16:19], s14 offen sc1 lds\n"
    "s_sub_u32 s35, s94, s93\n"
    "s_add_u32 s35, s35, 107520\n"
    "s_sub_u32 s30, s93, 1024\n"
    "s_add_u32 s31, s30, s35\n"
    "s_add_u32 m0, s30, 65536\n"
    "s_add_u32 s14, s31, 0\n"
    "buffer_load_dwordx4 v1, s[16:19], s14 offen sc1 lds\n"
    "s_add_u32 m0, s30, 68608\n"
    "s_add_u32 s14, s31, 3072\n"
    "buffer_load_dwordx4 v1, s[16:19], s14 offen sc1 lds\n"
    "s_add_u32 m0, s30, 71680\n"
    "s_add_u32 s14, s31, 6144\n"
    "buffer_load_dwordx4 v1, s[16:19], s14 offen sc1 lds\n"
    "s_add_u32 m0, s30, 74752\n"
    "s_add_u32 s14, s31, 9216\n"
    "buffer_load_dwordx4 v1, s[16:19], s14 offen sc1 lds\n"
    "s_add_u32 m0, s30, 77824\n"
    "s_add_u32 s14, s31, 12288\n"
    "buffer_load_dwordx4 v1, s[16:19], s14 offen sc1 lds\n"
    "s_sub_u32 s35, s94, s93\n"
    "s_add_u32 s35, s35, 122880\n"
    "s_sub_u32 s30, s93, 1024\n"
    "s_add_u32 s31, s30, s35\n"
    "s_add_u32 m0, s30, 81920\n"
    "s_add_u32 s14, s31, 0\n"
    "buffer_load_dwordx4 v1, s[16:19], s14 offen sc1 lds\n"
    "s_add_u32 m0, s30, 84992\n"
    "s_add_u32 s14, s31, 3072\n"
    "buffer_load_dwordx4 v1, s[16:19], s14 offen sc1 lds\n"
    "s_add_u32 m0, s30, 88064\n"
    "s_add_u32 s14, s31, 6144\n"
    "buffer_load_dwordx4 v1, s[16:19], s14 offen sc1 lds\n"
    "s_add_u32 m0, s30, 91136\n"
    "s_add_u32 s14, s31, 9216\n"
    "buffer_load_dwordx4 v1, s[16:19], s14 offen sc1 lds\n"
    "s_add_u32 m0, s30, 94208\n"
    "s_add_u32 s14, s31, 12288\n"
    "buffer_load_dwordx4 v1, s[16:19], s14 offen sc1 lds\n"
    "s_waitcnt vmcnt(0)\n"
    "Ltj_65_%=:\n"
    "s_barrier\n"
    "s_mul_i32 s38, s27, 110592\n"
    "s_add_u32 s38, s38, s43\n"
    "s_add_u32 s38, s38, 27414528\n"
    "v_add_u32 v100, 0, v1\n"
    "v_add_u32 v103, 0, v218\n"
    "v_add_u32 v101, 65536, v1\n"
    "v_add_u32 v104, 65536, v218\n"
    "v_add_u32 v102, 131072, v1\n"
    "v_add_u32 v105, 131072, v218\n"
    "s_add_u32 s91, s38, 0\n"
    "buffer_load_dwordx4 a[0:3], v0, s[16:19], s91 offen offset:0 sc1\n"
    "buffer_load_dwordx4 a[4:7], v0, s[16:19], s91 offen offset:256 sc1\n"
    "buffer_load_dwordx4 a[8:11], v0, s[16:19], s91 offen offset:2048 sc1\n"
    "buffer_load_dwordx4 a[12:15], v0, s[16:19], s91 offen offset:2304 sc1\n"
    "s_add_u32 s91, s38, 4096\n"
    "buffer_load_dwordx4 a[16:19], v0, s[16:19], s91 offen offset:0 sc1\n"
    "buffer_load_dwordx4 a[20:23], v0, s[16:19], s91 offen offset:256 sc1\n"
    "buffer_load_dwordx4 a[24:27], v0, s[16:19], s91 offen offset:2048 sc1\n"
    "buffer_load_dwordx4 a[28:31], v0, s[16:19], s91 offen offset:2304 sc1\n"
    "s_add_u32 s91, s38, 8192\n"
    "buffer_load_dwordx4 a[32:35], v0, s[16:19], s91 offen offset:0 sc1\n"
    "buffer_load_dwordx4 a[36:39], v0, s[16:19], s91 offen offset:256 sc1\n"
    "buffer_load_dwordx4 a[40:43], v0, s[16:19], s91 offen offset:2048 sc1\n"
    "buffer_load_dwordx4 a[44:47], v0, s[16:19], s91 offen offset:2304 sc1\n"
    "s_add_u32 s91, s38, 12288\n"
    "buffer_load_dwordx4 a[48:51], v0, s[16:19], s91 offen offset:0 sc1\n"
    "buffer_load_dwordx4 a[52:55], v0, s[16:19], s91 offen offset:256 sc1\n"
    "v_mov_b32 v48, v240\n"
    "v_mov_b32 v49, v241\n"
    "v_mov_b32 v50, v242\n"
    "v_mov_b32 v51, v243\n"
    "v_mov_b32 v52, v244\n"
    "v_mov_b32 v53, v245\n"
    "v_mov_b32 v54, v246\n"
    "v_mov_b32 v55, v247\n"
    "v_mov_b32 v56, v248\n"
    "v_mov_b32 v57, v249\n"
    "v_mov_b32 v58, v250\n"
    "v_mov_b32 v59, v251\n"
    "v_permlane16_swap_b32 v48, v50\n"
    "v_permlane16_swap_b32 v49, v51\n"
    "v_permlane16_swap_b32 v52, v54\n"
    "v_permlane16_swap_b32 v53, v55\n"
    "v_permlane16_swap_b32 v56, v58\n"
    "v_permlane16_swap_b32 v57, v59\n"
    "v_lshlrev_b32 v16, 16, v48\n"
    "v_and_b32 v17, 0xffff0000, v48\n"
    "v_lshlrev_b32 v18, 16, v49\n"
    "v_and_b32 v19, 0xffff0000, v49\n"
    "v_lshlrev_b32 v20, 16, v50\n"
    "v_and_b32 v21, 0xffff0000, v50\n"
    "v_lshlrev_b32 v22, 16, v51\n"
    "v_and_b32 v23, 0xffff0000, v51\n"
    "v_lshlrev_b32 v24, 16, v52\n"
    "v_and_b32 v25, 0xffff0000, v52\n"
    "v_lshlrev_b32 v26, 16, v53\n"
    "v_and_b32 v27, 0xffff0000, v53\n"
    "v_lshlrev_b32 v28, 16, v54\n"
    "v_and_b32 v29, 0xffff0000, v54\n"
    "v_lshlrev_b32 v30, 16, v55\n"
    "v_and_b32 v31, 0xffff0000, v55\n"
    "v_lshlrev_b32 v32, 16, v56\n"
    "v_and_b32 v33, 0xffff0000, v56\n"
    "v_lshlrev_b32 v34, 16, v57\n"
    "v_and_b32 v35, 0xffff0000, v57\n"
    "v_lshlrev_b32 v36, 16, v58\n"
    "v_and_b32 v37, 0xffff0000, v58\n"
    "v_lshlrev_b32 v38, 16, v59\n"
    "v_and_b32 v39, 0xffff0000, v59\n"
    "ds_read_b128 v[168:171], v100 offset:0\n"
    "ds_read_b128 v[172:175], v100 offset:1024\n"
    "ds_read_b128 v[176:179], v103 offset:0\n"
    "ds_read_b128 v[180:183], v100 offset:2560\n"
    "ds_read_b128 v[184:187], v100 offset:3584\n"
    "ds_read_b128 v[188:191], v103 offset:2560\n"
    "s_waitcnt vmcnt(12) lgkmcnt(3)\n"
    "v_mfma_f32_16x16x32_bf16 v[16:19], v[168:171], a[0:3], v[16:19]\n"
    "ds_read_b128 v[192:195], v100 offset:5120\n"
    "buffer_load_dwordx4 a[56:59], v0, s[16:19], s91 offen offset:2048 sc1\n"
    "v_mfma_f32_16x16x32_bf16 v[20:23], v[168:171], a[4:7], v[20:23]\n"
    "v_mfma_f32_16x16x32_bf16 v[24:27], v[172:175], a[0:3], v[24:27]\n"
    "ds_read_b128 v[196:199], v100 offset:6144\n"
    "buffer_load_dwordx4 a[60:63], v0, s[16:19], s91 offen offset:2304 sc1\n"
    "v_mfma_f32_16x16x32_bf16 v[28:31], v[172:175], a[4:7], v[28:31]\n"
    "v_mfma_f32_16x16x32_bf16 v[32:35], v[176:179], a[0:3], v[32:35]\n"
    "ds_read_b128 v[200:203], v103 offset:5120\n"
    "v_mfma_f32_16x16x32_bf16 v[36:39], v[176:179], a[4:7], v[36:39]\n"
    "s_waitcnt vmcnt(12) lgkmcnt(3)\n"
    "v_mfma_f32_16x16x32_bf16 v[16:19], v[180:183], a[8:11], v[16:19]\n"
    "ds_read_b128 v[204:207], v100 offset:7680\n"
    "s_add_u32 s91, s38, 16384\n"
    "buffer_load_dwordx4 a[64:67], v0, s[16:19], s91 offen offset:0 sc1\n"
    "v_mfma_f32_16x16x32_bf16 v[20:23], v[180:183], a[12:15], v[20:23]\n"
    "v_mfma_f32_16x16x32_bf16 v[24:27], v[184:187], a[8:11], v[24:27]\n"
    "ds_read_b128 v[168:171], v100 offset:8704\n"
    "buffer_load_dwordx4 a[68:71], v0, s[16:19], s91 offen offset:256 sc1\n"
    "v_mfma_f32_16x16x32_bf16 v[28:31], v[184:187], a[12:15], v[28:31]\n"
    "v_mfma_f32_16x16x32_bf16 v[32:35], v[188:191], a[8:11], v[32:35]\n"
    "ds_read_b128 v[172:175], v103 offset:7680\n"
    "v_mfma_f32_16x16x32_bf16 v[36:39], v[188:191], a[12:15], v[36:39]\n"
    "s_waitcnt vmcnt(12) lgkmcnt(3)\n"
    "v_mfma_f32_16x16x32_bf16 v[16:19], v[192:195], a[16:19], v[16:19]\n"
    "ds_read_b128 v[176:179], v100 offset:10240\n"
    "buffer_load_dwordx4 a[72:75], v0, s[16:19], s91 offen offset:2048 sc1\n"
    "v_mfma_f32_16x16x32_bf16 v[20:23], v[192:195], a[20:23], v[20:23]\n"
    "v_mfma_f32_16x16x32_bf16 v[24:27], v[196:199], a[16:19], v[24:27]\n"
    "ds_read_b128 v[180:183], v100 offset:11264\n"
    "buffer_load_dwordx4 a[76:79], v0, s[16:19], s91 offen offset:2304 sc1\n"
    "v_mfma_f32_16x16x32_bf16 v[28:31], v[196:199], a[20:23], v[28:31]\n"
    "v_mfma_f32_16x16x32_bf16 v[32:35], v[200:203], a[16:19], v[32:35]\n"
    "ds_read_b128 v[184:187], v103 offset:10240\n"
    "v_mfma_f32_16x16x32_bf16 v[36:39], v[200:203], a[20:23], v[36:39]\n"
    "s_waitcnt vmcnt(12) lgkmcnt(3)\n"
    "v_mfma_f32_16x16x32_bf16 v[16:19], v[204:207], a[24:27], v[16:19]\n"
    "ds_read_b128 v[188:191], v100 offset:12800\n"
    "s_add_u32 s91, s38, 20480\n"
    "buffer_load_dwordx4 a[80:83], v0, s[16:19], s91 offen offset:0 sc1\n"
    "v_mfma_f32_16x16x32_bf16 v[20:23], v[204:207], a[28:31], v[20:23]\n"
    "v_mfma_f32_16x16x32_bf16 v[24:27], v[168:171], a[24:27], v[24:27]\n"
    "ds_read_b128 v[192:195], v100 offset:13824\n"
    "buffer_load_dwordx4 a[84:87], v0, s[16:19], s91 offen offset:256 sc1\n"
    "v_mfma_f32_16x16x32_bf16 v[28:31], v[168:171], a[28:31], v[28:31]\n"
    "v_mfma_f32_16x16x32_bf16 v[32:35], v[172:175], a[24:27], v[32:35]\n"
    "ds_read_b128 v[196:199], v103 offset:12800\n"
    "v_mfma_f32_16x16x32_bf16 v[36:39], v[172:175], a[28:31], v[36:39]\n"
    "s_waitcnt vmcnt(12) lgkmcnt(3)\n"
    "v_mfma_f32_16x16x32_bf16 v[16:19], v[176:179], a[32:35], v[16:19]\n"
    "ds_read_b128 v[200:203], v100 offset:16384\n"
    "buffer_load_dwordx4 a[88:91], v0, s[16:19], s91 offen offset:2048 sc1\n"
    "v_mfma_f32_16x16x32_bf16 v[20:23], v[176:179], a[36:39], v[20:23]\n"
    "v_mfma_f32_16x16x32_bf16 v[24:27], v[180:183], a[32:35], v[24:27]\n"
    "ds_read_b128 v[204:207], v100 offset:17408\n"
    "buffer_load_dwordx4 a[92:95], v0, s[16:19], s91 offen offset:2304 sc1\n"
    "v_mfma_f32_16x16x32_bf16 v[28:31], v[180:183], a[36:39], v[28:31]\n"
    "v_mfma_f32_16x16x32_bf16 v[32:35], v[184:187], a[32:35], v[32:35]\n"
    "ds_read_b128 v[168:171], v103 offset:16384\n"
    "v_mfma_f32_16x16x32_bf16 v[36:39], v[184:187], a[36:39], v[36:39]\n"
    "s_waitcnt vmcnt(12) lgkmcnt(3)\n"
    "v_mfma_f32_16x16x32_bf16 v[16:19], v[188:191], a[40:43], v[16:19]\n"
    "ds_read_b128 v[172:175], v100 offset:18944\n"
    "s_add_u32 s91, s38, 24576\n"
    "buffer_load_dwordx4 a[96:99], v0, s[16:19], s91 offen offset:0 sc1\n"
    "v_mfma_f32_16x16x32_bf16 v[20:23], v[188:191], a[44:47], v[20:23]\n"
    "v_mfma_f32_16x16x32_bf16 v[24:27], v[192:195], a[40:43], v[24:27]\n"
    "ds_read_b128 v[176:179], v100 offset:19968\n"
    "buffer_load_dwordx4 a[100:103], v0, s[16:19], s91 offen offset:256 sc1\n"
    "v_mfma_f32_16x16x32_bf16 v[28:31], v[192:195], a[44:47], v[28:31]\n"
    "v_mfma_f32_16x16x32_bf16 v[32:35], v[196:199], a[40:43], v[32:35]\n"
    "ds_read_b128 v[180:183], v103 offset:18944\n"
    "v_mfma_f32_16x16x32_bf16 v[36:39], v[196:199], a[44:47], v[36:39]\n"
    "s_waitcnt vmcnt(12) lgkmcnt(3)\n"
    "v_mfma_f32_16x16x32_bf16 v[16:19], v[200:203], a[48:51], v[16:19]\n"
    "ds_read_b128 v[184:187], v100 offset:21504\n"
    "buffer_load_dwordx4 a[104:107], v0, s[16:19], s91 offen offset:2048 sc1\n"
    "v_mfma_f32_16x16x32_bf16 v[20:23], v[200:203], a[52:55], v[20:23]\n"
    "v_mfma_f32_16x16x32_bf16 v[24:27], v[204:207], a[48:51], v[24:27]\n"
    "ds_read_b128 v[188:191], v100 offset:22528\n"
    "buffer_load_dwordx4 a[108:111], v0, s[16:19], s91 offen offset:2304 sc1\n"
    "v_mfma_f32_16x16x32_bf16 v[28:31], v[204:207], a[52:55], v[28:31]\n"
    "v_mfma_f32_16x16x32_bf16 v[32:35], v[168:171], a[48:51], v[32:35]\n"
    "ds_read_b128 v[192:195], v103 offset:21504\n"
    "v_mfma_f32_16x16x32_bf16 v[36:39], v[168:171], a[52:55], v[36:39]\n"
    "s_waitcnt vmcnt(12) lgkmcnt(3)\n"
    "v_mfma_f32_16x16x32_bf16 v[16:19], v[172:175], a[56:59], v[16:19]\n"
    "ds_read_b128 v[196:199], v100 offset:24064\n"
    "s_add_u32 s91, s38, 28672\n"
    "buffer_load_dwordx4 a[112:115], v0, s[16:19], s91 offen offset:0 sc1\n"
    "v_mfma_f32_16x16x32_bf16 v[20:23], v[172:175], a[60:63], v[20:23]\n"
    "v_mfma_f32_16x16x32_bf16 v[24:27], v[176:179], a[56:59], v[24:27]\n"
    "ds_read_b128 v[200:203], v100 offset:25088\n"
    "buffer_load_dwordx4 a[116:119], v0, s[16:19], s91 offen offset:256 sc1\n"
    "v_mfma_f32_16x16x32_bf16 v[28:31], v[176:179], a[60:63], v[28:31]\n"
    "v_mfma_f32_16x16x32_bf16 v[32:35], v[180:183], a[56:59], v[32:35]\n"
    "ds_read_b128 v[204:207], v103 offset:24064\n"
    "v_mfma_f32_16x16x32_bf16 v[36:39], v[180:183], a[60:63], v[36:39]\n"
    "s_waitcnt vmcnt(12) lgkmcnt(3)\n"
    "v_mfma_f32_16x16x32_bf16 v[16:19], v[184:187], a[64:67], v[16:19]\n"
    "ds_read_b128 v[168:171], v100 offset:26624\n"
    "buffer_load_dwordx4 a[120:123], v0, s[16:19], s91 offen offset:2048 sc1\n"
    "v_mfma_f32_16x16x32_bf16 v[20:23], v[184:187], a[68:71], v[20:23]\n"
    "v_mfma_f32_16x16x32_bf16 v[24:27], v[188:191], a[64:67], v[24:27]\n"
    "ds_read_b128 v[172:175], v100 offset:27648\n"
    "buffer_load_dwordx4 a[124:127], v0, s[16:19], s91 offen offset:2304 sc1\n"
    "v_mfma_f32_16x16x32_bf16 v[28:31], v[188:191], a[68:71], v[28:31]\n"
    "v_mfma_f32_16x16x32_bf16 v[32:35], v[192:195], a[64:67], v[32:35]\n"
    "ds_read_b128 v[176:179], v103 offset:26624\n"
    "v_mfma_f32_16x16x32_bf16 v[36:39], v[192:195], a[68:71], v[36:39]\n"
    "s_waitcnt vmcnt(12) lgkmcnt(3)\n"
    "v_mfma_f32_16x16x32_bf16 v[16:19], v[196:199], a[72:75], v[16:19]\n"
    "ds_read_b128 v[180:183], v100 offset:29184\n"
    "s_add_u32 s91, s38, 32768\n"
    "buffer_load_dwordx4 a[128:131], v0, s[16:19], s91 offen offset:0 sc1\n"
    "v_mfma_f32_16x16x32_bf16 v[20:23], v[196:199], a[76:79], v[20:23]\n"
    "v_mfma_f32_16x16x32_bf16 v[24:27], v[200:203], a[72:75], v[24:27]\n"
    "ds_read_b128 v[184:187], v100 offset:30208\n"
    "buffer_load_dwordx4 a[132:135], v0, s[16:19], s91 offen offset:256 sc1\n"
    "v_mfma_f32_16x16x32_bf16 v[28:31], v[200:203], a[76:79], v[28:31]\n"
    "v_mfma_f32_16x16x32_bf16 v[32:35], v[204:207], a[72:75], v[32:35]\n"
    "ds_read_b128 v[188:191], v103 offset:29184\n"
    "v_mfma_f32_16x16x32_bf16 v[36:39], v[204:207], a[76:79], v[36:39]\n"
    "s_waitcnt vmcnt(12) lgkmcnt(3)\n"
    "v_mfma_f32_16x16x32_bf16 v[16:19], v[168:171], a[80:83], v[16:19]\n"
    "ds_read_b128 v[192:195], v101 offset:32768\n"
    "buffer_load_dwordx4 a[136:139], v0, s[16:19], s91 offen offset:2048 sc1\n"
    "v_mfma_f32_16x16x32_bf16 v[20:23], v[168:171], a[84:87], v[20:23]\n"
    "v_mfma_f32_16x16x32_bf16 v[24:27], v[172:175], a[80:83], v[24:27]\n"
    "ds_read_b128 v[196:199], v101 offset:33792\n"
    "buffer_load_dwordx4 a[140:143], v0, s[16:19], s91 offen offset:2304 sc1\n"
    "v_mfma_f32_16x16x32_bf16 v[28:31], v[172:175], a[84:87], v[28:31]\n"
    "v_mfma_f32_16x16x32_bf16 v[32:35], v[176:179], a[80:83], v[32:35]\n"
    "ds_read_b128 v[200:203], v104 offset:32768\n"
    "v_mfma_f32_16x16x32_bf16 v[36:39], v[176:179], a[84:87], v[36:39]\n"
    "s_waitcnt vmcnt(12) lgkmcnt(3)\n"
    "v_mfma_f32_16x16x32_bf16 v[16:19], v[180:183], a[88:91], v[16:19]\n"
    "ds_read_b128 v[204:207], v101 offset:35328\n"
    "s_add_u32 s91, s38, 36864\n"
    "buffer_load_dwordx4 a[144:147], v0, s[16:19], s91 offen offset:0 sc1\n"
    "v_mfma_f32_16x16x32_bf16 v[20:23], v[180:183], a[92:95], v[20:23]\n"
    "v_mfma_f32_16x16x32_bf16 v[24:27], v[184:187], a[88:91], v[24:27]\n"
    "ds_read_b128 v[168:171], v101 offset:36352\n"
    "buffer_load_dwordx4 a[148:151], v0, s[16:19], s91 offen offset:256 sc1\n"
    "v_mfma_f32_16x16x32_bf16 v[28:31], v[184:187], a[92:95], v[28:31]\n"
    "v_mfma_f32_16x16x32_bf16 v[32:35], v[188:191], a[88:91], v[32:35]\n"
    "ds_read_b128 v[172:175], v104 offset:35328\n"
    "v_mfma_f32_16x16x32_bf16 v[36:39], v[188:191], a[92:95], v[36:39]\n"
    "s_waitcnt vmcnt(12) lgkmcnt(3)\n"
    "v_mfma_f32_16x16x32_bf16 v[16:19], v[192:195], a[96:99], v[16:19]\n"
    "ds_read_b128 v[176:179], v101 offset:37888\n"
    "buffer_load_dwordx4 a[152:155], v0, s[16:19], s91 offen offset:2048 sc1\n"
    "v_mfma_f32_16x16x32_bf16 v[20:23], v[192:195], a[100:103], v[20:23]\n"
    "v_mfma_f32_16x16x32_bf16 v[24:27], v[196:199], a[96:99], v[24:27]\n"
    "ds_read_b128 v[180:183], v101 offset:38912\n"
    "buffer_load_dwordx4 a[156:159], v0, s[16:19], s91 offen offset:2304 sc1\n"
    "v_mfma_f32_16x16x32_bf16 v[28:31], v[196:199], a[100:103], v[28:31]\n"
    "v_mfma_f32_16x16x32_bf16 v[32:35], v[200:203], a[96:99], v[32:35]\n"
    "ds_read_b128 v[184:187], v104 offset:37888\n"
    "v_mfma_f32_16x16x32_bf16 v[36:39], v[200:203], a[100:103], v[36:39]\n"
    "s_waitcnt vmcnt(12) lgkmcnt(3)\n"
    "v_mfma_f32_16x16x32_bf16 v[16:19], v[204:207], a[104:107], v[16:19]\n"
    "ds_read_b128 v[188:191], v101 offset:40448\n"
    "s_add_u32 s91, s38, 40960\n"
    "buffer_load_dwordx4 a[160:163], v0, s[16:19], s91 offen offset:0 sc1\n"
    "v_mfma_f32_16x16x32_bf16 v[20:23], v[204:207], a[108:111], v[20:23]\n"
    "v_mfma_f32_16x16x32_bf16 v[24:27], v[168:171], a[104:107], v[24:27]\n"
    "ds_read_b128 v[192:195], v101 offset:41472\n"
    "buffer_load_dwordx4 a[164:167], v0, s[16:19], s91 offen offset:256 sc1\n"
    "v_mfma_f32_16x16x32_bf16 v[28:31], v[168:171], a[108:111], v[28:31]\n"
    "v_mfma_f32_16x16x32_bf16 v[32:35], v[172:175], a[104:107], v[32:35]\n"
    "ds_read_b128 v[196:199], v104 offset:40448\n"
    "v_mfma_f32_16x16x32_bf16 v[36:39], v[172:175], a[108:111], v[36:39]\n"
    "s_waitcnt vmcnt(12) lgkmcnt(3)\n"
    "v_mfma_f32_16x16x32_bf16 v[16:19], v[176:179], a[112:115], v[16:19]\n"
    "ds_read_b128 v[200:203], v101 offset:43008\n"
    "buffer_load_dwordx4 a[168:171], v0, s[16:19], s91 offen offset:2048 sc1\n"
    "v_mfma_f32_16x16x32_bf16 v[20:23], v[176:179], a[116:119], v[20:23]\n"
    "v_mfma_f32_16x16x32_bf16 v[24:27], v[180:183], a[112:115], v[24:27]\n"
    "ds_read_b128 v[204:207], v101 offset:44032\n"
    "buffer_load_dwordx4 a[172:175], v0, s[16:19], s91 offen offset:2304 sc1\n"
    "v_mfma_f32_16x16x32_bf16 v[28:31], v[180:183], a[116:119], v[28:31]\n"
    "v_mfma_f32_16x16x32_bf16 v[32:35], v[184:187], a[112:115], v[32:35]\n"
    "ds_read_b128 v[168:171], v104 offset:43008\n"
    "v_mfma_f32_16x16x32_bf16 v[36:39], v[184:187], a[116:119], v[36:39]\n"
    "s_waitcnt vmcnt(12) lgkmcnt(3)\n"
    "v_mfma_f32_16x16x32_bf16 v[16:19], v[188:191], a[120:123], v[16:19]\n"
    "ds_read_b128 v[172:175], v101 offset:45568\n"
    "s_add_u32 s91, s38, 45056\n"
    "buffer_load_dwordx4 a[176:179], v0, s[16:19], s91 offen offset:0 sc1\n"
    "v_mfma_f32_16x16x32_bf16 v[20:23], v[188:191], a[124:127], v[20:23]\n"
    "v_mfma_f32_16x16x32_bf16 v[24:27], v[192:195], a[120:123], v[24:27]\n"
    "ds_read_b128 v[176:179], v101 offset:46592\n"
    "buffer_load_dwordx4 a[180:183], v0, s[16:19], s91 offen offset:256 sc1\n"
    "v_mfma_f32_16x16x32_bf16 v[28:31], v[192:195], a[124:127], v[28:31]\n"
    "v_mfma_f32_16x16x32_bf16 v[32:35], v[196:199], a[120:123], v[32:35]\n"
    "ds_read_b128 v[180:183], v104 offset:45568\n"
    "v_mfma_f32_16x16x32_bf16 v[36:39], v[196:199], a[124:127], v[36:39]\n"
    "s_waitcnt vmcnt(12) lgkmcnt(3)\n"
    "v_mfma_f32_16x16x32_bf16 v[16:19], v[200:203], a[128:131], v[16:19]\n"
    "ds_read_b128 v[184:187], v101 offset:49152\n"
    "buffer_load_dwordx4 a[184:187], v0, s[16:19], s91 offen offset:2048 sc1\n"
    "v_mfma_f32_16x16x32_bf16 v[20:23], v[200:203], a[132:135], v[20:23]\n"
    "v_mfma_f32_16x16x32_bf16 v[24:27], v[204:207], a[128:131], v[24:27]\n"
    "ds_read_b128 v[188:191], v101 offset:50176\n"
    "buffer_load_dwordx4 a[188:191], v0, s[16:19], s91 offen offset:2304 sc1\n"
    "v_mfma_f32_16x16x32_bf16 v[28:31], v[204:207], a[132:135], v[28:31]\n"
    "v_mfma_f32_16x16x32_bf16 v[32:35], v[168:171], a[128:131], v[32:35]\n"
    "ds_read_b128 v[192:195], v104 offset:49152\n"
    "v_mfma_f32_16x16x32_bf16 v[36:39], v[168:171], a[132:135], v[36:39]\n"
    "s_waitcnt vmcnt(12) lgkmcnt(3)\n"
    "v_mfma_f32_16x16x32_bf16 v[16:19], v[172:175], a[136:139], v[16:19]\n"
    "ds_read_b128 v[196:199], v101 offset:51712\n"
    "s_add_u32 s91, s38, 49152\n"
    "buffer_load_dwordx4 a[192:195], v0, s[16:19], s91 offen offset:0 sc1\n"
    "v_mfma_f32_16x16x32_bf16 v[20:23], v[172:175], a[140:143], v[20:23]\n"
    "v_mfma_f32_16x16x32_bf16 v[24:27], v[176:179], a[136:139], v[24:27]\n"
    "ds_read_b128 v[200:203], v101 offset:52736\n"
    "buffer_load_dwordx4 a[196:199], v0, s[16:19], s91 offen offset:256 sc1\n"
    "v_mfma_f32_16x16x32_bf16 v[28:31], v[176:179], a[140:143], v[28:31]\n"
    "v_mfma_f32_16x16x32_bf16 v[32:35], v[180:183], a[136:139], v[32:35]\n"
    "ds_read_b128 v[204:207], v104 offset:51712\n"
    "v_mfma_f32_16x16x32_bf16 v[36:39], v[180:183], a[140:143], v[36:39]\n"
    "s_waitcnt vmcnt(12) lgkmcnt(3)\n"
    "v_mfma_f32_16x16x32_bf16 v[16:19], v[184:187], a[144:147], v[16:19]\n"
    "ds_read_b128 v[168:171], v101 offset:54272\n"
    "buffer_load_dwordx4 a[200:203], v0, s[16:19], s91 offen offset:2048 sc1\n"
    "v_mfma_f32_16x16x32_bf16 v[20:23], v[184:187], a[148:151], v[20:23]\n"
    "v_mfma_f32_16x16x32_bf16 v[24:27], v[188:191], a[144:147], v[24:27]\n"
    "ds_read_b128 v[172:175], v101 offset:55296\n"
    "buffer_load_dwordx4 a[204:207], v0, s[16:19], s91 offen offset:2304 sc1\n"
    "v_mfma_f32_16x16x32_bf16 v[28:31], v[188:191], a[148:151], v[28:31]\n"
    "v_mfma_f32_16x16x32_bf16 v[32:35], v[192:195], a[144:147], v[32:35]\n"
    "ds_read_b128 v[176:179], v104 offset:54272\n"
    "v_mfma_f32_16x16x32_bf16 v[36:39], v[192:195], a[148:151], v[36:39]\n"
    "s_waitcnt vmcnt(12) lgkmcnt(3)\n"
    "v_mfma_f32_16x16x32_bf16 v[16:19], v[196:199], a[152:155], v[16:19]\n"
    "ds_read_b128 v[180:183], v101 offset:56832\n"
    "s_add_u32 s91, s38, 53248\n"
    "buffer_load_dwordx4 a[208:211], v0, s[16:19], s91 offen offset:0 sc1\n"
    "v_mfma_f32_16x16x32_bf16 v[20:23], v[196:199], a[156:159], v[20:23]\n"
    "v_mfma_f32_16x16x32_bf16 v[24:27], v[200:203], a[152:155], v[24:27]\n"
    "ds_read_b128 v[184:187], v101 offset:57856\n"
    "buffer_load_dwordx4 a[212:215], v0, s[16:19], s91 offen offset:256 sc1\n"
    "v_mfma_f32_16x16x32_bf16 v[28:31], v[200:203], a[156:159], v[28:31]\n"
    "v_mfma_f32_16x16x32_bf16 v[32:35], v[204:207], a[152:155], v[32:35]\n"
    "ds_read_b128 v[188:191], v104 offset:56832\n"
    "v_mfma_f32_16x16x32_bf16 v[36:39], v[204:207], a[156:159], v[36:39]\n"
    "s_waitcnt vmcnt(12) lgkmcnt(3)\n"
    "v_mfma_f32_16x16x32_bf16 v[16:19], v[168:171], a[160:163], v[16:19]\n"
    "ds_read_b128 v[192:195], v101 offset:59392\n"
    "buffer_load_dwordx4 a[216:219], v0, s[16:19], s91 offen offset:2048 sc1\n"
    "v_mfma_f32_16x16x32_bf16 v[20:23], v[168:171], a[164:167], v[20:23]\n"
    "v_mfma_f32_16x16x32_bf16 v[24:27], v[172:175], a[160:163], v[24:27]\n"
    "ds_read_b128 v[196:199], v101 offset:60416\n"
    "buffer_load_dwordx4 a[220:223], v0, s[16:19], s91 offen offset:2304 sc1\n"
    "v_mfma_f32_16x16x32_bf16 v[28:31], v[172:175], a[164:167], v[28:31]\n"
    "v_mfma_f32_16x16x32_bf16 v[32:35], v[176:179], a[160:163], v[32:35]\n"
    "ds_read_b128 v[200:203], v104 offset:59392\n"
    "v_mfma_f32_16x16x32_bf16 v[36:39], v[176:179], a[164:167], v[36:39]\n"
    "s_waitcnt vmcnt(12) lgkmcnt(3)\n"
    "v_mfma_f32_16x16x32_bf16 v[16:19], v[180:183], a[168:171], v[16:19]\n"
    "ds_read_b128 v[204:207], v101 offset:61952\n"
    "s_add_u32 s91, s38, 57344\n"
    "buffer_load_dwordx4 a[224:227], v0, s[16:19], s91 offen offset:0 sc1\n"
    "v_mfma_f32_16x16x32_bf16 v[20:23], v[180:183], a[172:175], v[20:23]\n"
    "v_mfma_f32_16x16x32_bf16 v[24:27], v[184:187], a[168:171], v[24:27]\n"
    "ds_read_b128 v[168:171], v101 offset:62976\n"
    "buffer_load_dwordx4 a[228:231], v0, s[16:19], s91 offen offset:256 sc1\n"
    "v_mfma_f32_16x16x32_bf16 v[28:31], v[184:187], a[172:175], v[28:31]\n"
    "v_mfma_f32_16x16x32_bf16 v[32:35], v[188:191], a[168:171], v[32:35]\n"
    "ds_read_b128 v[172:175], v104 offset:61952\n"
    "v_mfma_f32_16x16x32_bf16 v[36:39], v[188:191], a[172:175], v[36:39]\n"
    "s_waitcnt vmcnt(12) lgkmcnt(3)\n"
    "v_mfma_f32_16x16x32_bf16 v[16:19], v[192:195], a[176:179], v[16:19]\n"
    "ds_read_b128 v[176:179], v102 offset:0\n"
    "buffer_load_dwordx4 a[232:235], v0, s[16:19], s91 offen offset:2048 sc1\n"
    "v_mfma_f32_16x16x32_bf16 v[20:23], v[192:195], a[180:183], v[20:23]\n"
    "v_mfma_f32_16x16x32_bf16 v[24:27], v[196:199], a[176:179], v[24:27]\n"
    "ds_read_b128 v[180:183], v102 offset:1024\n"
    "buffer_load_dwordx4 a[236:239], v0, s[16:19], s91 offen offset:2304 sc1\n"
    "v_mfma_f32_16x16x32_bf16 v[28:31], v[196:199], a[180:183], v[28:31]\n"
    "v_mfma_f32_16x16x32_bf16 v[32:35], v[200:203], a[176:179], v[32:35]\n"
    "ds_read_b128 v[184:187], v105 offset:0\n"
    "v_mfma_f32_16x16x32_bf16 v[36:39], v[200:203], a[180:183], v[36:39]\n"
    "s_waitcnt vmcnt(12) lgkmcnt(3)\n"
    "v_mfma_f32_16x16x32_bf16 v[16:19], v[204:207], a[184:187], v[16:19]\n"
    "ds_read_b128 v[188:191], v102 offset:2560\n"
    "s_add_u32 s91, s38, 61440\n"
    "buffer_load_dwordx4 a[0:3], v0, s[16:19], s91 offen offset:0 sc1\n"
    "v_mfma_f32_16x16x32_bf16 v[20:23], v[204:207], a[188:191], v[20:23]\n"
    "v_mfma_f32_16x16x32_bf16 v[24:27], v[168:171], a[184:187], v[24:27]\n"
    "ds_read_b128 v[192:195], v102 offset:3584\n"
    "buffer_load_dwordx4 a[4:7], v0, s[16:19], s91 offen offset:256 sc1\n"
    "v_mfma_f32_16x16x32_bf16 v[28:31], v[168:171], a[188:191], v[28:31]\n"
    "v_mfma_f32_16x16x32_bf16 v[32:35], v[172:175], a[184:187], v[32:35]\n"
    "ds_read_b128 v[196:199], v105 offset:2560\n"
    "v_mfma_f32_16x16x32_bf16 v[36:39], v[172:175], a[188:191], v[36:39]\n"
    "s_waitcnt vmcnt(12) lgkmcnt(3)\n"
    "v_mfma_f32_16x16x32_bf16 v[16:19], v[176:179], a[192:195], v[16:19]\n"
    "ds_read_b128 v[200:203], v102 offset:5120\n"
    "buffer_load_dwordx4 a[8:11], v0, s[16:19], s91 offen offset:2048 sc1\n"
    "v_mfma_f32_16x16x32_bf16 v[20:23], v[176:179], a[196:199], v[20:23]\n"
    "v_mfma_f32_16x16x32_bf16 v[24:27], v[180:183], a[192:195], v[24:27]\n"
    "ds_read_b128 v[204:207], v102 offset:6144\n"
    "buffer_load_dwordx4 a[12:15], v0, s[16:19], s91 offen offset:2304 sc1\n"
    "v_mfma_f32_16x16x32_bf16 v[28:31], v[180:183], a[196:199], v[28:31]\n"
    "v_mfma_f32_16x16x32_bf16 v[32:35], v[184:187], a[192:195], v[32:35]\n"
    "ds_read_b128 v[168:171], v105 offset:5120\n"
    "v_mfma_f32_16x16x32_bf16 v[36:39], v[184:187], a[196:199], v[36:39]\n"
    "s_waitcnt vmcnt(12) lgkmcnt(3)\n"
    "v_mfma_f32_16x16x32_bf16 v[16:19], v[188:191], a[200:203], v[16:19]\n"
    "ds_read_b128 v[172:175], v102 offset:7680\n"
    "s_add_u32 s91, s38, 65536\n"
    "buffer_load_dwordx4 a[16:19], v0, s[16:19], s91 offen offset:0 sc1\n"
    "v_mfma_f32_16x16x32_bf16 v[20:23], v[188:191], a[204:207], v[20:23]\n"
    "v_mfma_f32_16x16x32_bf16 v[24:27], v[192:195], a[200:203], v[24:27]\n"
    "ds_read_b128 v[176:179], v102 offset:8704\n"
    "buffer_load_dwordx4 a[20:23], v0, s[16:19], s91 offen offset:256 sc1\n"
    "v_mfma_f32_16x16x32_bf16 v[28:31], v[192:195], a[204:207], v[28:31]\n"
    "v_mfma_f32_16x16x32_bf16 v[32:35], v[196:199], a[200:203], v[32:35]\n"
    "ds_read_b128 v[180:183], v105 offset:7680\n"
    "v_mfma_f32_16x16x32_bf16 v[36:39], v[196:199], a[204:207], v[36:39]\n"
    "s_waitcnt vmcnt(12) lgkmcnt(3)\n"
    "v_mfma_f32_16x16x32_bf16 v[16:19], v[200:203], a[208:211], v[16:19]\n"
    "ds_read_b128 v[184:187], v102 offset:10240\n"
    "buffer_load_dwordx4 a[24:27], v0, s[16:19], s91 offen offset:2048 sc1\n"
    "v_mfma_f32_16x16x32_bf16 v[20:23], v[200:203], a[212:215], v[20:23]\n"
    "v_mfma_f32_16x16x32_bf16 v[24:27], v[204:207], a[208:211], v[24:27]\n"
    "ds_read_b128 v[188:191], v102 offset:11264\n"
    "buffer_load_dwordx4 a[28:31], v0, s[16:19], s91 offen offset:2304 sc1\n"
    "v_mfma_f32_16x16x32_bf16 v[28:31], v[204:207], a[212:215], v[28:31]\n"
    "v_mfma_f32_16x16x32_bf16 v[32:35], v[168:171], a[208:211], v[32:35]\n"
    "ds_read_b128 v[192:195], v105 offset:10240\n"
    "v_mfma_f32_16x16x32_bf16 v[36:39], v[168:171], a[212:215], v[36:39]\n"
    "s_waitcnt vmcnt(12) lgkmcnt(3)\n"
    "v_mfma_f32_16x16x32_bf16 v[16:19], v[172:175], a[216:219], v[16:19]\n"
    "ds_read_b128 v[196:199], v102 offset:12800\n"
    "s_add_u32 s91, s38, 69632\n"
    "buffer_load_dwordx4 a[32:35], v0, s[16:19], s91 offen offset:0 sc1\n"
    "v_mfma_f32_16x16x32_bf16 v[20:23], v[172:175], a[220:223], v[20:23]\n"
    "v_mfma_f32_16x16x32_bf16 v[24:27], v[176:179], a[216:219], v[24:27]\n"
    "ds_read_b128 v[200:203], v102 offset:13824\n"
    "buffer_load_dwordx4 a[36:39], v0, s[16:19], s91 offen offset:256 sc1\n"
    "v_mfma_f32_16x16x32_bf16 v[28:31], v[176:179], a[220:223], v[28:31]\n"
    "v_mfma_f32_16x16x32_bf16 v[32:35], v[180:183], a[216:219], v[32:35]\n"
    "ds_read_b128 v[204:207], v105 offset:12800\n"
    "v_mfma_f32_16x16x32_bf16 v[36:39], v[180:183], a[220:223], v[36:39]\n"
    "s_waitcnt vmcnt(12) lgkmcnt(3)\n"
    "v_mfma_f32_16x16x32_bf16 v[16:19], v[184:187], a[224:227], v[16:19]\n"
    "ds_read_b128 v[168:171], v100 offset:32768\n"
    "buffer_load_dwordx4 a[40:43], v0, s[16:19], s91 offen offset:2048 sc1\n"
    "v_mfma_f32_16x16x32_bf16 v[20:23], v[184:187], a[228:231], v[20:23]\n"
    "v_mfma_f32_16x16x32_bf16 v[24:27], v[188:191], a[224:227], v[24:27]\n"
    "ds_read_b128 v[172:175], v100 offset:33792\n"
    "buffer_load_dwordx4 a[44:47], v0, s[16:19], s91 offen offset:2304 sc1\n"
    "v_mfma_f32_16x16x32_bf16 v[28:31], v[188:191], a[228:231], v[28:31]\n"
    "v_mfma_f32_16x16x32_bf16 v[32:35], v[192:195], a[224:227], v[32:35]\n"
    "ds_read_b128 v[176:179], v103 offset:32768\n"
    "v_mfma_f32_16x16x32_bf16 v[36:39], v[192:195], a[228:231], v[36:39]\n"
    "s_waitcnt vmcnt(12) lgkmcnt(3)\n"
    "v_mfma_f32_16x16x32_bf16 v[16:19], v[196:199], a[232:235], v[16:19]\n"
    "ds_read_b128 v[180:183], v100 offset:35328\n"
    "s_add_u32 s91, s38, 73728\n"
    "buffer_load_dwordx4 a[48:51], v0, s[16:19], s91 offen offset:0 sc1\n"
    "v_mfma_f32_16x16x32_bf16 v[20:23], v[196:199], a[236:239], v[20:23]\n"
    "v_mfma_f32_16x16x32_bf16 v[24:27], v[200:203], a[232:235], v[24:27]\n"
    "ds_read_b128 v[184:187], v100 offset:36352\n"
    "buffer_load_dwordx4 a[52:55], v0, s[16:19], s91 offen offset:256 sc1\n"
    "v_mfma_f32_16x16x32_bf16 v[28:31], v[200:203], a[236:239], v[28:31]\n"
    "v_mfma_f32_16x16x32_bf16 v[32:35], v[204:207], a[232:235], v[32:35]\n"
    "ds_read_b128 v[188:191], v103 offset:35328\n"
    "v_mfma_f32_16x16x32_bf16 v[36:39], v[204:207], a[236:239], v[36:39]\n"
    "s_waitcnt vmcnt(12) lgkmcnt(3)\n"
    "v_mfma_f32_16x16x32_bf16 v[16:19], v[168:171], a[0:3], v[16:19]\n"
    "ds_read_b128 v[192:195], v100 offset:37888\n"
    "buffer_load_dwordx4 a[56:59], v0, s[16:19], s91 offen offset:2048 sc1\n"
    "v_mfma_f32_16x16x32_bf16 v[20:23], v[168:171], a[4:7], v[20:23]\n"
    "v_mfma_f32_16x16x32_bf16 v[24:27], v[172:175], a[0:3], v[24:27]\n"
    "ds_read_b128 v[196:199], v100 offset:38912\n"
    "buffer_load_dwordx4 a[60:63], v0, s[16:19], s91 offen offset:2304 sc1\n"
    "v_mfma_f32_16x16x32_bf16 v[28:31], v[172:175], a[4:7], v[28:31]\n"
    "v_mfma_f32_16x16x32_bf16 v[32:35], v[176:179], a[0:3], v[32:35]\n"
    "ds_read_b128 v[200:203], v103 offset:37888\n"
    "v_mfma_f32_16x16x32_bf16 v[36:39], v[176:179], a[4:7], v[36:39]\n"
    "s_waitcnt vmcnt(12) lgkmcnt(3)\n"
    "v_mfma_f32_16x16x32_bf16 v[16:19], v[180:183], a[8:11], v[16:19]\n"
    "ds_read_b128 v[204:207], v100 offset:40448\n"
    "s_add_u32 s91, s38, 77824\n"
    "buffer_load_dwordx4 a[64:67], v0, s[16:19], s91 offen offset:0 sc1\n"
    "v_mfma_f32_16x16x32_bf16 v[20:23], v[180:183], a[12:15], v[20:23]\n"
    "v_mfma_f32_16x16x32_bf16 v[24:27], v[184:187], a[8:11], v[24:27]\n"
    "ds_read_b128 v[168:171], v100 offset:41472\n"
    "buffer_load_dwordx4 a[68:71], v0, s[16:19], s91 offen offset:256 sc1\n"
    "v_mfma_f32_16x16x32_bf16 v[28:31], v[184:187], a[12:15], v[28:31]\n"
    "v_mfma_f32_16x16x32_bf16 v[32:35], v[188:191], a[8:11], v[32:35]\n"
    "ds_read_b128 v[172:175], v103 offset:40448\n"
    "v_mfma_f32_16x16x32_bf16 v[36:39], v[188:191], a[12:15], v[36:39]\n"
    "s_waitcnt vmcnt(12) lgkmcnt(3)\n"
    "v_mfma_f32_16x16x32_bf16 v[16:19], v[192:195], a[16:19], v[16:19]\n"
    "ds_read_b128 v[176:179], v100 offset:43008\n"
    "buffer_load_dwordx4 a[72:75], v0, s[16:19], s91 offen offset:2048 sc1\n"
    "v_mfma_f32_16x16x32_bf16 v[20:23], v[192:195], a[20:23], v[20:23]\n"
    "v_mfma_f32_16x16x32_bf16 v[24:27], v[196:199], a[16:19], v[24:27]\n"
    "ds_read_b128 v[180:183], v100 offset:44032\n"
    "buffer_load_dwordx4 a[76:79], v0, s[16:19], s91 offen offset:2304 sc1\n"
    "v_mfma_f32_16x16x32_bf16 v[28:31], v[196:199], a[20:23], v[28:31]\n"
    "v_mfma_f32_16x16x32_bf16 v[32:35], v[200:203], a[16:19], v[32:35]\n"
    "ds_read_b128 v[184:187], v103 offset:43008\n"
    "v_mfma_f32_16x16x32_bf16 v[36:39], v[200:203], a[20:23], v[36:39]\n"
    "s_waitcnt vmcnt(12) lgkmcnt(3)\n"
    "v_mfma_f32_16x16x32_bf16 v[16:19], v[204:207], a[24:27], v[16:19]\n"
    "ds_read_b128 v[188:191], v100 offset:45568\n"
    "s_add_u32 s91, s38, 81920\n"
    "buffer_load_dwordx4 a[80:83], v0, s[16:19], s91 offen offset:0 sc1\n"
    "v_mfma_f32_16x16x32_bf16 v[20:23], v[204:207], a[28:31], v[20:23]\n"
    "v_mfma_f32_16x16x32_bf16 v[24:27], v[168:171], a[24:27], v[24:27]\n"
    "ds_read_b128 v[192:195], v100 offset:46592\n"
    "buffer_load_dwordx4 a[84:87], v0, s[16:19], s91 offen offset:256 sc1\n"
    "v_mfma_f32_16x16x32_bf16 v[28:31], v[168:171], a[28:31], v[28:31]\n"
    "v_mfma_f32_16x16x32_bf16 v[32:35], v[172:175], a[24:27], v[32:35]\n"
    "ds_read_b128 v[196:199], v103 offset:45568\n"
    "v_mfma_f32_16x16x32_bf16 v[36:39], v[172:175], a[28:31], v[36:39]\n"
    "s_waitcnt vmcnt(12) lgkmcnt(3)\n"
    "v_mfma_f32_16x16x32_bf16 v[16:19], v[176:179], a[32:35], v[16:19]\n"
    "ds_read_b128 v[200:203], v100 offset:49152\n"
    "buffer_load_dwordx4 a[88:91], v0, s[16:19], s91 offen offset:2048 sc1\n"
    "v_mfma_f32_16x16x32_bf16 v[20:23], v[176:179], a[36:39], v[20:23]\n"
    "v_mfma_f32_16x16x32_bf16 v[24:27], v[180:183], a[32:35], v[24:27]\n"
    "ds_read_b128 v[204:207], v100 offset:50176\n"
    "buffer_load_dwordx4 a[92:95], v0, s[16:19], s91 offen offset:2304 sc1\n"
    "v_mfma_f32_16x16x32_bf16 v[28:31], v[180:183], a[36:39], v[28:31]\n"
    "v_mfma_f32_16x16x32_bf16 v[32:35], v[184:187], a[32:35], v[32:35]\n"
    "ds_read_b128 v[168:171], v103 offset:49152\n"
    "v_mfma_f32_16x16x32_bf16 v[36:39], v[184:187], a[36:39], v[36:39]\n"
    "s_waitcnt vmcnt(12) lgkmcnt(3)\n"
    "v_mfma_f32_16x16x32_bf16 v[16:19], v[188:191], a[40:43], v[16:19]\n"
    "ds_read_b128 v[172:175], v100 offset:51712\n"
    "s_add_u32 s91, s38, 86016\n"
    "buffer_load_dwordx4 a[96:99], v0, s[16:19], s91 offen offset:0 sc1\n"
    "v_mfma_f32_16x16x32_bf16 v[20:23], v[188:191], a[44:47], v[20:23]\n"
    "v_mfma_f32_16x16x32_bf16 v[24:27], v[192:195], a[40:43], v[24:27]\n"
    "ds_read_b128 v[176:179], v100 offset:52736\n"
    "buffer_load_dwordx4 a[100:103], v0, s[16:19], s91 offen offset:256 sc1\n"
    "v_mfma_f32_16x16x32_bf16 v[28:31], v[192:195], a[44:47], v[28:31]\n"
    "v_mfma_f32_16x16x32_bf16 v[32:35], v[196:199], a[40:43], v[32:35]\n"
    "ds_read_b128 v[180:183], v103 offset:51712\n"
    "v_mfma_f32_16x16x32_bf16 v[36:39], v[196:199], a[44:47], v[36:39]\n"
    "s_waitcnt vmcnt(12) lgkmcnt(3)\n"
    "v_mfma_f32_16x16x32_bf16 v[16:19], v[200:203], a[48:51], v[16:19]\n"
    "ds_read_b128 v[184:187], v100 offset:54272\n"
    "buffer_load_dwordx4 a[104:107], v0, s[16:19], s91 offen offset:2048 sc1\n"
    "v_mfma_f32_16x16x32_bf16 v[20:23], v[200:203], a[52:55], v[20:23]\n"
    "v_mfma_f32_16x16x32_bf16 v[24:27], v[204:207], a[48:51], v[24:27]\n"
    "ds_read_b128 v[188:191], v100 offset:55296\n"
    "buffer_load_dwordx4 a[108:111], v0, s[16:19], s91 offen offset:2304 sc1\n"
    "v_mfma_f32_16x16x32_bf16 v[28:31], v[204:207], a[52:55], v[28:31]\n"
    "v_mfma_f32_16x16x32_bf16 v[32:35], v[168:171], a[48:51], v[32:35]\n"
    "ds_read_b128 v[192:195], v103 offset:54272\n"
    "v_mfma_f32_16x16x32_bf16 v[36:39], v[168:171], a[52:55], v[36:39]\n"
    "s_waitcnt vmcnt(12) lgkmcnt(3)\n"
    "v_mfma_f32_16x16x32_bf16 v[16:19], v[172:175], a[56:59], v[16:19]\n"
    "ds_read_b128 v[196:199], v100 offset:56832\n"
    "s_add_u32 s91, s38, 90112\n"
    "buffer_load_dwordx4 a[112:115], v0, s[16:19], s91 offen offset:0 sc1\n"
    "v_mfma_f32_16x16x32_bf16 v[20:23], v[172:175], a[60:63], v[20:23]\n"
    "v_mfma_f32_16x16x32_bf16 v[24:27], v[176:179], a[56:59], v[24:27]\n"
    "ds_read_b128 v[200:203], v100 offset:57856\n"
    "buffer_load_dwordx4 a[116:119], v0, s[16:19], s91 offen offset:256 sc1\n"
    "v_mfma_f32_16x16x32_bf16 v[28:31], v[176:179], a[60:63], v[28:31]\n"
    "v_mfma_f32_16x16x32_bf16 v[32:35], v[180:183], a[56:59], v[32:35]\n"
    "ds_read_b128 v[204:207], v103 offset:56832\n"
    "v_mfma_f32_16x16x32_bf16 v[36:39], v[180:183], a[60:63], v[36:39]\n"
    "s_waitcnt vmcnt(12) lgkmcnt(3)\n"
    "v_mfma_f32_16x16x32_bf16 v[16:19], v[184:187], a[64:67], v[16:19]\n"
    "ds_read_b128 v[168:171], v100 offset:59392\n"
    "buffer_load_dwordx4 a[120:123], v0, s[16:19], s91 offen offset:2048 sc1\n"
    "v_mfma_f32_16x16x32_bf16 v[20:23], v[184:187], a[68:71], v[20:23]\n"
    "v_mfma_f32_16x16x32_bf16 v[24:27], v[188:191], a[64:67], v[24:27]\n"
    "ds_read_b128 v[172:175], v100 offset:60416\n"
    "buffer_load_dwordx4 a[124:127], v0, s[16:19], s91 offen offset:2304 sc1\n"
    "v_mfma_f32_16x16x32_bf16 v[28:31], v[188:191], a[68:71], v[28:31]\n"
    "v_mfma_f32_16x16x32_bf16 v[32:35], v[192:195], a[64:67], v[32:35]\n"
    "ds_read_b128 v[176:179], v103 offset:59392\n"
    "v_mfma_f32_16x16x32_bf16 v[36:39], v[192:195], a[68:71], v[36:39]\n"
    "s_waitcnt vmcnt(12) lgkmcnt(3)\n"
    "v_mfma_f32_16x16x32_bf16 v[16:19], v[196:199], a[72:75], v[16:19]\n"
    "ds_read_b128 v[180:183], v100 offset:61952\n"
    "s_add_u32 s91, s38, 94208\n"
    "buffer_load_dwordx4 a[128:131], v0, s[16:19], s91 offen offset:0 sc1\n"
    "v_mfma_f32_16x16x32_bf16 v[20:23], v[196:199], a[76:79], v[20:23]\n"
    "v_mfma_f32_16x16x32_bf16 v[24:27], v[200:203], a[72:75], v[24:27]\n"
    "ds_read_b128 v[184:187], v100 offset:62976\n"
    "buffer_load_dwordx4 a[132:135], v0, s[16:19], s91 offen offset:256 sc1\n"
    "v_mfma_f32_16x16x32_bf16 v[28:31], v[200:203], a[76:79], v[28:31]\n"
    "v_mfma_f32_16x16x32_bf16 v[32:35], v[204:207], a[72:75], v[32:35]\n"
    "ds_read_b128 v[188:191], v103 offset:61952\n"
    "v_mfma_f32_16x16x32_bf16 v[36:39], v[204:207], a[76:79], v[36:39]\n"
    "s_waitcnt vmcnt(12) lgkmcnt(3)\n"
    "v_mfma_f32_16x16x32_bf16 v[16:19], v[168:171], a[80:83], v[16:19]\n"
    "ds_read_b128 v[192:195], v101 offset:0\n"
    "buffer_load_dwordx4 a[136:139], v0, s[16:19], s91 offen offset:2048 sc1\n"
    "v_mfma_f32_16x16x32_bf16 v[20:23], v[168:171], a[84:87], v[20:23]\n"
    "v_mfma_f32_16x16x32_bf16 v[24:27], v[172:175], a[80:83], v[24:27]\n"
    "ds_read_b128 v[196:199], v101 offset:1024\n"
    "buffer_load_dwordx4 a[140:143], v0, s[16:19], s91 offen offset:2304 sc1\n"
    "v_mfma_f32_16x16x32_bf16 v[28:31], v[172:175], a[84:87], v[28:31]\n"
    "v_mfma_f32_16x16x32_bf16 v[32:35], v[176:179], a[80:83], v[32:35]\n"
    "ds_read_b128 v[200:203], v104 offset:0\n"
    "v_mfma_f32_16x16x32_bf16 v[36:39], v[176:179], a[84:87], v[36:39]\n"
    "s_waitcnt vmcnt(12) lgkmcnt(3)\n"
    "v_mfma_f32_16x16x32_bf16 v[16:19], v[180:183], a[88:91], v[16:19]\n"
    "ds_read_b128 v[204:207], v101 offset:2560\n"
    "s_add_u32 s91, s38, 98304\n"
    "buffer_load_dwordx4 a[144:147], v0, s[16:19], s91 offen offset:0 sc1\n"
    "v_mfma_f32_16x16x32_bf16 v[20:23], v[180:183], a[92:95], v[20:23]\n"
    "v_mfma_f32_16x16x32_bf16 v[24:27], v[184:187], a[88:91], v[24:27]\n"
    "ds_read_b128 v[168:171], v101 offset:3584\n"
    "buffer_load_dwordx4 a[148:151], v0, s[16:19], s91 offen offset:256 sc1\n"
    "v_mfma_f32_16x16x32_bf16 v[28:31], v[184:187], a[92:95], v[28:31]\n"
    "v_mfma_f32_16x16x32_bf16 v[32:35], v[188:191], a[88:91], v[32:35]\n"
    "ds_read_b128 v[172:175], v104 offset:2560\n"
    "v_mfma_f32_16x16x32_bf16 v[36:39], v[188:191], a[92:95], v[36:39]\n"
    "s_waitcnt vmcnt(12) lgkmcnt(3)\n"
    "v_mfma_f32_16x16x32_bf16 v[16:19], v[192:195], a[96:99], v[16:19]\n"
    "ds_read_b128 v[176:179], v101 offset:5120\n"
    "buffer_load_dwordx4 a[152:155], v0, s[16:19], s91 offen offset:2048 sc1\n"
    "v_mfma_f32_16x16x32_bf16 v[20:23], v[192:195], a[100:103], v[20:23]\n"
    "v_mfma_f32_16x16x32_bf16 v[24:27], v[196:199], a[96:99], v[24:27]\n"
    "ds_read_b128 v[180:183], v101 offset:6144\n"
    "buffer_load_dwordx4 a[156:159], v0, s[16:19], s91 offen offset:2304 sc1\n"
    "v_mfma_f32_16x16x32_bf16 v[28:31], v[196:199], a[100:103], v[28:31]\n"
    "v_mfma_f32_16x16x32_bf16 v[32:35], v[200:203], a[96:99], v[32:35]\n"
    "ds_read_b128 v[184:187], v104 offset:5120\n"
    "v_mfma_f32_16x16x32_bf16 v[36:39], v[200:203], a[100:103], v[36:39]\n"
    "s_waitcnt vmcnt(12) lgkmcnt(3)\n"
    "v_mfma_f32_16x16x32_bf16 v[16:19], v[204:207], a[104:107], v[16:19]\n"
    "ds_read_b128 v[188:191], v101 offset:7680\n"
    "s_add_u32 s91, s38, 102400\n"
    "buffer_load_dwordx4 a[160:163], v0, s[16:19], s91 offen offset:0 sc1\n"
    "v_mfma_f32_16x16x32_bf16 v[20:23], v[204:207], a[108:111], v[20:23]\n"
    "v_mfma_f32_16x16x32_bf16 v[24:27], v[168:171], a[104:107], v[24:27]\n"
    "ds_read_b128 v[192:195], v101 offset:8704\n"
    "buffer_load_dwordx4 a[164:167], v0, s[16:19], s91 offen offset:256 sc1\n"
    "v_mfma_f32_16x16x32_bf16 v[28:31], v[168:171], a[108:111], v[28:31]\n"
    "v_mfma_f32_16x16x32_bf16 v[32:35], v[172:175], a[104:107], v[32:35]\n"
    "ds_read_b128 v[196:199], v104 offset:7680\n"
    "v_mfma_f32_16x16x32_bf16 v[36:39], v[172:175], a[108:111], v[36:39]\n"
    "s_waitcnt vmcnt(12) lgkmcnt(3)\n"
    "v_mfma_f32_16x16x32_bf16 v[16:19], v[176:179], a[112:115], v[16:19]\n"
    "ds_read_b128 v[200:203], v101 offset:10240\n"
    "buffer_load_dwordx4 a[168:171], v0, s[16:19], s91 offen offset:2048 sc1\n"
    "v_mfma_f32_16x16x32_bf16 v[20:23], v[176:179], a[116:119], v[20:23]\n"
    "v_mfma_f32_16x16x32_bf16 v[24:27], v[180:183], a[112:115], v[24:27]\n"
    "ds_read_b128 v[204:207], v101 offset:11264\n"
    "buffer_load_dwordx4 a[172:175], v0, s[16:19], s91 offen offset:2304 sc1\n"
    "v_mfma_f32_16x16x32_bf16 v[28:31], v[180:183], a[116:119], v[28:31]\n"
    "v_mfma_f32_16x16x32_bf16 v[32:35], v[184:187], a[112:115], v[32:35]\n"
    "ds_read_b128 v[168:171], v104 offset:10240\n"
    "v_mfma_f32_16x16x32_bf16 v[36:39], v[184:187], a[116:119], v[36:39]\n"
    "s_waitcnt vmcnt(12) lgkmcnt(3)\n"
    "v_mfma_f32_16x16x32_bf16 v[16:19], v[188:191], a[120:123], v[16:19]\n"
    "ds_read_b128 v[172:175], v101 offset:12800\n"
    "s_add_u32 s91, s38, 106496\n"
    "buffer_load_dwordx4 a[176:179], v0, s[16:19], s91 offen offset:0 sc1\n"
    "v_mfma_f32_16x16x32_bf16 v[20:23], v[188:191], a[124:127], v[20:23]\n"
    "v_mfma_f32_16x16x32_bf16 v[24:27], v[192:195], a[120:123], v[24:27]\n"
    "ds_read_b128 v[176:179], v101 offset:13824\n"
    "buffer_load_dwordx4 a[180:183], v0, s[16:19], s91 offen offset:256 sc1\n"
    "v_mfma_f32_16x16x32_bf16 v[28:31], v[192:195], a[124:127], v[28:31]\n"
    "v_mfma_f32_16x16x32_bf16 v[32:35], v[196:199], a[120:123], v[32:35]\n"
    "ds_read_b128 v[180:183], v104 offset:12800\n"
    "v_mfma_f32_16x16x32_bf16 v[36:39], v[196:199], a[124:127], v[36:39]\n"
    "s_waitcnt vmcnt(12) lgkmcnt(3)\n"
    "v_mfma_f32_16x16x32_bf16 v[16:19], v[200:203], a[128:131], v[16:19]\n"
    "ds_read_b128 v[184:187], v101 offset:16384\n"
    "buffer_load_dwordx4 a[184:187], v0, s[16:19], s91 offen offset:2048 sc1\n"
    "v_mfma_f32_16x16x32_bf16 v[20:23], v[200:203], a[132:135], v[20:23]\n"
    "v_mfma_f32_16x16x32_bf16 v[24:27], v[204:207], a[128:131], v[24:27]\n"
    "ds_read_b128 v[188:191], v101 offset:17408\n"
    "buffer_load_dwordx4 a[188:191], v0, s[16:19], s91 offen offset:2304 sc1\n"
    "v_mfma_f32_16x16x32_bf16 v[28:31], v[204:207], a[132:135], v[28:31]\n"
    "v_mfma_f32_16x16x32_bf16 v[32:35], v[168:171], a[128:131], v[32:35]\n"
    "ds_read_b128 v[192:195], v104 offset:16384\n"
    "v_mfma_f32_16x16x32_bf16 v[36:39], v[168:171], a[132:135], v[36:39]\n"
    "s_waitcnt vmcnt(12) lgkmcnt(3)\n"
    "v_mfma_f32_16x16x32_bf16 v[16:19], v[172:175], a[136:139], v[16:19]\n"
    "ds_read_b128 v[196:199], v101 offset:18944\n"
    "v_mfma_f32_16x16x32_bf16 v[20:23], v[172:175], a[140:143], v[20:23]\n"
    "v_mfma_f32_16x16x32_bf16 v[24:27], v[176:179], a[136:139], v[24:27]\n"
    "ds_read_b128 v[200:203], v101 offset:19968\n"
    "v_mfma_f32_16x16x32_bf16 v[28:31], v[176:179], a[140:143], v[28:31]\n"
    "v_mfma_f32_16x16x32_bf16 v[32:35], v[180:183], a[136:139], v[32:35]\n"
    "ds_read_b128 v[204:207], v104 offset:18944\n"
    "v_mfma_f32_16x16x32_bf16 v[36:39], v[180:183], a[140:143], v[36:39]\n"
    "s_waitcnt vmcnt(10) lgkmcnt(3)\n"
    "v_mfma_f32_16x16x32_bf16 v[16:19], v[184:187], a[144:147], v[16:19]\n"
    "ds_read_b128 v[168:171], v101 offset:21504\n"
    "v_mfma_f32_16x16x32_bf16 v[20:23], v[184:187], a[148:151], v[20:23]\n"
    "v_mfma_f32_16x16x32_bf16 v[24:27], v[188:191], a[144:147], v[24:27]\n"
    "ds_read_b128 v[172:175], v101 offset:22528\n"
    "v_mfma_f32_16x16x32_bf16 v[28:31], v[188:191], a[148:151], v[28:31]\n"
    "v_mfma_f32_16x16x32_bf16 v[32:35], v[192:195], a[144:147], v[32:35]\n"
    "ds_read_b128 v[176:179], v104 offset:21504\n"
    "v_mfma_f32_16x16x32_bf16 v[36:39], v[192:195], a[148:151], v[36:39]\n"
    "s_waitcnt vmcnt(8) lgkmcnt(3)\n"
    "v_mfma_f32_16x16x32_bf16 v[16:19], v[196:199], a[152:155], v[16:19]\n"
    "ds_read_b128 v[180:183], v101 offset:24064\n"
    "v_mfma_f32_16x16x32_bf16 v[20:23], v[196:199], a[156:159], v[20:23]\n"
    "v_mfma_f32_16x16x32_bf16 v[24:27], v[200:203], a[152:155], v[24:27]\n"
    "ds_read_b128 v[184:187], v101 offset:25088\n"
    "v_mfma_f32_16x16x32_bf16 v[28:31], v[200:203], a[156:159], v[28:31]\n"
    "v_mfma_f32_16x16x32_bf16 v[32:35], v[204:207], a[152:155], v[32:35]\n"
    "ds_read_b128 v[188:191], v104 offset:24064\n"
    "v_mfma_f32_16x16x32_bf16 v[36:39], v[204:207], a[156:159], v[36:39]\n"
    "s_waitcnt vmcnt(6) lgkmcnt(3)\n"
    "v_mfma_f32_16x16x32_bf16 v[16:19], v[168:171], a[160:163], v[16:19]\n"
    "ds_read_b128 v[192:195], v101 offset:26624\n"
    "v_mfma_f32_16x16x32_bf16 v[20:23], v[168:171], a[164:167], v[20:23]\n"
    "v_mfma_f32_16x16x32_bf16 v[24:27], v[172:175], a[160:163], v[24:27]\n"
    "ds_read_b128 v[196:199], v101 offset:27648\n"
    "v_mfma_f32_16x16x32_bf16 v[28:31], v[172:175], a[164:167], v[28:31]\n"
    "v_mfma_f32_16x16x32_bf16 v[32:35], v[176:179], a[160:163], v[32:35]\n"
    "ds_read_b128 v[200:203], v104 offset:26624\n"
    "v_mfma_f32_16x16x32_bf16 v[36:39], v[176:179], a[164:167], v[36:39]\n"
    "s_waitcnt vmcnt(4) lgkmcnt(3)\n"
    "v_mfma_f32_16x16x32_bf16 v[16:19], v[180:183], a[168:171], v[16:19]\n"
    "ds_read_b128 v[204:207], v101 offset:29184\n"
    "v_mfma_f32_16x16x32_bf16 v[20:23], v[180:183], a[172:175], v[20:23]\n"
    "v_mfma_f32_16x16x32_bf16 v[24:27], v[184:187], a[168:171], v[24:27]\n"
    "ds_read_b128 v[168:171], v101 offset:30208\n"
    "v_mfma_f32_16x16x32_bf16 v[28:31], v[184:187], a[172:175], v[28:31]\n"
    "v_mfma_f32_16x16x32_bf16 v[32:35], v[188:191], a[168:171], v[32:35]\n"
    "ds_read_b128 v[172:175], v104 offset:29184\n"
    "v_mfma_f32_16x16x32_bf16 v[36:39], v[188:191], a[172:175], v[36:39]\n"
    "s_waitcnt vmcnt(2) lgkmcnt(3)\n"
    "v_mfma_f32_16x16x32_bf16 v[16:19], v[192:195], a[176:179], v[16:19]\n"
    "v_mfma_f32_16x16x32_bf16 v[20:23], v[192:195], a[180:183], v[20:23]\n"
    "v_mfma_f32_16x16x32_bf16 v[24:27], v[196:199], a[176:179], v[24:27]\n"
    "v_mfma_f32_16x16x32_bf16 v[28:31], v[196:199], a[180:183], v[28:31]\n"
    "v_mfma_f32_16x16x32_bf16 v[32:35], v[200:203], a[176:179], v[32:35]\n"
    "v_mfma_f32_16x16x32_bf16 v[36:39], v[200:203], a[180:183], v[36:39]\n"
    "s_waitcnt vmcnt(0) lgkmcnt(0)\n"
    "v_mfma_f32_16x16x32_bf16 v[16:19], v[204:207], a[184:187], v[16:19]\n"
    "v_mfma_f32_16x16x32_bf16 v[20:23], v[204:207], a[188:191], v[20:23]\n"
    "v_mfma_f32_16x16x32_bf16 v[24:27], v[168:171], a[184:187], v[24:27]\n"
    "v_mfma_f32_16x16x32_bf16 v[28:31], v[168:171], a[188:191], v[28:31]\n"
    "v_mfma_f32_16x16x32_bf16 v[32:35], v[172:175], a[184:187], v[32:35]\n"
    "v_mfma_f32_16x16x32_bf16 v[36:39], v[172:175], a[188:191], v[36:39]\n"
    "s_nop 2\n"
    "v_cvt_pk_bf16_f32 v240, v16, v17\n"
    "v_cvt_pk_bf16_f32 v241, v18, v19\n"
    "v_cvt_pk_bf16_f32 v242, v20, v21\n"
    "v_cvt_pk_bf16_f32 v243, v22, v23\n"
    "v_cvt_pk_bf16_f32 v244, v24, v25\n"
    "v_cvt_pk_bf16_f32 v245, v26, v27\n"
    "v_cvt_pk_bf16_f32 v246, v28, v29\n"
    "v_cvt_pk_bf16_f32 v247, v30, v31\n"
    "v_cvt_pk_bf16_f32 v248, v32, v33\n"
    "v_cvt_pk_bf16_f32 v249, v34, v35\n"
    "v_cvt_pk_bf16_f32 v250, v36, v37\n"
    "v_cvt_pk_bf16_f32 v251, v38, v39\n"
    "v_cndmask_b32 v248, v248, 0, s[48:49]\n"
    "v_cndmask_b32 v249, v249, 0, s[48:49]\n"
    "v_cndmask_b32 v250, v250, 0, s[48:49]\n"
    "v_cndmask_b32 v251, v251, 0, s[48:49]\n"
    "s_cmp_lg_u32 s28, 0\n"
    "s_cbranch_scc1 Lfin_22_%=\n"
    "v_mov_b32 v64, v240\n"
    "v_mov_b32 v65, v241\n"
    "v_mov_b32 v66, v242\n"
    "v_mov_b32 v67, v243\n"
    "v_mov_b32 v68, v244\n"
    "v_mov_b32 v69, v245\n"
    "v_mov_b32 v70, v246\n"
    "v_mov_b32 v71, v247\n"
    "v_mov_b32 v72, v248\n"
    "v_mov_b32 v73, v249\n"
    "v_mov_b32 v74, v250\n"
    "v_mov_b32 v75, v251\n"
    "v_permlane16_swap_b32 v240, v242\n"
    "v_permlane16_swap_b32 v241, v243\n"
    "v_permlane16_swap_b32 v244, v246\n"
    "v_permlane16_swap_b32 v245, v247\n"
    "v_permlane16_swap_b32 v248, v250\n"
    "v_permlane16_swap_b32 v249, v251\n"
    "s_mul_i32 s39, s27, 40960\n"
    "s_add_u32 s39, s39, s53\n"
    "s_add_u32 s39, s39, s40\n"
    "s_add_u32 s39, s39, 15880192\n"
    "buffer_store_dwordx4 v[240:243], v1, s[16:19], s39 offen sc1\n"
    "buffer_store_dwordx4 v[244:247], v1, s[16:19], s39 offen offset:1024 sc1\n"
    "buffer_store_dwordx4 v[248:251], v9, s[16:19], s39 offen offset:2048 sc1\n"
    "v_dot2_f32_bf16 v48, v64, v222, 0\n"
    "v_dot2_f32_bf16 v49, v64, v64, 0\n"
    "v_dot2_f32_bf16 v48, v65, v222, v48\n"
    "v_dot2_f32_bf16 v49, v65, v65, v49\n"
    "v_dot2_f32_bf16 v48, v68, v222, v48\n"
    "v_dot2_f32_bf16 v49, v68, v68, v49\n"
    "v_dot2_f32_bf16 v48, v69, v222, v48\n"
    "v_dot2_f32_bf16 v49, v69, v69, v49\n"
    "v_dot2_f32_bf16 v48, v72, v222, v48\n"
    "v_dot2_f32_bf16 v49, v72, v72, v49\n"
    "v_dot2_f32_bf16 v48, v73, v222, v48\n"
    "v_dot2_f32_bf16 v49, v73, v73, v49\n"
    "v_dot2_f32_bf16 v50, v66, v222, 0\n"
    "v_dot2_f32_bf16 v51, v66, v66, 0\n"
    "v_dot2_f32_bf16 v50, v67, v222, v50\n"
    "v_dot2_f32_bf16 v51, v67, v67, v51\n"
    "v_dot2_f32_bf16 v50, v70, v222, v50\n"
    "v_dot2_f32_bf16 v51, v70, v70, v51\n"
    "v_dot2_f32_bf16 v50, v71, v222, v50\n"
    "v_dot2_f32_bf16 v51, v71, v71, v51\n"
    "v_dot2_f32_bf16 v50, v74, v222, v50\n"
    "v_dot2_f32_bf16 v51, v74, v74, v51\n"
    "v_dot2_f32_bf16 v50, v75, v222, v50\n"
    "v_dot2_f32_bf16 v51, v75, v75, v51\n"
    "v_mov_b32 v52, v48\n"
    "v_mov_b32 v53, v49\n"
    "v_mov_b32 v54, v50\n"
    "v_mov_b32 v55, v51\n"
    "v_permlane32_swap_b32 v48, v52\n"
    "v_permlane32_swap_b32 v49, v53\n"
    "v_permlane32_swap_b32 v50, v54\n"
    "v_permlane32_swap_b32 v51, v55\n"
    "v_add_f32 v48, v48, v52\n"
    "v_add_f32 v49, v49, v53\n"
    "v_add_f32 v50, v50, v54\n"
    "v_add_f32 v51, v51, v55\n"
    "v_mov_b32 v52, v48\n"
    "v_mov_b32 v53, v49\n"
    "v_mov_b32 v54, v50\n"
    "v_mov_b32 v55, v51\n"
    "v_permlane16_swap_b32 v48, v52\n"
    "v_permlane16_swap_b32 v49, v53\n"
    "v_permlane16_swap_b32 v50, v54\n"
    "v_permlane16_swap_b32 v51, v55\n"
    "v_add_f32 v48, v48, v52\n"
    "v_add_f32 v49, v49, v53\n"
    "v_add_f32 v50, v50, v54\n"
    "v_add_f32 v51, v51, v55\n"
    "v_cndmask_b32 v48, v48, v50, s[8:9]\n"
    "v_cndmask_b32 v49, v49, v51, s[8:9]\n"
    "s_lshl_b32 s31, s27, 4\n"
    "s_add_u32 s31, s31, s25\n"
    "s_lshl_b32 s31, s31, 8\n"
    "s_add_u32 s31, s31, s41\n"
    "s_add_u32 s31, s31, 21647360\n"
    "v_lshrrev_b32 v52, 1, v9\n"
    "buffer_store_dwordx2 v[48:49], v52, s[16:19], s31 offen sc1\n"
    "s_mul_i32 s37, s25, 163840\n"
    "s_add_u32 s37, s37, 262144\n"
    "s_add_u32 s37, s37, s29\n"
    "s_waitcnt lgkmcnt(0)\n"
    "s_barrier\n"
    "s_cmp_eq_u32 s26, 0\n"
    "s_cbranch_scc1 Lt1_68_%=\n"
    "s_add_u32 s35, s37, 0\n"
    "s_sub_u32 s35, s35, s29\n"
    "s_sub_u32 s30, s93, 1024\n"
    "s_add_u32 s31, s30, s35\n"
    "s_add_u32 m0, s30, 32768\n"
    "s_add_u32 s14, s31, 0\n"
    "buffer_load_dwordx4 v1, s[16:19], s14 offen sc1 lds\n"
    "s_add_u32 m0, s30, 35840\n"
    "s_add_u32 s14, s31, 3072\n"
    "buffer_load_dwordx4 v1, s[16:19], s14 offen sc1 lds\n"
    "s_add_u32 m0, s30, 38912\n"
    "s_add_u32 s14, s31, 6144\n"
    "buffer_load_dwordx4 v1, s[16:19], s14 offen sc1 lds\n"
    "s_add_u32 m0, s30, 41984\n"
    "s_add_u32 s14, s31, 9216\n"
    "buffer_load_dwordx4 v1, s[16:19], s14 offen sc1 lds\n"
    "s_waitcnt vmcnt(4)\n"
    "s_branch Lt2_69_%=\n"
    "Lt1_68_%=:\n"
    "s_waitcnt vmcnt(0)\n"
    "Lt2_69_%=:\n"
    "s_barrier\n"
    "s_cmp_lg_u32 s26, 0\n"
    "s_cbranch_scc1 Lnotw0_70_%=\n"
    "s_lshl_b32 s36, s24, 10\n"
    "s_add_u32 s36, s36, s45\n"
    "s_add_u32 s36, s36, 49152\n"
    "v_mov_b32 v208, 0\n"
    "v_mov_b32 v209, 1\n"
    "s_mov_b64 exec, 1\n"
    "s_mov_b32 s50, 0\n"
    "Lgol_73_%=:\n"
    "buffer_atomic_add v209, v208, s[16:19], s36 offen sc0\n"
    "s_waitcnt vmcnt(0)\n"
    "v_readfirstlane_b32 s52, v209\n"
    "s_cmp_eq_u32 s50, 0\n"
    "s_cselect_b32 s51, 1, 0\n"
    "s_add_u32 s52, s52, s51\n"
    "v_mov_b32 v209, 0\n"
    "s_add_u32 s50, s50, 1\n"
    "s_cmp_ge_u32 s52, 16\n"
    "s_cbranch_scc1 Lgod_72_%=\n"
    "s_cmp_lt_u32 s50, 20000\n"
    "s_cbranch_scc1 Lgol_73_%=\n"
    "Lgod_72_%=:\n"
    "s_mov_b64 exec, -1\n"
    "s_branch Ltj_71_%=\n"
    "Lnotw0_70_%=:\n"
    "s_add_u32 m0, s30, 45056\n"
    "s_add_u32 s14, s31, 12288\n"
    "buffer_load_dwordx4 v1, s[16:19], s14 offen sc1 lds\n"
    "s_add_u32 m0, s30, 48128\n"
    "s_add_u32 s14, s31, 15360\n"
    "buffer_load_dwordx4 v1, s[16:19], s14 offen sc1 lds\n"
    "s_add_u32 m0, s30, 51200\n"
    "s_add_u32 s14, s31, 18432\n"
    "buffer_load_dwordx4 v1, s[16:19], s14 offen sc1 lds\n"
    "s_add_u32 m0, s30, 54272\n"
    "s_add_u32 s14, s31, 21504\n"
    "buffer_load_dwordx4 v1, s[16:19], s14 offen sc1 lds\n"
    "s_add_u32 m0, s30, 57344\n"
    "s_add_u32 s14, s31, 24576\n"
    "buffer_load_dwordx4 v1, s[16:19], s14 offen sc1 lds\n"
    "s_add_u32 m0, s30, 60416\n"
    "s_add_u32 s14, s31, 27648\n"
    "buffer_load_dwordx4 v1, s[16:19], s14 offen sc1 lds\n"
    "s_add_u32 m0, s30, 63488\n"
    "s_add_u32 s14, s31, 30720\n"
    "buffer_load_dwordx4 v1, s[16:19], s14 offen sc1 lds\n"
    "s_add_u32 m0, s30, 66560\n"
    "s_add_u32 s14, s31, 33792\n"
    "buffer_load_dwordx4 v1, s[16:19], s14 offen sc1 lds\n"
    "s_add_u32 m0, s30, 69632\n"
    "s_add_u32 s14, s31, 36864\n"
    "buffer_load_dwordx4 v1, s[16:19], s14 offen sc1 lds\n"
    "s_add_u32 m0, s30, 72704\n"
    "s_add_u32 s14, s31, 39936\n"
    "buffer_load_dwordx4 v1, s[16:19], s14 offen sc1 lds\n"
    "s_add_u32 m0, s30, 75776\n"
    "s_add_u32 s14, s31, 43008\n"
    "buffer_load_dwordx4 v1, s[16:19], s14 offen sc1 lds\n"
    "s_add_u32 m0, s30, 78848\n"
    "s_add_u32 s14, s31, 46080\n"
    "buffer_load_dwordx4 v1, s[16:19], s14 offen sc1 lds\n"
    "s_add_u32 m0, s30, 81920\n"
    "s_add_u32 s14, s31, 49152\n"
    "buffer_load_dwordx4 v1, s[16:19], s14 offen sc1 lds\n"
    "s_add_u32 m0, s30, 84992\n"
    "s_add_u32 s14, s31, 52224\n"
    "buffer_load_dwordx4 v1, s[16:19], s14 offen sc1 lds\n"
    "s_add_u32 m0, s30, 88064\n"
    "s_add_u32 s14, s31, 55296\n"
    "buffer_load_dwordx4 v1, s[16:19], s14 offen sc1 lds\n"
    "s_add_u32 m0, s30, 91136\n"
    "s_add_u32 s14, s31, 58368\n"
    "buffer_load_dwordx4 v1, s[16:19], s14 offen sc1 lds\n"
    "s_add_u32 m0, s30, 94208\n"
    "s_add_u32 s14, s31, 61440\n"
    "buffer_load_dwordx4 v1, s[16:19], s14 offen sc1 lds\n"
    "s_add_u32 m0, s30, 97280\n"
    "s_add_u32 s14, s31, 64512\n"
    "buffer_load_dwordx4 v1, s[16:19], s14 offen sc1 lds\n"
    "s_add_u32 m0, s30, 100352\n"
    "s_add_u32 s14, s31, 67584\n"
    "buffer_load_dwordx4 v1, s[16:19], s14 offen sc1 lds\n"
    "s_add_u32 m0, s30, 103424\n"
    "s_add_u32 s14, s31, 70656\n"
    "buffer_load_dwordx4 v1, s[16:19], s14 offen sc1 lds\n"
    "s_add_u32 m0, s30, 106496\n"
    "s_add_u32 s14, s31, 73728\n"
    "buffer_load_dwordx4 v1, s[16:19], s14 offen sc1 lds\n"
    "s_add_u32 m0, s30, 109568\n"
    "s_add_u32 s14, s31, 76800\n"
    "buffer_load_dwordx4 v1, s[16:19], s14 offen sc1 lds\n"
    "s_add_u32 m0, s30, 112640\n"
    "s_add_u32 s14, s31, 79872\n"
    "buffer_load_dwordx4 v1, s[16:19], s14 offen sc1 lds\n"
    "s_add_u32 m0, s30, 115712\n"
    "s_add_u32 s14, s31, 82944\n"
    "buffer_load_dwordx4 v1, s[16:19], s14 offen sc1 lds\n"
    "s_add_u32 m0, s30, 118784\n"
    "s_add_u32 s14, s31, 86016\n"
    "buffer_load_dwordx4 v1, s[16:19], s14 offen sc1 lds\n"
    "s_add_u32 m0, s30, 121856\n"
    "s_add_u32 s14, s31, 89088\n"
    "buffer_load_dwordx4 v1, s[16:19], s14 offen sc1 lds\n"
    "s_add_u32 m0, s30, 124928\n"
    "s_add_u32 s14, s31, 92160\n"
    "buffer_load_dwordx4 v1, s[16:19], s14 offen sc1 lds\n"
    "s_add_u32 m0, s30, 128000\n"
    "s_add_u32 s14, s31, 95232\n"
    "buffer_load_dwordx4 v1, s[16:19], s14 offen sc1 lds\n"
    "s_add_u32 m0, s30, 131072\n"
    "s_add_u32 s14, s31, 98304\n"
    "buffer_load_dwordx4 v1, s[16:19], s14 offen sc1 lds\n"
    "s_add_u32 m0, s30, 134144\n"
    "s_add_u32 s14, s31, 101376\n"
    "buffer_load_dwordx4 v1, s[16:19], s14 offen sc1 lds\n"
    "s_add_u32 m0, s30, 137216\n"
    "s_add_u32 s14, s31, 104448\n"
    "buffer_load_dwordx4 v1, s[16:19], s14 offen sc1 lds\n"
    "s_add_u32 m0, s30, 140288\n"
    "s_add_u32 s14, s31, 107520\n"
    "buffer_load_dwordx4 v1, s[16:19], s14 offen sc1 lds\n"
    "s_add_u32 m0, s30, 143360\n"
    "s_add_u32 s14, s31, 110592\n"
    "buffer_load_dwordx4 v1, s[16:19], s14 offen sc1 lds\n"
    "s_add_u32 m0, s30, 144384\n"
    "s_add_u32 s14, s31, 111616\n"
    "buffer_load_dwordx4 v1, s[16:19], s14 offen sc1 lds\n"
    "s_add_u32 s35, s37, 114688\n"
    "s_sub_u32 s35, s35, s29\n"
    "s_sub_u32 s30, s93, 1024\n"
    "s_add_u32 s31, s30, s35\n"
    "s_add_u32 m0, s30, 0\n"
    "s_add_u32 s14, s31, 0\n"
    "buffer_load_dwordx4 v1, s[16:19], s14 offen sc1 lds\n"
    "s_add_u32 m0, s30, 3072\n"
    "s_add_u32 s14, s31, 3072\n"
    "buffer_load_dwordx4 v1, s[16:19], s14 offen sc1 lds\n"
    "s_add_u32 m0, s30, 6144\n"
    "s_add_u32 s14, s31, 6144\n"
    "buffer_load_dwordx4 v1, s[16:19], s14 offen sc1 lds\n"
    "s_add_u32 m0, s30, 9216\n"
    "s_add_u32 s14, s31, 9216\n"
    "buffer_load_dwordx4 v1, s[16:19], s14 offen sc1 lds\n"
    "s_add_u32 m0, s30, 12288\n"
    "s_add_u32 s14, s31, 12288\n"
    "buffer_load_dwordx4 v1, s[16:19], s14 offen sc1 lds\n"
    "s_add_u32 m0, s30, 13312\n"
    "s_add_u32 s14, s31, 13312\n"
    "buffer_load_dwordx4 v1, s[16:19], s14 offen sc1 lds\n"
    "s_waitcnt vmcnt(0)\n"
    "Ltj_71_%=:\n"
    "s_barrier\n"
    "s_mov_b32 s28, 1\n"
    "s_mov_b32 s40, 5242880\n"
    "s_mov_b32 s41, 524288\n"
    "s_mov_b32 s42, 2621440\n"
    "s_mov_b32 s43, 7077888\n"
    "s_mov_b32 s44, 6291456\n"
    "s_mov_b32 s46, 32768\n"
    "s_mov_b32 s45, 65536\n"
    "s_nop 12\n"
    "s_branch Ltop_21_%=\n"
    "Lfin_22_%=:\n"
    "s_nop 12\n"
    "v_permlane16_swap_b32 v240, v242\n"
    "v_permlane16_swap_b32 v241, v243\n"
    "v_permlane16_swap_b32 v244, v246\n"
    "v_permlane16_swap_b32 v245, v247\n"
    "v_permlane16_swap_b32 v248, v250\n"
    "v_permlane16_swap_b32 v249, v251\n"
    "v_mul_u32_u24 v48, 1280, v4\n"
    "v_lshl_add_u32 v48, v5, 4, v48\n"
    "v_mov_b32 v49, 0x7ffffff0\n"
    "v_cndmask_b32 v49, v48, v49, s[48:49]\n"
    "s_mul_i32 s31, s27, 40960\n"
    "s_mul_i32 s35, s25, 80\n"
    "s_add_u32 s31, s31, s35\n"
    "buffer_store_dwordx4 v[240:243], v48, s[20:23], s31 offen\n"
    "buffer_store_dwordx4 v[244:247], v48, s[20:23], s31 offen offset:32\n"
    "buffer_store_dwordx4 v[248:251], v49, s[20:23], s31 offen offset:64\n"
    "s_waitcnt vmcnt(0) lgkmcnt(0)\n"

    : "+v"(tid) : "s"(__builtin_amdgcn_kernarg_segment_ptr()), "s"(blockIdx.x)
    : "memory", "vcc", "scc", "v1", "v2", "v3", "v4", "v5", "v6", "v7", "v8", "v9", "v10", "v11", "v12", "v13", "v14", "v15", "v16", "v17", "v18", "v19", "v20", "v21", "v22", "v23", "v24", "v25", "v26", "v27", "v28", "v29", "v30", "v31", "v32", "v33", "v34", "v35", "v36", "v37", "v38", "v39", "v40", "v41", "v42", "v43", "v44", "v45", "v46", "v47", "v48", "v49", "v50", "v51", "v52", "v53", "v54", "v55", "v56", "v57", "v58", "v59", "v60", "v61", "v62", "v63", "v64", "v65", "v66", "v67", "v68", "v69", "v70", "v71", "v72", "v73", "v74", "v75", "v76", "v77", "v78", "v79", "v80", "v81", "v82", "v83", "v84", "v85", "v86", "v87", "v88", "v89", "v90", "v91", "v92", "v93", "v94", "v95", "v96", "v97", "v98", "v99", "v100", "v101", "v102", "v103", "v104", "v105", "v106", "v107", "v108", "v109", "v110", "v111", "v112", "v113", "v114", "v115", "v116", "v117", "v118", "v119", "v120", "v121", "v122", "v123", "v124", "v125", "v126", "v127", "v128", "v129", "v130", "v131", "v132", "v133", "v134", "v135", "v136", "v137", "v138", "v139", "v140", "v141", "v142", "v143", "v144", "v145", "v146", "v147", "v148", "v149", "v150", "v151", "v152", "v153", "v154", "v155", "v156", "v157", "v158", "v159", "v160", "v161", "v162", "v163", "v164", "v165", "v166", "v167", "v168", "v169", "v170", "v171", "v172", "v173", "v174", "v175", "v176", "v177", "v178", "v179", "v180", "v181", "v182", "v183", "v184", "v185", "v186", "v187", "v188", "v189", "v190", "v191", "v192", "v193", "v194", "v195", "v196", "v197", "v198", "v199", "v200", "v201", "v202", "v203", "v204", "v205", "v206", "v207", "v208", "v209", "v210", "v211", "v212", "v213", "v214", "v215", "v216", "v217", "v218", "v219", "v220", "v221", "v222", "v223", "v224", "v225", "v226", "v227", "v228", "v229", "v230", "v231", "v232", "v233", "v234", "v235", "v236", "v237", "v238", "v239", "v240", "v241", "v242", "v243", "v244", "v245", "v246", "v247", "v248", "v249", "v250", "v251", "v252", "v253", "v254", "v255", "a0", "a1", "a2", "a3", "a4", "a5", "a6", "a7", "a8", "a9", "a10", "a11", "a12", "a13", "a14", "a15", "a16", "a17", "a18", "a19", "a20", "a21", "a22", "a23", "a24", "a25", "a26", "a27", "a28", "a29", "a30", "a31", "a32", "a33", "a34", "a35", "a36", "a37", "a38", "a39", "a40", "a41", "a42", "a43", "a44", "a45", "a46", "a47", "a48", "a49", "a50", "a51", "a52", "a53", "a54", "a55", "a56", "a57", "a58", "a59", "a60", "a61", "a62", "a63", "a64", "a65", "a66", "a67", "a68", "a69", "a70", "a71", "a72", "a73", "a74", "a75", "a76", "a77", "a78", "a79", "a80", "a81", "a82", "a83", "a84", "a85", "a86", "a87", "a88", "a89", "a90", "a91", "a92", "a93", "a94", "a95", "a96", "a97", "a98", "a99", "a100", "a101", "a102", "a103", "a104", "a105", "a106", "a107", "a108", "a109", "a110", "a111", "a112", "a113", "a114", "a115", "a116", "a117", "a118", "a119", "a120", "a121", "a122", "a123", "a124", "a125", "a126", "a127", "a128", "a129", "a130", "a131", "a132", "a133", "a134", "a135", "a136", "a137", "a138", "a139", "a140", "a141", "a142", "a143", "a144", "a145", "a146", "a147", "a148", "a149", "a150", "a151", "a152", "a153", "a154", "a155", "a156", "a157", "a158", "a159", "a160", "a161", "a162", "a163", "a164", "a165", "a166", "a167", "a168", "a169", "a170", "a171", "a172", "a173", "a174", "a175", "a176", "a177", "a178", "a179", "a180", "a181", "a182", "a183", "a184", "a185", "a186", "a187", "a188", "a189", "a190", "a191", "a192", "a193", "a194", "a195", "a196", "a197", "a198", "a199", "a200", "a201", "a202", "a203", "a204", "a205", "a206", "a207", "a208", "a209", "a210", "a211", "a212", "a213", "a214", "a215", "a216", "a217", "a218", "a219", "a220", "a221", "a222", "a223", "a224", "a225", "a226", "a227", "a228", "a229", "a230", "a231", "a232", "a233", "a234", "a235", "a236", "a237", "a238", "a239", "a240", "a241", "a242", "a243", "a244", "a245", "a246", "a247", "a248", "a249", "a250", "a251", "a252", "a253", "a254", "a255", "s8", "s9", "s10", "s11", "s12", "s13", "s14", "s15", "s16", "s17", "s18", "s19", "s20", "s21", "s22", "s23", "s24", "s25", "s26", "s27", "s28", "s29", "s30", "s31", "s35", "s36", "s37", "s38", "s39", "s40", "s41", "s42", "s43", "s44", "s45", "s46", "s47", "s48", "s49", "s50", "s51", "s52", "s53", "s54", "s55", "s56", "s57", "s58", "s59", "s60", "s61", "s62", "s63", "s64", "s65", "s66", "s67", "s68", "s69", "s70", "s71", "s72", "s73", "s74", "s75", "s76", "s77", "s78", "s79", "s80", "s81", "s82", "s83", "s84", "s85", "s86", "s87", "s88", "s89", "s90", "s91", "s92", "s93", "s94", "s95", "s96", "s97", "s98", "s99");
}

extern "C" void kernel_launch(void* const* d_in, const int* in_sizes, int n_in, void* d_out, int out_size, void* d_ws, size_t ws_size, hipStream_t stream){
  (void)hipFuncSetAttribute((const void*)mk_main, hipFuncAttributeMaxDynamicSharedMemorySize, 163840);
  mk_main<<<256,256,163840,stream>>>((char*)d_ws,(char*)d_out,(const char*)d_in[0],(const char*)d_in[1],(const char*)d_in[2],(const char*)d_in[4],(const char*)d_in[5],(const char*)d_in[6]);
}
```
